```python
import torch

def _softmax(x, axis=-1):
    return torch.softmax(x, dim=axis)

def _gelu(x):
    return torch.nn.functional.gelu(x, approximate='tanh')

def _silu(x):
    return torch.nn.functional.silu(x)

def _sigmoid(x):
    return torch.sigmoid(x)

def _layernorm(x, axis=-1, eps=1e-05):
    m = x.mean(dim=axis, keepdim=True)
    v = x.var(dim=axis, keepdim=True, unbiased=False)
    return (x - m) / torch.sqrt(v + eps)

def _scatter_add(tgt, idx, src, axis=0):
    return tgt.clone().index_add_(axis, idx, src)

def _segment_sum(src, seg, n):
    out = torch.zeros((n,) + src.shape[1:], dtype=src.dtype)
    return out.index_add_(0, seg, src)

def _pad(x, pw):
    flat = tuple((v for pair in reversed(pw) for v in pair))
    return torch.nn.functional.pad(x, flat)
kernel_name = 'adaLN_cond__mha_block__moe_arch_mod__prompt'

def setup_inputs():

    _w = lambda a: (lambda t: t.to(torch.bfloat16) if t.is_floating_point() else t)(torch.as_tensor(a))

    def _A_setup():
        B, S, D, H = (1, 2048, 1024, 32)
        return {'x': _w((lambda *s: torch.randn(*s, dtype=torch.bfloat16) * 0.05)(B, S, D)), 'wqkv': _w((lambda *s: torch.randn(*s, dtype=torch.bfloat16) * 0.05)(D, 3 * D)), 'wo': _w((lambda *s: torch.randn(*s, dtype=torch.bfloat16) * 0.05)(D, D))}
    _A_in = _A_setup()

    def _B_setup():
        B, S, D, F, C = (1, 2048, 1024, 3584, 1)
        return {'x': _w((lambda *s: torch.randn(*s, dtype=torch.bfloat16) * 0.05)(B, S, D)), 'w_r': _w((lambda *s: torch.randn(*s, dtype=torch.bfloat16) * 0.05)(D)), 'w_gate': _w((lambda *s: torch.randn(*s, dtype=torch.bfloat16) * 0.05)(D, F)), 'w_up': _w((lambda *s: torch.randn(*s, dtype=torch.bfloat16) * 0.05)(D, F)), 'w_down': _w((lambda *s: torch.randn(*s, dtype=torch.bfloat16) * 0.05)(F, D))}
    _B_in = _B_setup()
    return {'A_x': _A_in['x'], 'A_wqkv': _A_in['wqkv'], 'A_wo': _A_in['wo'], 'B_x': _B_in['x'], 'B_w_r': _B_in['w_r'], 'B_w_gate': _B_in['w_gate'], 'B_w_up': _B_in['w_up'], 'B_w_down': _B_in['w_down'], 'c': _w((lambda *s: torch.randn(*s, dtype=torch.bfloat16) * 0.1)(1, 1024) * 8.0), 'w_mod': _w((lambda *s: torch.randn(*s, dtype=torch.bfloat16) * 0.1)(1024, 6 * 1024) * 1024 ** (-0.5) * 8.0)}

def reference(A_x, A_wqkv, A_wo, B_x, B_w_r, B_w_gate, B_w_up, B_w_down, c, w_mod):

    def _A_ref(x, wqkv, wo):
        B, S, D = x.shape
        H = 32
        Dh = D // H
        qkv = (lambda x, *d: x.permute(*d))((x @ wqkv).reshape(B, S, 3, H, Dh), 2, 0, 3, 1, 4)
        q, k, v = (qkv[0], qkv[1], qkv[2])
        scores = torch.einsum('bhqd,bhkd->bhqk', q, k) * Dh ** (-0.5)
        p = _softmax(scores, axis=-1)
        o = torch.einsum('bhqk,bhkd->bhqd', p, v)
        return (lambda x, *d: x.permute(*d))(o, 0, 2, 1, 3).reshape(B, S, D) @ wo

    def _B_ref(x, w_r, w_gate, w_up, w_down):
        B, S, D = x.shape
        C = 1
        r = x @ w_r
        idx = torch.argsort(-r, axis=-1)[:, :C]
        idx_e = (lambda x, n, axis: torch.repeat_interleave(x, n, dim=axis))(idx.reshape(B, C, 1), D, axis=2)
        x_sel = (lambda x, i, axis: torch.take_along_dim(x, i, dim=axis))(x, idx_e, axis=1)
        g = _silu(x_sel @ w_gate)
        u = x_sel @ w_up
        y = g * u @ w_down
        r_sel = _sigmoid((lambda x, i, axis: torch.take_along_dim(x, i, dim=axis))(r, idx, axis=1))
        y = y * r_sel.reshape(B, C, 1)
        b_off = (torch.arange(B) * S).reshape(B, 1)
        flat_idx = (idx + b_off).reshape(B * C)
        zero = (x * 0.0).reshape(B * S, D)
        upd = _scatter_add(zero, flat_idx, y.reshape(B * C, D), axis=0)
        return x + upd.reshape(B, S, D)
    x = A_x
    mod = (_silu(c) @ w_mod).reshape(c.shape[0], 6, 1, -1)
    sa, ca, ga, sb, cb, gb = (mod[:, 0].clone(), mod[:, 1].clone(), mod[:, 2].clone(), mod[:, 3].clone(), mod[:, 4].clone(), mod[:, 5].clone())
    h = _layernorm(x, axis=-1) * (1 + ca) + sa
    x = x + ga * _A_ref(h, A_wqkv, A_wo)
    h = _layernorm(x, axis=-1) * (1 + cb) + sb
    x = x + gb * _B_ref(h, B_w_r, B_w_gate, B_w_up, B_w_down)
    return x
```

```cpp
#include <hip/hip_runtime.h>
#include <hip/hip_bf16.h>
#include <stdint.h>

typedef __attribute__((ext_vector_type(8))) short bf16x8;
typedef __attribute__((ext_vector_type(4))) short s16x4;
typedef __attribute__((ext_vector_type(16))) float f32x16;
typedef __attribute__((ext_vector_type(4))) float f32x4;
typedef __attribute__((ext_vector_type(2))) float f32x2;
typedef __attribute__((ext_vector_type(4))) unsigned u32x4;
typedef __attribute__((ext_vector_type(2))) unsigned u32x2;
typedef __bf16 bf16x2_t __attribute__((ext_vector_type(2)));
typedef unsigned short u16;
typedef __attribute__((address_space(3))) const char* lds_cptr;
typedef __attribute__((address_space(3))) char* lds_ptr;

#define DI __device__ __forceinline__
#define MFMA32(a, b, c) __builtin_amdgcn_mfma_f32_32x32x16_bf16((a), (b), (c), 0, 0, 0)
#define MFMA16(a, b, c) __builtin_amdgcn_mfma_f32_16x16x32_bf16((a), (b), (c), 0, 0, 0)
#define SBAR() __builtin_amdgcn_sched_barrier(0)

constexpr int S_ = 2048, D_ = 1024, H_ = 32, F_ = 3584;
constexpr float LN_EPS = 1e-5f;
constexpr float QSCALE = 0.17677669529663687f * 1.4426950408889634f;

constexpr size_t WS_MOD = 0;
constexpr size_t WS_KN2 = 32768;
constexpr size_t WS_SEL = 49152;
constexpr size_t WS_META = WS_SEL + 4096;
constexpr size_t WS_PART = 65536;
constexpr size_t WS_YP = WS_PART + 262144;
constexpr size_t WS_HF = 2u << 20;
constexpr size_t WS_QF = WS_HF + (4u << 20);
constexpr size_t WS_KVF = WS_QF + (4u << 20);
constexpr size_t WS_OF = WS_KVF + (8u << 20);
constexpr size_t WS_X1 = WS_OF + (4u << 20);

DI float bflo(unsigned u) { return __uint_as_float(u << 16); }
DI float bfhi(unsigned u) { return __uint_as_float(u & 0xffff0000u); }
DI unsigned pk2(float lo, float hi) {
    f32x2 v = {lo, hi};
    return __builtin_bit_cast(unsigned, __builtin_convertvector(v, bf16x2_t));
}
DI float rbf(float x) { return bflo(pk2(x, x) & 0xffffu); }
DI float bf2f(u16 v) { return __uint_as_float((unsigned)v << 16); }
DI s16x4 trread(lds_cptr p) {
    typedef short v4i16_t __attribute__((ext_vector_type(4)));
    return __builtin_bit_cast(s16x4, __builtin_amdgcn_ds_read_tr16_b64_v4i16((__attribute__((address_space(3))) v4i16_t*)p));
}
DI bf16x8 ldsfrag(lds_cptr p) { return *(const __attribute__((address_space(3))) bf16x8*)p; }
DI bf16x8 cat4(s16x4 a, s16x4 b) { return __builtin_shufflevector(a, b, 0, 1, 2, 3, 4, 5, 6, 7); }
DI void glds16(const void* sbase, unsigned voff, unsigned lds_base) {
    unsigned sv;
    asm volatile("s_mov_b32 %0, m0\n\ts_mov_b32 m0, %3\n\ts_nop 0\n\tglobal_load_lds_dwordx4 %1, %2\n\ts_mov_b32 m0, %0"
                 : "=&s"(sv) : "v"(voff), "s"(sbase), "s"(lds_base) : "memory");
}
#define WAIT_VM(n) asm volatile("s_waitcnt vmcnt(" #n ")" ::: "memory")
#define WAIT_BAR(n) asm volatile("s_waitcnt vmcnt(" #n ") lgkmcnt(0)\n\ts_barrier" ::: "memory")
#define LDS_BAR() asm volatile("s_waitcnt lgkmcnt(0)\n\ts_barrier" ::: "memory")
DI float xor32(float v) {
    auto r = __builtin_amdgcn_permlane32_swap(__float_as_uint(v), __float_as_uint(v), false, false);
    return (threadIdx.x & 32) ? __uint_as_float(r[0]) : __uint_as_float(r[1]);
}
DI float wave_sum(float v) {
#pragma unroll
    for (int o = 1; o < 64; o <<= 1) v += __shfl_xor(v, o);
    return v;
}
DI float wave_max(float v) {
#pragma unroll
    for (int o = 1; o < 64; o <<= 1) v = fmaxf(v, __shfl_xor(v, o));
    return v;
}

__global__ __launch_bounds__(512) void k_mod(const u16* __restrict__ c, const u16* __restrict__ wmod, float* __restrict__ mod) {
    __shared__ float red[8][16];
    const int b = blockIdx.x, t = threadIdx.x;
    const int chunk = (b >> 5) * 32 + (b & 7) * 4 + ((b >> 3) & 3);
    const int col = chunk * 16 + (t & 1) * 8, rg = t >> 1;
    u32x4 w[4]; float sc[4];
#pragma unroll
    for (int i = 0; i < 4; ++i) w[i] = *(const u32x4*)(wmod + (size_t)(rg + 256 * i) * 6144 + col);
#pragma unroll
    for (int i = 0; i < 4; ++i) { float cv = bf2f(c[rg + 256 * i]); sc[i] = rbf(cv / (1.f + __expf(-cv))); }
    float acc[8];
#pragma unroll
    for (int j = 0; j < 8; ++j) acc[j] = 0.f;
#pragma unroll
    for (int i = 0; i < 4; ++i)
#pragma unroll
        for (int j = 0; j < 4; ++j) { acc[2 * j] += sc[i] * bflo(w[i][j]); acc[2 * j + 1] += sc[i] * bfhi(w[i][j]); }
#pragma unroll
    for (int j = 0; j < 8; ++j)
#pragma unroll
        for (int o = 2; o < 64; o <<= 1) acc[j] += __shfl_xor(acc[j], o);
    if ((t & 63) < 2) {
#pragma unroll
        for (int j = 0; j < 8; ++j) red[t >> 6][(t & 1) * 8 + j] = acc[j];
    }
    __syncthreads();
    if (t < 16) {
        float s = 0.f;
#pragma unroll
        for (int w8 = 0; w8 < 8; ++w8) s += red[w8][t];
        mod[chunk * 16 + t] = rbf(s);
    }
}

__global__ __launch_bounds__(512) void k_ln1(const u16* __restrict__ x, const float* __restrict__ mod, u16* __restrict__ Hf) {
    const int lane = threadIdx.x & 63, row = blockIdx.x * 8 + (threadIdx.x >> 6);
    u32x4 v[2];
    v[0] = *(const u32x4*)(x + (size_t)row * D_ + lane * 8);
    v[1] = *(const u32x4*)(x + (size_t)row * D_ + 512 + lane * 8);
    float f[16];
#pragma unroll
    for (int i = 0; i < 2; ++i)
#pragma unroll
        for (int j = 0; j < 4; ++j) { f[i * 8 + 2 * j] = bflo(v[i][j]); f[i * 8 + 2 * j + 1] = bfhi(v[i][j]); }
    float s = 0.f;
#pragma unroll
    for (int j = 0; j < 16; ++j) s += f[j];
    const float mean = wave_sum(s) * (1.f / D_);
    float q = 0.f;
#pragma unroll
    for (int j = 0; j < 16; ++j) { f[j] -= mean; q += f[j] * f[j]; }
    const float rstd = rsqrtf(wave_sum(q) * (1.f / D_) + LN_EPS);
#pragma unroll
    for (int i = 0; i < 2; ++i) {
        const int k0 = i * 512 + lane * 8;
        u32x4 o;
#pragma unroll
        for (int j = 0; j < 4; ++j) {
            float a = f[i * 8 + 2 * j] * rstd * rbf(1.f + mod[1024 + k0 + 2 * j]) + mod[k0 + 2 * j];
            float bb = f[i * 8 + 2 * j + 1] * rstd * rbf(1.f + mod[1024 + k0 + 2 * j + 1]) + mod[k0 + 2 * j + 1];
            o[j] = pk2(a, bb);
        }
        const int ch = k0 >> 3;
        *(u32x4*)((char*)Hf + (size_t)(row >> 5) * 65536 + (ch >> 1) * 1024 + ((ch & 1) * 32 + (row & 31)) * 16) = o;
    }
}

constexpr int QKV_STAGE = 40960;
__global__ __launch_bounds__(512, 2) void k_qkv(const u16* __restrict__ Hf, const u16* __restrict__ W, u16* __restrict__ Qf,
                                                u16* __restrict__ KVf, float* __restrict__ kn2) {
    __shared__ __attribute__((aligned(16))) char lds[3 * QKV_STAGE];
    const int b = blockIdx.x, tid = threadIdx.x, lane = tid & 63;
    const int w = __builtin_amdgcn_readfirstlane(tid >> 6);
    const int xcd = b & 7, idx = b >> 3;
    const int rp = (xcd & 1) * 8 + (idx & 7), cp = (xcd >> 1) * 4 + (idx >> 3);
    const unsigned lds0 = (unsigned)(uintptr_t)lds;
    const char* abase = (const char*)Hf + (size_t)rp * 4 * 65536;
    const unsigned aoff0 = (unsigned)(((2 * w) >> 2) * 65536 + ((2 * w) & 3) * 1024 + lane * 16);
    const int blk = lane >> 4;
    const unsigned boff = (unsigned)(((w * 8 + (blk >> 1) * 4 + ((lane >> 2) & 3)) * 3072 + cp * 64 + (blk & 1) * 32 + (lane & 3) * 8) * 2);
    const char* bbase = (const char*)W;
#define QKV_STAGE_LOAD(kt, slot)                                                                        \
    do {                                                                                                \
        const unsigned sb_ = lds0 + (slot) * QKV_STAGE;                                                 \
        const char* ab_ = abase + (kt) * 4096;                                                          \
        const char* bb_ = bbase + (size_t)(kt) * 64 * 3072 * 2;                                         \
        glds16(ab_, aoff0, sb_ + (2 * w) * 1024);                                                       \
        glds16(ab_ + 1024, aoff0, sb_ + (2 * w + 1) * 1024);                                            \
        glds16(bb_, boff, sb_ + 16384 + w * 1024);                                                      \
        glds16(bb_ + 2048, boff, sb_ + 16384 + 8192 + w * 1024);                                        \
        glds16(bb_ + 4096, boff, sb_ + 16384 + 16384 + w * 1024);                                       \
    } while (0)
    const int rh = w & 1, hd = (w >> 1) & 1, kg = w >> 2;
    const int G = lane >> 4, hh = lane >> 5;
    const lds_cptr ap = (lds_cptr)lds + (rh * 8 + kg * 2) * 1024 + lane * 16;
    const lds_cptr bp = (lds_cptr)lds + 16384 + (kg * 4 + hh) * 1024 + hd * 256 + ((lane & 15) >> 2) * 64 + (G & 1) * 32 + (lane & 3) * 8;
    f32x16 acc[2][3];
#pragma unroll
    for (int i = 0; i < 2; ++i)
#pragma unroll
        for (int j = 0; j < 3; ++j) acc[i][j] = f32x16{};
    QKV_STAGE_LOAD(0, 0);
    QKV_STAGE_LOAD(1, 1);
    int slot = 0;
    for (int kt = 0; kt < 16; ++kt) {
        if (kt < 15) { WAIT_BAR(5); } else { WAIT_BAR(0); }
        if (kt + 2 < 16) { const int s2 = slot == 0 ? 2 : slot - 1; QKV_STAGE_LOAD(kt + 2, s2); }
        const lds_cptr a_ = ap + slot * QKV_STAGE, b_ = bp + slot * QKV_STAGE;
#pragma unroll
        for (int s = 0; s < 2; ++s) {
            bf16x8 af[2], bfr[3];
#pragma unroll
            for (int i = 0; i < 2; ++i) af[i] = ldsfrag(a_ + i * 4096 + s * 1024);
#pragma unroll
            for (int j = 0; j < 3; ++j) bfr[j] = cat4(trread(b_ + j * 8192 + s * 2048), trread(b_ + j * 8192 + s * 2048 + 512));
#pragma unroll
            for (int i = 0; i < 2; ++i) {
                acc[i][0] = MFMA32(bfr[0], af[i], acc[i][0]);
                acc[i][1] = MFMA32(bfr[1], af[i], acc[i][1]);
                acc[i][2] = MFMA32(af[i], bfr[2], acc[i][2]);
            }
        }
        slot = slot == 2 ? 0 : slot + 1;
    }
    LDS_BAR();
    {
        __attribute__((address_space(3))) f32x4* mine = (__attribute__((address_space(3))) f32x4*)((lds_ptr)lds + w * 12288) + lane;
#pragma unroll
        for (int j = 0; j < 3; ++j)
#pragma unroll
            for (int g = 0; g < 4; ++g) {
                f32x4 v;
#pragma unroll
                for (int e = 0; e < 4; ++e) v[e] = kg ? acc[0][j][4 * g + e] : acc[1][j][4 * g + e];
                mine[(j * 4 + g) * 64] = v;
            }
    }
    LDS_BAR();
    f32x16 r[3];
    {
        const __attribute__((address_space(3))) f32x4* oth = (const __attribute__((address_space(3))) f32x4*)((lds_cptr)lds + (w ^ 4) * 12288) + lane;
#pragma unroll
        for (int j = 0; j < 3; ++j)
#pragma unroll
            for (int g = 0; g < 4; ++g) {
                f32x4 v = oth[(j * 4 + g) * 64];
#pragma unroll
                for (int e = 0; e < 4; ++e) r[j][4 * g + e] = v[e] + (kg ? acc[1][j][4 * g + e] : acc[0][j][4 * g + e]);
            }
    }
    const int tile = rp * 4 + rh * 2 + kg, head = cp * 2 + hd;
    u32x4* qo = (u32x4*)((char*)Qf + ((size_t)(head * 64 + tile) * 2) * 1024) + lane;
    u32x4* kvo = (u32x4*)((char*)KVf + ((size_t)(head * 64 + tile) * 4) * 1024) + lane;
    float n2 = 0.f;
#pragma unroll
    for (int s = 0; s < 2; ++s) {
        u32x4 q, k, v;
#pragma unroll
        for (int j = 0; j < 4; ++j) {
            q[j] = pk2(r[0][8 * s + 2 * j] * QSCALE, r[0][8 * s + 2 * j + 1] * QSCALE);
            k[j] = pk2(r[1][8 * s + 2 * j], r[1][8 * s + 2 * j + 1]);
            v[j] = pk2(r[2][8 * s + 2 * j], r[2][8 * s + 2 * j + 1]);
            const float k0 = bflo(k[j]), k1 = bfhi(k[j]);
            n2 += k0 * k0 + k1 * k1;
        }
        qo[s * 64] = q; kvo[s * 64] = k; kvo[(2 + s) * 64] = v;
    }
    n2 += xor32(n2);
    n2 = wave_max(n2);
    if (lane == 0) kn2[head * 64 + tile] = n2;
}

constexpr int AT_SLOT = 32768;
__global__ __launch_bounds__(512, 2) void k_attn(const u16* __restrict__ Qf, const u16* __restrict__ KVf, const float* __restrict__ kn2,
                                                 u16* __restrict__ Of) {
    __shared__ __attribute__((aligned(16))) char lds[4 * AT_SLOT + 64];
    const int b = blockIdx.x, tid = threadIdx.x, lane = tid & 63;
    const int w = __builtin_amdgcn_readfirstlane(tid >> 6);
    const int head = (b & 7) * 4 + ((b >> 3) & 3), qt = (b >> 5) * 8 + w;
    const unsigned lds0 = (unsigned)(uintptr_t)lds;
    const char* kvbase = (const char*)KVf + (size_t)head * 262144;
    const unsigned voff = (unsigned)(w * 4096 + lane * 16);
#define AT_LOAD(c)                                                                  \
    do {                                                                            \
        const char* g_ = kvbase + (c) * AT_SLOT;                                    \
        const unsigned d_ = lds0 + ((c) & 3) * AT_SLOT + w * 4096;                  \
        glds16(g_, voff, d_); glds16(g_ + 1024, voff, d_ + 1024);                   \
        glds16(g_ + 2048, voff, d_ + 2048); glds16(g_ + 3072, voff, d_ + 3072);     \
    } while (0)
    const bf16x8* qp = (const bf16x8*)((const char*)Qf + (size_t)(head * 64 + qt) * 2048) + lane;
    const bf16x8 q0 = qp[0], q1 = qp[64];
    const float kmx = wave_max(kn2[head * 64 + lane]);
    float qn = 0.f;
#pragma unroll
    for (int j = 0; j < 8; ++j) { float a = bf2f((u16)q0[j]), c = bf2f((u16)q1[j]); qn += a * a + c * c; }
    qn += xor32(qn);
    float mref = sqrtf(qn * kmx) * 1.001f;
    bf16x8 ones;
    { const short o = (((lane >> 4) & 1) == (((lane & 15) >> 2) & 1)) ? (short)0x3f80 : (short)0;
#pragma unroll
      for (int j = 0; j < 8; ++j) ones[j] = o; }
    const lds_cptr fp = (lds_cptr)lds + lane * 16;
    volatile __attribute__((address_space(3))) int* flag = (volatile __attribute__((address_space(3))) int*)((lds_ptr)lds + 4 * AT_SLOT);
    if (tid == 0) *flag = 0;
    f32x16 o; f32x4 ls;
    for (int attempt = 0; attempt < 2; ++attempt) {
        f32x16 negm;
#pragma unroll
        for (int i = 0; i < 16; ++i) negm[i] = -mref;
        o = f32x16{}; ls = f32x4{};
        AT_LOAD(0); AT_LOAD(1); AT_LOAD(2);
        f32x16 sA, sB;
#define AT_S(dst, p_) do { const bf16x8 k0_ = ldsfrag(p_), k1_ = ldsfrag((p_) + 1024); dst = MFMA32(k0_, q0, negm); dst = MFMA32(k1_, q1, dst); } while (0)
#define AT_PV(src, p_) do {                                                                             \
            const bf16x8 v0_ = ldsfrag((p_) + 2048), v1_ = ldsfrag((p_) + 3072);                        \
            u32x4 p0_, p1_;                                                                             \
            _Pragma("unroll") for (int j = 0; j < 4; ++j) {                                             \
                p0_[j] = pk2(__builtin_amdgcn_exp2f(src[2 * j]), __builtin_amdgcn_exp2f(src[2 * j + 1]));         \
                p1_[j] = pk2(__builtin_amdgcn_exp2f(src[8 + 2 * j]), __builtin_amdgcn_exp2f(src[9 + 2 * j])); }   \
            const bf16x8 pa_ = __builtin_bit_cast(bf16x8, p0_), pb_ = __builtin_bit_cast(bf16x8, p1_);  \
            o = MFMA32(v0_, pa_, o); ls = MFMA16(ones, pa_, ls);                                        \
            o = MFMA32(v1_, pb_, o); ls = MFMA16(ones, pb_, ls);                                        \
        } while (0)
        WAIT_BAR(8);
        AT_S(sA, fp);
        for (int c = 0; c < 8; ++c) {
            if (c < 6) { WAIT_BAR(4); } else { WAIT_BAR(0); }
            if (c + 3 < 8) AT_LOAD(c + 3);
            const lds_cptr p = fp + (c & 3) * AT_SLOT;
            const lds_cptr pn = fp + ((c + 1) & 3) * AT_SLOT;
#pragma unroll
            for (int t = 0; t < 8; t += 2) {
                AT_S(sB, p + (t + 1) * 4096);
                AT_PV(sA, p + t * 4096);
                if (t + 2 < 8) AT_S(sA, p + (t + 2) * 4096); else AT_S(sA, pn);
                AT_PV(sB, p + (t + 1) * 4096);
            }
        }
        const bool bad = !(ls[0] >= 8.6736174e-19f);
        if (attempt == 0 && __any(bad)) { if (lane == 0) *flag = 1; }
        LDS_BAR();
        const int redo = __builtin_amdgcn_readfirstlane(*flag);
        if (attempt == 1 || !redo) break;
        float mx = -INFINITY;
        const bf16x8* kp = (const bf16x8*)kvbase + lane;
        for (int t = 0; t < 64; ++t) {
            const bf16x8 k0 = kp[t * 256], k1 = kp[t * 256 + 64];
            f32x16 s = MFMA32(k0, q0, f32x16{}); s = MFMA32(k1, q1, s);
#pragma unroll
            for (int i = 0; i < 16; ++i) mx = fmaxf(mx, s[i]);
        }
        mx = fmaxf(mx, xor32(mx));
        mref = mx;
        LDS_BAR();
    }
    const float rl = 1.f / ls[0];
    u32x4* op = (u32x4*)((char*)Of + ((size_t)(qt * 32 + head) * 2) * 1024) + lane;
#pragma unroll
    for (int s = 0; s < 2; ++s) {
        u32x4 v;
#pragma unroll
        for (int j = 0; j < 4; ++j) v[j] = pk2(o[8 * s + 2 * j] * rl, o[8 * s + 2 * j + 1] * rl);
        op[s * 64] = v;
    }
}

__global__ __launch_bounds__(512, 2) void k_oproj(const u16* __restrict__ Of, const u16* __restrict__ Wo, const u16* __restrict__ x,
                                                  const float* __restrict__ mod, const u16* __restrict__ wr, u16* __restrict__ x1,
                                                  f32x4* __restrict__ part) {
    __shared__ __attribute__((aligned(16))) char lds[8 * 16384 + 4096];
    const int b = blockIdx.x, tid = threadIdx.x, lane = tid & 63;
    const int w = __builtin_amdgcn_readfirstlane(tid >> 6);
    const int xcd = b & 7, idx = b >> 3;
    const int rp = xcd * 4 + (idx & 3), cp = idx >> 2;
    const int nh = w & 1, kg = w >> 1;
    const unsigned lds0 = (unsigned)(uintptr_t)lds + w * 16384;
    const int blk = lane >> 4, hh = lane >> 5;
    const char* bbase = (const char*)Wo + ((size_t)kg * 256 * 1024 + cp * 128 + nh * 64) * 2;
    const unsigned boff = (unsigned)((((blk >> 1) * 4 + ((lane >> 2) & 3)) * 1024 + (blk & 1) * 32 + (lane & 3) * 8) * 2);
    const char* abase = (const char*)Of + (size_t)(rp * 2) * 65536 + kg * 16384;
    const unsigned aoff = lane * 16;
    u32x4 af[16][2];
#define OP_LDA(j) do {                                                                                                      \
        asm volatile("global_load_dwordx4 %0, %1, %2 offset:0" : "=v"(af[j][0]) : "v"(aoff), "s"(abase + (j) * 1024) : "memory");      \
        asm volatile("global_load_dwordx4 %0, %1, %2 offset:0" : "=v"(af[j][1]) : "v"(aoff), "s"(abase + 65536 + (j) * 1024) : "memory"); \
    } while (0)
#define OP_LDB(j) do {                                                                      \
        glds16(bbase + (size_t)(2 * (j)) * 16384, boff, lds0 + ((2 * (j)) & 15) * 1024);            \
        glds16(bbase + (size_t)(2 * (j) + 1) * 16384, boff, lds0 + ((2 * (j) + 1) & 15) * 1024);    \
    } while (0)
#pragma unroll
    for (int j = 0; j < 8; ++j) { OP_LDB(j); OP_LDA(j); }
#pragma unroll
    for (int j = 8; j < 16; ++j) { OP_LDA(j); }
    const lds_cptr bp = (lds_cptr)lds + w * 16384 + hh * 512 + ((lane & 15) >> 2) * 64 + (blk & 1) * 32 + (lane & 3) * 8;
    f32x16 acc[2][2];
#pragma unroll
    for (int i = 0; i < 2; ++i)
#pragma unroll
        for (int j = 0; j < 2; ++j) acc[i][j] = f32x16{};
#define OP_STEP(j, N) do {                                                                                      \
        asm volatile("s_waitcnt vmcnt(" #N ")" : "+v"(af[j][0]), "+v"(af[j][1]) :: "memory");                   \
        const lds_cptr p_ = bp + ((2 * (j)) & 15) * 1024;                                                       \
        const bf16x8 b0_ = cat4(trread(p_), trread(p_ + 1024)), b1_ = cat4(trread(p_ + 256), trread(p_ + 1024 + 256)); \
        const bf16x8 a0_ = __builtin_bit_cast(bf16x8, af[j][0]), a1_ = __builtin_bit_cast(bf16x8, af[j][1]);    \
        acc[0][0] = MFMA32(b0_, a0_, acc[0][0]); acc[0][1] = MFMA32(b1_, a0_, acc[0][1]);                       \
        acc[1][0] = MFMA32(b0_, a1_, acc[1][0]); acc[1][1] = MFMA32(b1_, a1_, acc[1][1]);                       \
        if ((j) < 8) { asm volatile("s_waitcnt lgkmcnt(0)" ::: "memory"); OP_LDB((j) + 8); }                    \
    } while (0)
    OP_STEP(0, 44); OP_STEP(1, 42); OP_STEP(2, 40); OP_STEP(3, 38); OP_STEP(4, 36); OP_STEP(5, 34); OP_STEP(6, 32); OP_STEP(7, 30);
    OP_STEP(8, 14); OP_STEP(9, 12); OP_STEP(10, 10); OP_STEP(11, 8); OP_STEP(12, 6); OP_STEP(13, 4); OP_STEP(14, 2); OP_STEP(15, 0);
    asm volatile("s_waitcnt lgkmcnt(0)" ::: "memory");
    {
        __attribute__((address_space(3))) f32x4* mine = (__attribute__((address_space(3))) f32x4*)((lds_ptr)lds + w * 16384) + lane;
#pragma unroll
        for (int t = 0; t < 4; ++t)
#pragma unroll
            for (int g = 0; g < 4; ++g) {
                f32x4 v;
#pragma unroll
                for (int e = 0; e < 4; ++e) v[e] = acc[t >> 1][t & 1][4 * g + e];
                mine[(t * 4 + g) * 64] = v;
            }
    }
    LDS_BAR();
    float r[16];
#pragma unroll
    for (int i = 0; i < 16; ++i) r[i] = 0.f;
#pragma unroll
    for (int k2 = 0; k2 < 4; ++k2) {
        const __attribute__((address_space(3))) f32x4* src = (const __attribute__((address_space(3))) f32x4*)((lds_cptr)lds + (k2 * 2 + nh) * 16384) + kg * 256 + lane;
#pragma unroll
        for (int g = 0; g < 4; ++g) {
            f32x4 v = src[g * 64];
#pragma unroll
            for (int e = 0; e < 4; ++e) r[4 * g + e] += v[e];
        }
    }
    const int row = rp * 64 + (kg >> 1) * 32 + (lane & 31);
    const int n0 = cp * 128 + nh * 64 + (kg & 1) * 32 + 4 * hh;
    float xv[16]; float s = 0.f;
#pragma unroll
    for (int g = 0; g < 4; ++g) {
        const u32x2 xi = *(const u32x2*)(x + (size_t)row * D_ + n0 + 8 * g);
        const f32x4 ga = *(const f32x4*)(mod + 2048 + n0 + 8 * g);
        float xin[4] = {bflo(xi[0]), bfhi(xi[0]), bflo(xi[1]), bfhi(xi[1])};
        u32x2 o;
        float t0 = xin[0] + rbf(ga[0] * rbf(r[4 * g])), t1 = xin[1] + rbf(ga[1] * rbf(r[4 * g + 1]));
        float t2 = xin[2] + rbf(ga[2] * rbf(r[4 * g + 2])), t3 = xin[3] + rbf(ga[3] * rbf(r[4 * g + 3]));
        o[0] = pk2(t0, t1); o[1] = pk2(t2, t3);
        *(u32x2*)(x1 + (size_t)row * D_ + n0 + 8 * g) = o;
        xv[4 * g] = bflo(o[0]); xv[4 * g + 1] = bfhi(o[0]); xv[4 * g + 2] = bflo(o[1]); xv[4 * g + 3] = bfhi(o[1]);
        s += xv[4 * g] + xv[4 * g + 1] + xv[4 * g + 2] + xv[4 * g + 3];
    }
    s += xor32(s);
    const float mt = s * (1.f / 32.f);
    float m2 = 0.f, dt = 0.f, gs = 0.f;
#pragma unroll
    for (int g = 0; g < 4; ++g) {
        const f32x4 cb = *(const f32x4*)(mod + 4096 + n0 + 8 * g);
        const u32x2 wi = *(const u32x2*)(wr + n0 + 8 * g);
        const float wv[4] = {bflo(wi[0]), bfhi(wi[0]), bflo(wi[1]), bfhi(wi[1])};
#pragma unroll
        for (int e = 0; e < 4; ++e) {
            const float gw = rbf(1.f + cb[e]) * wv[e], d = xv[4 * g + e] - mt;
            m2 += d * d; dt += d * gw; gs += gw;
        }
    }
    m2 += xor32(m2); dt += xor32(dt); gs += xor32(gs);
    __attribute__((address_space(3))) f32x4* st = (__attribute__((address_space(3))) f32x4*)((lds_ptr)lds + 8 * 16384);
    if (lane < 32) st[(nh * 2 + (kg & 1)) * 64 + (kg >> 1) * 32 + lane] = f32x4{mt, m2, dt, gs};
    LDS_BAR();
    if (tid < 64) {
        f32x4 p[4];
#pragma unroll
        for (int i = 0; i < 4; ++i) p[i] = st[i * 64 + tid];
        const float mean = 0.25f * (p[0][0] + p[1][0] + p[2][0] + p[3][0]);
        float M2 = 0.f, DT = 0.f, GS = 0.f;
#pragma unroll
        for (int i = 0; i < 4; ++i) { const float d = p[i][0] - mean; M2 += p[i][1] + 32.f * d * d; DT += p[i][2] + d * p[i][3]; GS += p[i][3]; }
        part[(size_t)(rp * 64 + tid) * 8 + cp] = f32x4{mean, M2, DT, GS};
    }
}

constexpr int NFB = 224;
__global__ __launch_bounds__(1024) void k_ffn(const f32x4* __restrict__ part, const u16* __restrict__ x1, const float* __restrict__ mod,
                                              const u16* __restrict__ wr, const u16* __restrict__ wg, const u16* __restrict__ wu,
                                              const u16* __restrict__ wd, float* __restrict__ yp, float* __restrict__ h2sel,
                                              float* __restrict__ meta, u16* __restrict__ out) {
    __shared__ __attribute__((aligned(16))) float sh_h[1024];
    __shared__ __attribute__((aligned(16))) float sh_red[4][1024];
    __shared__ float sh_best[8][4];
    __shared__ float sh_row[8][2];
    __shared__ float sh_gu[8][32];
    __shared__ float sh_act[16];
    __shared__ float sh_sbw[8];
    const int b = blockIdx.x, tid = threadIdx.x, lane = tid & 63;
    const int w = __builtin_amdgcn_readfirstlane(tid >> 6);
    const bool ffn = b < NFB;
    const int chunk = (b >> 5) * 32 + (b & 7) * 4 + ((b >> 3) & 3);
    const int t = tid & 511;
    u32x4 g[4], u[4], d[4];
    if (w >= 8) {
        if (ffn) {
            const int col = chunk * 16 + (t & 1) * 8, rg = t >> 1;
#pragma unroll
            for (int i = 0; i < 4; ++i) {
                g[i] = *(const u32x4*)(wg + (size_t)(rg + 256 * i) * F_ + col);
                u[i] = *(const u32x4*)(wu + (size_t)(rg + 256 * i) * F_ + col);
            }
#pragma unroll
            for (int i = 0; i < 4; ++i) d[i] = *(const u32x4*)(wd + (size_t)(chunk * 16 + (t >> 7) * 4 + i) * D_ + (t & 127) * 8);
        }
        __syncthreads();
        __syncthreads();
        if (ffn) {
            const int rg = t >> 1;
            float ag[8], au[8];
#pragma unroll
            for (int j = 0; j < 8; ++j) { ag[j] = 0.f; au[j] = 0.f; }
#pragma unroll
            for (int i = 0; i < 4; ++i) {
                const float hv = sh_h[rg + 256 * i];
#pragma unroll
                for (int j = 0; j < 4; ++j) {
                    ag[2 * j] += hv * bflo(g[i][j]); ag[2 * j + 1] += hv * bfhi(g[i][j]);
                    au[2 * j] += hv * bflo(u[i][j]); au[2 * j + 1] += hv * bfhi(u[i][j]);
                }
            }
#pragma unroll
            for (int j = 0; j < 8; ++j)
#pragma unroll
                for (int o = 2; o < 64; o <<= 1) { ag[j] += __shfl_xor(ag[j], o); au[j] += __shfl_xor(au[j], o); }
            if (lane < 2) {
#pragma unroll
                for (int j = 0; j < 8; ++j) { sh_gu[w - 8][lane * 8 + j] = ag[j]; sh_gu[w - 8][16 + lane * 8 + j] = au[j]; }
            }
        }
        __syncthreads();
        if (ffn && t < 16) {
            float sg = 0.f, su = 0.f;
#pragma unroll
            for (int i = 0; i < 8; ++i) { sg += sh_gu[i][t]; su += sh_gu[i][16 + t]; }
            sg = rbf(sg); su = rbf(su);
            const float si = rbf(sg / (1.f + __expf(-sg)));
            sh_act[t] = rbf(si * su);
        }
        __syncthreads();
        if (ffn) {
            float y[8];
#pragma unroll
            for (int j = 0; j < 8; ++j) y[j] = 0.f;
#pragma unroll
            for (int i = 0; i < 4; ++i) {
                const float a = sh_act[(t >> 7) * 4 + i];
#pragma unroll
                for (int j = 0; j < 4; ++j) { y[2 * j] += a * bflo(d[i][j]); y[2 * j + 1] += a * bfhi(d[i][j]); }
            }
            *(f32x4*)&sh_red[t >> 7][(t & 127) * 8] = f32x4{y[0], y[1], y[2], y[3]};
            *(f32x4*)&sh_red[t >> 7][(t & 127) * 8 + 4] = f32x4{y[4], y[5], y[6], y[7]};
        }
        __syncthreads();
        if (ffn && t < 256) {
            f32x4 s = *(const f32x4*)&sh_red[0][t * 4];
#pragma unroll
            for (int i = 1; i < 4; ++i) s += *(const f32x4*)&sh_red[i][t * 4];
            *(f32x4*)(yp + (size_t)chunk * 1024 + t * 4) = s;
        }
    } else {
        const int myrow = b * 8 + w;
        const u32x4 xa = *(const u32x4*)(x1 + (size_t)myrow * D_ + lane * 8), xb = *(const u32x4*)(x1 + (size_t)myrow * D_ + 512 + lane * 8);
        float sbw = bf2f(wr[2 * t]) * mod[3072 + 2 * t] + bf2f(wr[2 * t + 1]) * mod[3072 + 2 * t + 1];
        sbw = wave_sum(sbw);
        if (lane == 0) sh_sbw[w] = sbw;
        float best = -INFINITY, bmean = 0.f, brstd = 0.f; int bidx = 0x7fffffff;
#pragma unroll 2
        for (int rr = 0; rr < 4; ++rr) {
            const int row = rr * 512 + t;
            f32x4 p[8];
#pragma unroll
            for (int i = 0; i < 8; ++i) p[i] = part[(size_t)row * 8 + i];
            float mean = 0.f;
#pragma unroll
            for (int i = 0; i < 8; ++i) mean += p[i][0];
            mean *= 0.125f;
            float M2 = 0.f, DT = 0.f;
#pragma unroll
            for (int i = 0; i < 8; ++i) { const float dd = p[i][0] - mean; M2 += p[i][1] + 128.f * dd * dd; DT += p[i][2] + dd * p[i][3]; }
            const float rstd = rsqrtf(M2 * (1.f / D_) + LN_EPS);
            const float rv = DT * rstd;
            if ((row >> 3) == b) { sh_row[row & 7][0] = mean; sh_row[row & 7][1] = rstd; }
            if (rv > best || bidx == 0x7fffffff) { best = rv; bidx = row; bmean = mean; brstd = rstd; }
        }
#pragma unroll
        for (int o = 1; o < 64; o <<= 1) {
            const float ob = __shfl_xor(best, o), om = __shfl_xor(bmean, o), orr = __shfl_xor(brstd, o); const int oi = __shfl_xor(bidx, o);
            if (ob > best || (ob == best && oi < bidx)) { best = ob; bidx = oi; bmean = om; brstd = orr; }
        }
        if (lane == 0) { sh_best[w][0] = best; sh_best[w][1] = __int_as_float(bidx); sh_best[w][2] = bmean; sh_best[w][3] = brstd; }
        __syncthreads();
        best = sh_best[0][0]; bidx = __float_as_int(sh_best[0][1]); bmean = sh_best[0][2]; brstd = sh_best[0][3];
        float sbwt = sh_sbw[0];
#pragma unroll
        for (int i = 1; i < 8; ++i) {
            const float ob = sh_best[i][0]; const int oi = __float_as_int(sh_best[i][1]);
            sbwt += sh_sbw[i];
            if (ob > best || (ob == best && oi < bidx)) { best = ob; bidx = oi; bmean = sh_best[i][2]; brstd = sh_best[i][3]; }
        }
        {
            const unsigned xs = *(const unsigned*)(x1 + (size_t)bidx * D_ + 2 * t);
            const float h0 = rbf((bflo(xs) - bmean) * brstd * rbf(1.f + mod[4096 + 2 * t]) + mod[3072 + 2 * t]);
            const float h1 = rbf((bfhi(xs) - bmean) * brstd * rbf(1.f + mod[4096 + 2 * t + 1]) + mod[3072 + 2 * t + 1]);
            sh_h[2 * t] = h0; sh_h[2 * t + 1] = h1;
            if (b == 0) {
                h2sel[2 * t] = h0; h2sel[2 * t + 1] = h1;
                if (t == 0) { const float rv = rbf(best + sbwt); meta[0] = __int_as_float(bidx); meta[1] = rbf(1.f / (1.f + __expf(-rv))); }
            }
        }
        __syncthreads();
        {
            const float mean = sh_row[w][0], rstd = sh_row[w][1];
#pragma unroll
            for (int i = 0; i < 2; ++i) {
                const u32x4 xv = i ? xb : xa; const int k0 = i * 512 + lane * 8;
                u32x4 o;
#pragma unroll
                for (int j = 0; j < 4; ++j) {
                    const int k = k0 + 2 * j;
                    const float x0 = bflo(xv[j]), x1v = bfhi(xv[j]);
                    const float h0 = rbf((x0 - mean) * rstd * rbf(1.f + mod[4096 + k]) + mod[3072 + k]);
                    const float h1 = rbf((x1v - mean) * rstd * rbf(1.f + mod[4096 + k + 1]) + mod[3072 + k + 1]);
                    o[j] = pk2(x0 + rbf(mod[5120 + k] * h0), x1v + rbf(mod[5120 + k + 1] * h1));
                }
                *(u32x4*)(out + (size_t)myrow * D_ + k0) = o;
            }
        }
        __syncthreads();
        __syncthreads();
        __syncthreads();
    }
}

__global__ __launch_bounds__(256) void k_fin(const float* __restrict__ yp, const float* __restrict__ h2sel, const float* __restrict__ meta,
                                             const u16* __restrict__ x1, const float* __restrict__ mod, u16* __restrict__ out) {
    __shared__ f32x4 red[64][4];
    const int t = threadIdx.x, c4 = t & 3, rg = t >> 2;
    const int col = blockIdx.x * 16 + c4 * 4;
    f32x4 s = f32x4{};
#pragma unroll
    for (int i = 0; i < 4; ++i) { const int r = rg + 64 * i; if (r < NFB) s += *(const f32x4*)(yp + (size_t)r * 1024 + col); }
    red[rg][c4] = s;
    __syncthreads();
    if (t < 16) {
        float y = 0.f;
        for (int i = 0; i < 64; ++i) y += red[i][t >> 2][t & 3];
        const int idx = __float_as_int(meta[0]); const float sig = meta[1];
        const int n = blockIdx.x * 16 + t;
        const float yy = rbf(rbf(y) * sig);
        const float hb = rbf(h2sel[n] + yy);
        const float o = bf2f(x1[(size_t)idx * D_ + n]) + rbf(mod[5120 + n] * hb);
        out[(size_t)idx * D_ + n] = (u16)(pk2(o, o) & 0xffffu);
    }
}

extern "C" void kernel_launch(void* const* d_in, const int* in_sizes, int n_in, void* d_out, int out_size, void* d_ws, size_t ws_size,
                              hipStream_t stream) {
    const u16* x = (const u16*)d_in[0];
    const u16* wqkv = (const u16*)d_in[1];
    const u16* wo = (const u16*)d_in[2];
    const u16* wr = (const u16*)d_in[4];
    const u16* wg = (const u16*)d_in[5];
    const u16* wu = (const u16*)d_in[6];
    const u16* wd = (const u16*)d_in[7];
    const u16* c = (const u16*)d_in[8];
    const u16* wmod = (const u16*)d_in[9];
    char* ws = (char*)d_ws;
    float* mod = (float*)(ws + WS_MOD);
    float* kn2 = (float*)(ws + WS_KN2);
    float* h2sel = (float*)(ws + WS_SEL);
    float* meta = (float*)(ws + WS_META);
    f32x4* part = (f32x4*)(ws + WS_PART);
    float* yp = (float*)(ws + WS_YP);
    u16* Hf = (u16*)(ws + WS_HF);
    u16* Qf = (u16*)(ws + WS_QF);
    u16* KVf = (u16*)(ws + WS_KVF);
    u16* Of = (u16*)(ws + WS_OF);
    u16* x1 = (u16*)(ws + WS_X1);
    u16* out = (u16*)d_out;
    k_mod<<<384, 512, 0, stream>>>(c, wmod, mod);
    k_ln1<<<256, 512, 0, stream>>>(x, mod, Hf);
    k_qkv<<<256, 512, 0, stream>>>(Hf, wqkv, Qf, KVf, kn2);
    k_attn<<<256, 512, 0, stream>>>(Qf, KVf, kn2, Of);
    k_oproj<<<256, 512, 0, stream>>>(Of, wo, x, mod, wr, x1, part);
    k_ffn<<<256, 1024, 0, stream>>>(part, x1, mod, wr, wg, wu, wd, yp, h2sel, meta, out);
    k_fin<<<64, 256, 0, stream>>>(yp, h2sel, meta, x1, mod, out);
}
```

```cpp
#include <hip/hip_runtime.h>
#include <hip/hip_bf16.h>
#include <stdint.h>

typedef __attribute__((ext_vector_type(8))) short bf16x8;
typedef __attribute__((ext_vector_type(4))) short s16x4;
typedef __attribute__((ext_vector_type(16))) float f32x16;
typedef __attribute__((ext_vector_type(4))) float f32x4;
typedef __attribute__((ext_vector_type(2))) float f32x2;
typedef __attribute__((ext_vector_type(4))) unsigned u32x4;
typedef __attribute__((ext_vector_type(2))) unsigned u32x2;
typedef __bf16 bf16x2_t __attribute__((ext_vector_type(2)));
typedef unsigned short u16;
typedef __attribute__((address_space(3))) const char* lds_cptr;
typedef __attribute__((address_space(3))) char* lds_ptr;

#define DI __device__ __forceinline__
#define MFMA32(a, b, c) __builtin_amdgcn_mfma_f32_32x32x16_bf16((a), (b), (c), 0, 0, 0)
#define MFMA16(a, b, c) __builtin_amdgcn_mfma_f32_16x16x32_bf16((a), (b), (c), 0, 0, 0)
#define SBAR() __builtin_amdgcn_sched_barrier(0)

constexpr int S_ = 2048, D_ = 1024, H_ = 32, F_ = 3584;
constexpr float LN_EPS = 1e-5f;
constexpr float QSCALE = 0.17677669529663687f * 1.4426950408889634f;

constexpr size_t WS_MOD = 0;
constexpr size_t WS_KN2 = 32768;
constexpr size_t WS_SEL = 49152;
constexpr size_t WS_META = WS_SEL + 8192;
constexpr size_t WS_GP = WS_META + 256;
constexpr size_t WS_MODB = WS_GP + 256;
constexpr size_t WS_PART = 131072;
constexpr size_t WS_YP = WS_PART + 262144;
constexpr size_t WS_GRAN = WS_YP + 917504;
constexpr size_t WS_STAT = 98304;
constexpr size_t WS_QF = 6u << 20;
constexpr size_t WS_KVF = WS_QF + (4u << 20);
constexpr size_t WS_OF = WS_KVF + (8u << 20);
constexpr size_t WS_X1 = WS_OF + (4u << 20);

#define DIAG_ARG
#define DIAG_BEGIN
#define DIAG_T(i)
#define DIAG_END(kid)
#define DIAG_PASS
DI float bflo(unsigned u) { return __uint_as_float(u << 16); }
DI float bfhi(unsigned u) { return __uint_as_float(u & 0xffff0000u); }
DI unsigned pk2(float lo, float hi) {
    f32x2 v = {lo, hi};
    return __builtin_bit_cast(unsigned, __builtin_convertvector(v, bf16x2_t));
}
DI float rbf(float x) { return bflo(pk2(x, x) & 0xffffu); }
DI float bf2f(u16 v) { return __uint_as_float((unsigned)v << 16); }
DI s16x4 trread(lds_cptr p) {
    typedef short v4i16_t __attribute__((ext_vector_type(4)));
    return __builtin_bit_cast(s16x4, __builtin_amdgcn_ds_read_tr16_b64_v4i16((__attribute__((address_space(3))) v4i16_t*)p));
}
DI bf16x8 ldsfrag(lds_cptr p) { return *(const __attribute__((address_space(3))) bf16x8*)p; }
DI bf16x8 cat4(s16x4 a, s16x4 b) { return __builtin_shufflevector(a, b, 0, 1, 2, 3, 4, 5, 6, 7); }
DI void glds16(const void* sbase, unsigned voff, unsigned lds_base) {
    unsigned sv;
    asm volatile("s_mov_b32 %0, m0\n\ts_mov_b32 m0, %3\n\ts_nop 0\n\tglobal_load_lds_dwordx4 %1, %2\n\ts_mov_b32 m0, %0"
                 : "=&s"(sv) : "v"(voff), "s"(sbase), "s"(lds_base) : "memory");
}
#define GLD4(dst, voff, sbase) asm volatile("global_load_dwordx4 %0, %1, %2" : "=v"(dst) : "v"(voff), "s"(sbase) : "memory")
#define GLD2(dst, voff, sbase) asm volatile("global_load_dwordx2 %0, %1, %2" : "=v"(dst) : "v"(voff), "s"(sbase) : "memory")
#define GLD1(dst, voff, sbase) asm volatile("global_load_dword %0, %1, %2" : "=v"(dst) : "v"(voff), "s"(sbase) : "memory")
#define GLDH(dst, voff, sbase) asm volatile("global_load_ushort %0, %1, %2" : "=v"(dst) : "v"(voff), "s"(sbase) : "memory")
#define WAIT_VM(n) asm volatile("s_waitcnt vmcnt(" #n ")" ::: "memory")
#define WAIT_BAR(n) asm volatile("s_waitcnt vmcnt(" #n ") lgkmcnt(0)\n\ts_barrier" ::: "memory")
#define LDS_BAR() asm volatile("s_waitcnt lgkmcnt(0)\n\ts_barrier" ::: "memory")
DI float xor32(float v) {
    auto r = __builtin_amdgcn_permlane32_swap(__float_as_uint(v), __float_as_uint(v), false, false);
    return (threadIdx.x & 32) ? __uint_as_float(r[0]) : __uint_as_float(r[1]);
}
DI float wave_sum(float v) {
#pragma unroll
    for (int o = 1; o < 64; o <<= 1) v += __shfl_xor(v, o);
    return v;
}
DI float wave_max(float v) {
#pragma unroll
    for (int o = 1; o < 64; o <<= 1) v = fmaxf(v, __shfl_xor(v, o));
    return v;
}

template <int CTRL> DI float dpp(float x) { return __builtin_bit_cast(float, __builtin_amdgcn_mov_dpp(__builtin_bit_cast(int, x), CTRL, 0xf, 0xf, true)); }
constexpr int XOR1 = 0xB1, XOR2 = 0x4E, ROR4 = 0x124, ROR8 = 0x128;
DI float sw32sum(float a, float b) {
    auto r = __builtin_amdgcn_permlane32_swap(__float_as_uint(a), __float_as_uint(b), false, false);
    return __uint_as_float(r[0]) + __uint_as_float(r[1]);
}
DI float sw16sum(float a, float b) {
    auto r = __builtin_amdgcn_permlane16_swap(__float_as_uint(a), __float_as_uint(b), false, false);
    return __uint_as_float(r[0]) + __uint_as_float(r[1]);
}
DI float row_sum16(float v) { v += dpp<XOR1>(v); v += dpp<XOR2>(v); v += dpp<ROR4>(v); v += dpp<ROR8>(v); return v; }
DI float row_max16(float v) { v = fmaxf(v, dpp<XOR1>(v)); v = fmaxf(v, dpp<XOR2>(v)); v = fmaxf(v, dpp<ROR4>(v)); v = fmaxf(v, dpp<ROR8>(v)); return v; }
DI float wsum(float v) { v = row_sum16(v); v = sw16sum(v, v); return sw32sum(v, v); }
DI float wmax(float m) {
    m = row_max16(m);
    { auto s_ = __builtin_amdgcn_permlane16_swap(__float_as_uint(m), __float_as_uint(m), false, false); m = fmaxf(__uint_as_float(s_[0]), __uint_as_float(s_[1])); }
    { auto s_ = __builtin_amdgcn_permlane32_swap(__float_as_uint(m), __float_as_uint(m), false, false); m = fmaxf(__uint_as_float(s_[0]), __uint_as_float(s_[1])); }
    return m;
}
DI float sfma(float a, float b, float c) { float r; asm("v_fma_f32 %0, %1, %2, %3" : "=v"(r) : "v"(a), "v"(b), "v"(c)); return r; }
#define LAS __attribute__((address_space(3)))
constexpr int M_SA = 0, M_G1 = 1024, M_GA = 2048, M_SB = 3072, M_G2 = 4096, M_GB = 5120, M_GW = 6144;

__global__ __launch_bounds__(512) void k_mod(const u16* __restrict__ c, const u16* __restrict__ wmod, const u16* __restrict__ wr,
                                             const u16* __restrict__ x, float* __restrict__ mod, u16* __restrict__ modb,
                                             f32x2* __restrict__ stat, unsigned long long* __restrict__ gran DIAG_ARG) {
    DIAG_BEGIN
    __shared__ __attribute__((aligned(16))) float red[8][16];
    const int t = threadIdx.x, lane = t & 63;
    if (blockIdx.x < 128) {
        const int row = blockIdx.x * 16 + (t >> 6) * 2;
        u32x4 v[4];
#pragma unroll
        for (int i = 0; i < 4; ++i) v[i] = *(const u32x4*)(x + (size_t)row * D_ + i * 512 + lane * 8);
        if (blockIdx.x < 32) gran[blockIdx.x * 512 + t] = 0ull;
#pragma unroll
        for (int r = 0; r < 2; ++r) {
            float f[16], s = 0.f;
#pragma unroll
            for (int j = 0; j < 8; ++j) { f[2 * j] = bflo(v[2 * r + (j >> 2)][j & 3]); f[2 * j + 1] = bfhi(v[2 * r + (j >> 2)][j & 3]); s += f[2 * j] + f[2 * j + 1]; }
            const float mean = wsum(s) * (1.f / D_);
            float q = 0.f;
#pragma unroll
            for (int j = 0; j < 16; ++j) { const float d = f[j] - mean; q += d * d; }
            const float rstd = rsqrtf(wsum(q) * (1.f / D_) + LN_EPS);
            if (lane == 0) stat[row + r] = f32x2{rstd, -mean * rstd};
        }
        DIAG_END(1);
        return;
    }
    const int b = blockIdx.x - 128;
    const int chunk = (b >> 5) * 32 + (b & 7) * 4 + ((b >> 3) & 3);
    const int col = chunk * 16 + (t & 1) * 8, rg = t >> 1;
    u32x4 w[4]; unsigned cv[4], wrb;
#pragma unroll
    for (int i = 0; i < 4; ++i) GLDH(cv[i], (unsigned)(rg * 2), c + 256 * i);
#pragma unroll
    for (int i = 0; i < 4; ++i) GLD4(w[i], (unsigned)((rg * 6144 + col) * 2), wmod + (size_t)256 * i * 6144);
    GLDH(wrb, (unsigned)(((chunk * 16 + (t & 15)) & 1023) * 2), wr);
    asm volatile("s_waitcnt vmcnt(5)" : "+v"(cv[0]), "+v"(cv[1]), "+v"(cv[2]), "+v"(cv[3]) :: "memory");
    float sc[4];
#pragma unroll
    for (int i = 0; i < 4; ++i) { const float cf = bflo(cv[i]); sc[i] = rbf(cf / (1.f + __expf(-cf))); }
    asm volatile("s_waitcnt vmcnt(0)" : "+v"(w[0]), "+v"(w[1]), "+v"(w[2]), "+v"(w[3]), "+v"(wrb) :: "memory");
    float acc[8];
#pragma unroll
    for (int j = 0; j < 8; ++j) acc[j] = 0.f;
#pragma unroll
    for (int i = 0; i < 4; ++i)
#pragma unroll
        for (int j = 0; j < 4; ++j) { acc[2 * j] += sc[i] * bflo(w[i][j]); acc[2 * j + 1] += sc[i] * bfhi(w[i][j]); }
#pragma unroll
    for (int j = 0; j < 4; ++j) acc[j] = sw32sum(acc[j], acc[4 + j]);
    f32x2 z;
#pragma unroll
    for (int j = 0; j < 2; ++j) {
        float q = sw16sum(acc[j], acc[2 + j]);
        q += dpp<XOR2>(q); q += dpp<ROR4>(q); q += dpp<ROR8>(q);
        z[j] = q;
    }
    if ((lane & 14) == 0) *(f32x2*)&red[t >> 6][(lane & 1) * 8 + (lane >> 5) * 4 + ((lane >> 4) & 1) * 2] = z;
    LDS_BAR();
    if (t < 16) {
        float s = ((red[0][t] + red[1][t]) + (red[2][t] + red[3][t])) + ((red[4][t] + red[5][t]) + (red[6][t] + red[7][t]));
        const int n = chunk * 16 + t, sec = n >> 10;
        s = rbf(s);
        if (sec == 1 || sec == 4) s = rbf(1.f + s);
        mod[n] = s; modb[n] = (u16)(__float_as_uint(s) >> 16);
        if (sec == 4) mod[M_GW + (n & 1023)] = s * bflo(wrb);
    }
    DIAG_END(0);
}

#define QABL 0
constexpr int QA_SLOT = 8576, QB_SLOT = 12288, QB_OFF = 5 * QA_SLOT, QG_OFF = QB_OFF + 9 * QB_SLOT, QK_LDS = QG_OFF + 8192;
__global__ __launch_bounds__(768, 3) void k_qkv(const u16* __restrict__ x, const f32x2* __restrict__ stat, const float* __restrict__ mod,
                                                const u16* __restrict__ W, u16* __restrict__ Qf, u16* __restrict__ KVf,
                                                float* __restrict__ kn2 DIAG_ARG) {
    DIAG_BEGIN
    __shared__ __attribute__((aligned(16))) char lds[QK_LDS];
    const int b = blockIdx.x, tid = threadIdx.x, lane = tid & 63;
    const int w = __builtin_amdgcn_readfirstlane(tid >> 6);
    const int xcd = b & 7, idx = b >> 3;
    const int rp = (xcd & 1) * 8 + (idx & 7), cp = (xcd >> 1) * 4 + (idx >> 3);
    const unsigned lds0 = (unsigned)(uintptr_t)lds;
    if (w >= 8) {
        const int lw = w - 8, blk = lane >> 4;
        const unsigned boff = (unsigned)(((lw * 8 + (blk >> 1) * 4 + ((lane >> 2) & 3)) * 3072 + cp * 64 + (blk & 1) * 32 + (lane & 3) * 8) * 2);
        glds16((const char*)mod + lw * 2048, lane * 16, lds0 + QG_OFF + lw * 2048);
        glds16((const char*)mod + lw * 2048 + 1024, lane * 16, lds0 + QG_OFF + lw * 2048 + 1024);
        const char* src = (const char*)W;
        unsigned dst = lds0 + QB_OFF + lw * 1024;
        const unsigned dend = lds0 + QB_OFF + 9 * QB_SLOT;
#define QB_UNIT() do { if (!(QABL & 4)) { glds16(src, boff, dst); glds16(src + 2048, boff, dst + 4096); glds16(src + 4096, boff, dst + 8192); }  \
                       src += 32 * 3072 * 2; dst += QB_SLOT; if (dst >= dend) dst -= 9 * QB_SLOT; } while (0)
#pragma unroll 1
        for (int u = 0; u < 6; ++u) QB_UNIT();
        WAIT_BAR(18);
#pragma unroll 1
        for (int j = 0; j < 13; ++j) { WAIT_BAR(12); QB_UNIT(); QB_UNIT(); }
        WAIT_BAR(12); WAIT_BAR(6); WAIT_BAR(0);
    } else if (w >= 4) {
        const int p = tid - 256, c = p & 7, r = p >> 3;
        const unsigned xoff = (unsigned)(((rp * 128 + r) * D_ + c * 8) * 2);
        u32x4 xa[4], xb[4]; f32x2 st[4];
#define QX_LOAD(dst, j) do { _Pragma("unroll") for (int i = 0; i < 4; ++i) GLD4(dst[i], xoff, x + (size_t)i * 32 * D_ + (j) * 64); } while (0)
#pragma unroll
        for (int i = 0; i < 4; ++i) GLD2(st[i], (unsigned)((rp * 128 + r) * 8), stat + 32 * i);
        QX_LOAD(xa, 0); QX_LOAD(xb, 1);
        const lds_cptr gp = (lds_cptr)lds + QG_OFF + c * 32;
        const unsigned wbase = (c & 3) * 528 + r * 16 + (c >> 2) * 64;
        int slot = c >> 2;
        asm volatile("s_waitcnt vmcnt(8)" : "+v"(st[0]), "+v"(st[1]), "+v"(st[2]), "+v"(st[3]) :: "memory");
        LDS_BAR();
#define QA_STEP(xs, j) do {                                                                                       \
            const lds_cptr g_ = gp + (j) * 256;                                                                   \
            const f32x4 s0 = *(const LAS f32x4*)g_, s1 = *(const LAS f32x4*)(g_ + 16);                            \
            const f32x4 g0 = *(const LAS f32x4*)(g_ + 4096), g1 = *(const LAS f32x4*)(g_ + 4096 + 16);            \
            asm volatile("s_waitcnt vmcnt(4)" : "+v"(xs[0]), "+v"(xs[1]), "+v"(xs[2]), "+v"(xs[3]) :: "memory");  \
            const lds_ptr d_ = (lds_ptr)lds + slot * QA_SLOT + wbase;                                             \
            _Pragma("unroll") for (int i = 0; i < 4; ++i) {                                                       \
                u32x4 o;                                                                                          \
                _Pragma("unroll") for (int e = 0; e < 4; ++e) {                                                   \
                    const float gl = e < 2 ? g0[2 * e] : g1[2 * e - 4], gh = e < 2 ? g0[2 * e + 1] : g1[2 * e - 3];   \
                    const float sl = e < 2 ? s0[2 * e] : s1[2 * e - 4], sh = e < 2 ? s0[2 * e + 1] : s1[2 * e - 3];   \
                    o[e] = pk2(sfma(sfma(bflo(xs[i][e]), st[i][0], st[i][1]), gl, sl), sfma(sfma(bfhi(xs[i][e]), st[i][0], st[i][1]), gh, sh)); \
                }                                                                                                 \
                *(LAS u32x4*)(d_ + i * 2112) = (QABL & 1) ? xs[i] : o;                                                                 \
            }                                                                                                     \
            QX_LOAD(xs, ((j) + 2) & 15);                                   \
            slot += 2; if (slot >= 5) slot -= 5;                                                                  \
            LDS_BAR();                                                                                            \
        } while (0)
#pragma unroll 1
        for (int j = 0; j < 16; j += 2) { QA_STEP(xa, j); QA_STEP(xb, j + 1); }
        asm volatile("s_waitcnt vmcnt(0)" : "+v"(xa[0]), "+v"(xa[1]), "+v"(xa[2]), "+v"(xa[3]), "+v"(xb[0]), "+v"(xb[1]), "+v"(xb[2]), "+v"(xb[3]) :: "memory");
    } else {
        const int rh = w & 1, hd = w >> 1, G = lane >> 4, hh = lane >> 5;
        const lds_cptr ap = (lds_cptr)lds + rh * 4224 + hh * 528 + (lane & 31) * 16;
        const lds_cptr bp = (lds_cptr)lds + QB_OFF + hh * 1024 + hd * 256 + ((lane & 15) >> 2) * 64 + (G & 1) * 32 + (lane & 3) * 8;
        f32x16 acc[2][3];
#pragma unroll
        for (int i = 0; i < 2; ++i)
#pragma unroll
            for (int j = 0; j < 3; ++j) acc[i][j] = f32x16{};
        bf16x8 a0[2], b0[3], a1[2], b1[3];
#define QC_READ(af, bf, a_, b_, s) do {                                                                           \
            af[0] = ldsfrag((a_) + (s) * 1056); af[1] = ldsfrag((a_) + (s) * 1056 + 2112);                        \
            _Pragma("unroll") for (int j = 0; j < 3; ++j) bf[j] = cat4(trread((b_) + j * 4096 + (s) * 2048), trread((b_) + j * 4096 + (s) * 2048 + 512)); \
        } while (0)
#define QC_RB(bf, j, b_, s) do { if (!(QABL & 2)) bf[j] = cat4(trread((b_) + (j) * 4096 + (s) * 2048), trread((b_) + (j) * 4096 + (s) * 2048 + 512)); } while (0)
#define QC_STEP(af, bf, an, bn, a_, b_, s) do {                                                                   \
            acc[0][0] = MFMA32(bf[0], af[0], acc[0][0]); SBAR();                                                  \
            if (!(QABL & 2)) { an[0] = ldsfrag((a_) + (s) * 1056); an[1] = ldsfrag((a_) + (s) * 1056 + 2112); } SBAR();   \
            acc[0][1] = MFMA32(bf[1], af[0], acc[0][1]); SBAR();                                                  \
            QC_RB(bn, 0, b_, s); SBAR();                                                                          \
            acc[0][2] = MFMA32(af[0], bf[2], acc[0][2]); SBAR();                                                  \
            QC_RB(bn, 1, b_, s); SBAR();                                                                          \
            acc[1][0] = MFMA32(bf[0], af[1], acc[1][0]); SBAR();                                                  \
            QC_RB(bn, 2, b_, s); SBAR();                                                                          \
            acc[1][1] = MFMA32(bf[1], af[1], acc[1][1]);                                                          \
            acc[1][2] = MFMA32(af[1], bf[2], acc[1][2]); SBAR();                                                  \
        } while (0)
        asm volatile("s_barrier" ::: "memory");
        asm volatile("s_barrier" ::: "memory");
        DIAG_T(0);
        int as = 0, bs = 0;
        lds_cptr a_ = ap, b_ = bp;
        QC_READ(a0, b0, a_, b_, 0);
        if (QABL & 2) QC_READ(a1, b1, a_, b_, 1);
        SBAR(); __builtin_amdgcn_s_waitcnt(0xC07F); SBAR();
#define QC_NEXT(odd) do { as += QA_SLOT; if (as >= 5 * QA_SLOT) as = 0; bs += QB_SLOT; if (bs >= 9 * QB_SLOT) bs = 0;   \
                          a_ = ap + as + (odd) * 64; b_ = bp + bs; } while (0)
#pragma unroll 1
        for (int j = 0; j < 16; ++j) {
            QC_STEP(a0, b0, a1, b1, a_, b_, 1);
            QC_NEXT(1);
            QC_STEP(a1, b1, a0, b0, a_, b_, 0);
            if (j < 15) asm volatile("s_barrier" ::: "memory");
            QC_STEP(a0, b0, a1, b1, a_, b_, 1);
            QC_NEXT(0);
            QC_STEP(a1, b1, a0, b0, a_, b_, 0);
        }
        DIAG_T(1);
        const int head = cp * 2 + hd;
#pragma unroll
        for (int i = 0; i < 2; ++i) {
            const int tile = rp * 4 + rh * 2 + i;
            u32x4* qo = (u32x4*)((char*)Qf + ((size_t)(head * 64 + tile) * 2) * 1024) + lane;
            u32x4* kvo = (u32x4*)((char*)KVf + ((size_t)(head * 64 + tile) * 4) * 1024) + lane;
            float n2a = 0.f, n2b = 0.f;
#pragma unroll
            for (int s = 0; s < 2; ++s) {
                u32x4 q, k, v;
#pragma unroll
                for (int j = 0; j < 4; ++j) {
                    const float k0 = acc[i][1][8 * s + 2 * j], k1 = acc[i][1][8 * s + 2 * j + 1];
                    q[j] = pk2(acc[i][0][8 * s + 2 * j] * QSCALE, acc[i][0][8 * s + 2 * j + 1] * QSCALE);
                    k[j] = pk2(k0, k1);
                    v[j] = pk2(acc[i][2][8 * s + 2 * j], acc[i][2][8 * s + 2 * j + 1]);
                    n2a += k0 * k0; n2b += k1 * k1;
                }
                qo[s * 64] = q; kvo[s * 64] = k; kvo[(2 + s) * 64] = v;
            }
            float n2 = n2a + n2b;
            n2 = wmax(sw32sum(n2, n2));
            if (lane == 0) kn2[head * 64 + tile] = n2;
        }
    }
    DIAG_END(2);
}

constexpr int AT_SLOT = 32768;
__global__ __launch_bounds__(512, 2) void k_attn(const u16* __restrict__ Qf, const u16* __restrict__ KVf, const float* __restrict__ kn2,
                                                 u16* __restrict__ Of DIAG_ARG) {
    DIAG_BEGIN
    __shared__ __attribute__((aligned(16))) char lds[4 * AT_SLOT + 64];
    const int b = blockIdx.x, tid = threadIdx.x, lane = tid & 63;
    const int w = __builtin_amdgcn_readfirstlane(tid >> 6);
    const int head = (b & 7) * 4 + ((b >> 3) & 3), qt = (b >> 5) * 8 + w;
    const unsigned lds0 = (unsigned)(uintptr_t)lds;
    const char* kvbase = (const char*)KVf + (size_t)head * 262144;
    const unsigned voff = (unsigned)(w * 4096 + lane * 16);
#define AT_LOAD(c)                                                                  \
    do {                                                                            \
        const char* g_ = kvbase + (c) * AT_SLOT;                                    \
        const unsigned d_ = lds0 + ((c) & 3) * AT_SLOT + w * 4096;                  \
        glds16(g_, voff, d_); glds16(g_ + 1024, voff, d_ + 1024);                   \
        glds16(g_ + 2048, voff, d_ + 2048); glds16(g_ + 3072, voff, d_ + 3072);     \
    } while (0)
    const bf16x8* qp = (const bf16x8*)((const char*)Qf + (size_t)(head * 64 + qt) * 2048) + lane;
    const bf16x8 q0 = qp[0], q1 = qp[64];
    const float kmx = wave_max(kn2[head * 64 + lane]);
    float qn = 0.f;
#pragma unroll
    for (int j = 0; j < 8; ++j) { float a = bf2f((u16)q0[j]), c = bf2f((u16)q1[j]); qn += a * a + c * c; }
    qn += xor32(qn);
    float mref = sqrtf(qn * kmx) * 1.01f;
    bf16x8 ones;
    { const short o = (((lane >> 4) & 1) == (((lane & 15) >> 2) & 1)) ? (short)0x3f80 : (short)0;
#pragma unroll
      for (int j = 0; j < 8; ++j) ones[j] = o; }
    const lds_cptr fp = (lds_cptr)lds + lane * 16;
    volatile __attribute__((address_space(3))) int* flag = (volatile __attribute__((address_space(3))) int*)((lds_ptr)lds + 4 * AT_SLOT);
    if (tid == 0) *flag = 0;
    f32x16 o; f32x4 ls;
    for (int attempt = 0; attempt < 2; ++attempt) {
        f32x16 negm;
#pragma unroll
        for (int i = 0; i < 16; ++i) negm[i] = -mref;
        o = f32x16{}; ls = f32x4{};
        AT_LOAD(0); AT_LOAD(1); AT_LOAD(2);
        f32x16 sA, sB;
#define AT_S(dst, p_) do { const bf16x8 k0_ = ldsfrag(p_), k1_ = ldsfrag((p_) + 1024); dst = MFMA32(k0_, q0, negm); dst = MFMA32(k1_, q1, dst); } while (0)
#define AT_PV(src, p_) do {                                                                             \
            const bf16x8 v0_ = ldsfrag((p_) + 2048), v1_ = ldsfrag((p_) + 3072);                        \
            u32x4 p0_, p1_;                                                                             \
            _Pragma("unroll") for (int j = 0; j < 4; ++j) {                                             \
                p0_[j] = pk2(__builtin_amdgcn_exp2f(src[2 * j]), __builtin_amdgcn_exp2f(src[2 * j + 1]));         \
                p1_[j] = pk2(__builtin_amdgcn_exp2f(src[8 + 2 * j]), __builtin_amdgcn_exp2f(src[9 + 2 * j])); }   \
            const bf16x8 pa_ = __builtin_bit_cast(bf16x8, p0_), pb_ = __builtin_bit_cast(bf16x8, p1_);  \
            o = MFMA32(v0_, pa_, o); ls = MFMA16(ones, pa_, ls);                                        \
            o = MFMA32(v1_, pb_, o); ls = MFMA16(ones, pb_, ls);                                        \
        } while (0)
        WAIT_BAR(8);
        AT_S(sA, fp);
        for (int c = 0; c < 8; ++c) {
            if (c < 6) { WAIT_BAR(4); } else { WAIT_BAR(0); }
            if (c + 3 < 8) AT_LOAD(c + 3);
            const lds_cptr p = fp + (c & 3) * AT_SLOT;
            const lds_cptr pn = fp + ((c + 1) & 3) * AT_SLOT;
#pragma unroll
            for (int t = 0; t < 8; t += 2) {
                AT_S(sB, p + (t + 1) * 4096);
                AT_PV(sA, p + t * 4096);
                if (t + 2 < 8) AT_S(sA, p + (t + 2) * 4096); else AT_S(sA, pn);
                AT_PV(sB, p + (t + 1) * 4096);
            }
        }
        const bool bad = !(ls[0] >= 8.6736174e-19f);
        if (attempt == 0 && __any(bad)) { if (lane == 0) *flag = 1; }
        LDS_BAR();
        const int redo = __builtin_amdgcn_readfirstlane(*flag);
        if (attempt == 1 || !redo) break;
        float mx = -INFINITY;
        const bf16x8* kp = (const bf16x8*)kvbase + lane;
        for (int t = 0; t < 64; ++t) {
            const bf16x8 k0 = kp[t * 256], k1 = kp[t * 256 + 64];
            f32x16 s = MFMA32(k0, q0, f32x16{}); s = MFMA32(k1, q1, s);
#pragma unroll
            for (int i = 0; i < 16; ++i) mx = fmaxf(mx, s[i]);
        }
        mx = fmaxf(mx, xor32(mx));
        mref = mx;
        LDS_BAR();
    }
    const float rl = 1.f / ls[0];
    u32x4* op = (u32x4*)((char*)Of + ((size_t)(qt * 32 + head) * 2) * 1024) + lane;
#pragma unroll
    for (int s = 0; s < 2; ++s) {
        u32x4 v;
#pragma unroll
        for (int j = 0; j < 4; ++j) v[j] = pk2(o[8 * s + 2 * j] * rl, o[8 * s + 2 * j + 1] * rl);
        op[s * 64] = v;
    }
    DIAG_END(3);
}

__global__ __launch_bounds__(512, 2) void k_oproj(const u16* __restrict__ Of, const u16* __restrict__ Wo, const u16* __restrict__ x,
                                                  const float* __restrict__ mod, u16* __restrict__ x1,
                                                  float* __restrict__ part, float* __restrict__ Gp DIAG_ARG) {
    DIAG_BEGIN
    __shared__ __attribute__((aligned(16))) char lds[8 * 16384 + 4096];
    const int b = blockIdx.x, tid = threadIdx.x, lane = tid & 63;
    const int w = __builtin_amdgcn_readfirstlane(tid >> 6);
    const int xcd = b & 7, idx = b >> 3;
    const int rp = (xcd >> 1) * 8 + (idx & 7), cp = (xcd & 1) * 4 + (idx >> 3);
    const int nh = w & 1, kg = w >> 1;
    const unsigned lds0 = (unsigned)(uintptr_t)lds + w * 16384;
    const int blk = lane >> 4, hh = lane >> 5;
    const char* bbase = (const char*)Wo + ((size_t)kg * 256 * 1024 + cp * 128 + nh * 64) * 2;
    const unsigned boff = (unsigned)((((blk >> 1) * 4 + ((lane >> 2) & 3)) * 1024 + (blk & 1) * 32 + (lane & 3) * 8) * 2);
    const char* abase = (const char*)Of + (size_t)(rp * 2) * 65536 + kg * 16384;
    const unsigned aoff = lane * 16;
    const int row = rp * 64 + (kg >> 1) * 32 + (lane & 31);
    const int nw = cp * 128 + nh * 64 + (kg & 1) * 32;
    const int n0 = nw + 4 * hh;
    u32x4 af[16][2];
#define OP_LDA(j) do {                                                                                                      \
        asm volatile("global_load_dwordx4 %0, %1, %2 offset:0" : "=v"(af[j][0]) : "v"(aoff), "s"(abase + (j) * 1024) : "memory");      \
        asm volatile("global_load_dwordx4 %0, %1, %2 offset:0" : "=v"(af[j][1]) : "v"(aoff), "s"(abase + 65536 + (j) * 1024) : "memory"); \
    } while (0)
#define OP_LDB(j) do {                                                                      \
        glds16(bbase + (size_t)(2 * (j)) * 16384, boff, lds0 + ((2 * (j)) & 15) * 1024);            \
        glds16(bbase + (size_t)(2 * (j) + 1) * 16384, boff, lds0 + ((2 * (j) + 1) & 15) * 1024);    \
    } while (0)
#pragma unroll
    for (int j = 0; j < 8; ++j) { OP_LDB(j); OP_LDA(j); }
#pragma unroll
    for (int j = 8; j < 16; ++j) { OP_LDA(j); }
    u32x2 xi[4];
    {
        const unsigned xoff = (unsigned)((row * D_ + n0) * 2);
        asm volatile("global_load_dwordx2 %0, %1, %2 offset:0" : "=v"(xi[0]) : "v"(xoff), "s"(x) : "memory");
        asm volatile("global_load_dwordx2 %0, %1, %2 offset:16" : "=v"(xi[1]) : "v"(xoff), "s"(x) : "memory");
        asm volatile("global_load_dwordx2 %0, %1, %2 offset:32" : "=v"(xi[2]) : "v"(xoff), "s"(x) : "memory");
        asm volatile("global_load_dwordx2 %0, %1, %2 offset:48" : "=v"(xi[3]) : "v"(xoff), "s"(x) : "memory");
    }
    const lds_cptr bp = (lds_cptr)lds + w * 16384 + hh * 512 + ((lane & 15) >> 2) * 64 + (blk & 1) * 32 + (lane & 3) * 8;
    f32x16 acc[2][2];
#pragma unroll
    for (int i = 0; i < 2; ++i)
#pragma unroll
        for (int j = 0; j < 2; ++j) acc[i][j] = f32x16{};
#define OP_STEP(j, N) do {                                                                                      \
        asm volatile("s_waitcnt vmcnt(" #N ")" : "+v"(af[j][0]), "+v"(af[j][1]) :: "memory");                   \
        const lds_cptr p_ = bp + ((2 * (j)) & 15) * 1024;                                                       \
        const bf16x8 b0_ = cat4(trread(p_), trread(p_ + 1024)), b1_ = cat4(trread(p_ + 256), trread(p_ + 1024 + 256)); \
        const bf16x8 a0_ = __builtin_bit_cast(bf16x8, af[j][0]), a1_ = __builtin_bit_cast(bf16x8, af[j][1]);    \
        acc[0][0] = MFMA32(b0_, a0_, acc[0][0]); acc[0][1] = MFMA32(b1_, a0_, acc[0][1]);                       \
        acc[1][0] = MFMA32(b0_, a1_, acc[1][0]); acc[1][1] = MFMA32(b1_, a1_, acc[1][1]);                       \
        if ((j) < 8) { asm volatile("s_waitcnt lgkmcnt(0)" ::: "memory"); OP_LDB((j) + 8); }                    \
    } while (0)
    DIAG_T(0);
    OP_STEP(0, 48); DIAG_T(1); OP_STEP(1, 46); OP_STEP(2, 44); OP_STEP(3, 42); OP_STEP(4, 40); OP_STEP(5, 38); OP_STEP(6, 36); OP_STEP(7, 34);
    DIAG_T(2);
    f32x4 ga[4], gw[4];
    {
        const unsigned voff = (unsigned)(hh * 16);
        const float* mg = mod + M_GA + nw; const float* mw = mod + M_GW + nw;
        asm volatile("global_load_dwordx4 %0, %1, %2 offset:0" : "=v"(ga[0]) : "v"(voff), "s"(mg) : "memory");
        asm volatile("global_load_dwordx4 %0, %1, %2 offset:32" : "=v"(ga[1]) : "v"(voff), "s"(mg) : "memory");
        asm volatile("global_load_dwordx4 %0, %1, %2 offset:64" : "=v"(ga[2]) : "v"(voff), "s"(mg) : "memory");
        asm volatile("global_load_dwordx4 %0, %1, %2 offset:96" : "=v"(ga[3]) : "v"(voff), "s"(mg) : "memory");
        asm volatile("global_load_dwordx4 %0, %1, %2 offset:0" : "=v"(gw[0]) : "v"(voff), "s"(mw) : "memory");
        asm volatile("global_load_dwordx4 %0, %1, %2 offset:32" : "=v"(gw[1]) : "v"(voff), "s"(mw) : "memory");
        asm volatile("global_load_dwordx4 %0, %1, %2 offset:64" : "=v"(gw[2]) : "v"(voff), "s"(mw) : "memory");
        asm volatile("global_load_dwordx4 %0, %1, %2 offset:96" : "=v"(gw[3]) : "v"(voff), "s"(mw) : "memory");
    }
    OP_STEP(8, 22); OP_STEP(9, 20); OP_STEP(10, 18); OP_STEP(11, 16); OP_STEP(12, 14); OP_STEP(13, 12); OP_STEP(14, 10); OP_STEP(15, 8);
    DIAG_T(3);
    asm volatile("s_waitcnt lgkmcnt(0)" ::: "memory");
    {
        __attribute__((address_space(3))) f32x4* mine = (__attribute__((address_space(3))) f32x4*)((lds_ptr)lds + w * 16384) + lane;
#pragma unroll
        for (int t = 0; t < 4; ++t)
            if (t != kg) {
#pragma unroll
                for (int g = 0; g < 4; ++g) {
                    f32x4 v;
#pragma unroll
                    for (int e = 0; e < 4; ++e) v[e] = acc[t >> 1][t & 1][4 * g + e];
                    mine[(t * 4 + g) * 64] = v;
                }
            }
    }
    f32x16 own = kg == 0 ? acc[0][0] : kg == 1 ? acc[0][1] : kg == 2 ? acc[1][0] : acc[1][1];
    LDS_BAR();
    DIAG_T(4);
    float r[16];
#pragma unroll
    for (int i = 0; i < 16; ++i) r[i] = 0.f;
#pragma unroll
    for (int k2 = 0; k2 < 4; ++k2) {
        if (k2 == kg) {
#pragma unroll
            for (int i = 0; i < 16; ++i) r[i] += own[i];
        } else {
            const __attribute__((address_space(3))) f32x4* src = (const __attribute__((address_space(3))) f32x4*)((lds_cptr)lds + (k2 * 2 + nh) * 16384) + kg * 256 + lane;
#pragma unroll
            for (int g = 0; g < 4; ++g) {
                f32x4 v = src[g * 64];
#pragma unroll
                for (int e = 0; e < 4; ++e) r[4 * g + e] += v[e];
            }
        }
    }
    asm volatile("s_waitcnt vmcnt(0)" : "+v"(xi[0]), "+v"(xi[1]), "+v"(xi[2]), "+v"(xi[3]), "+v"(ga[0]), "+v"(ga[1]), "+v"(ga[2]), "+v"(ga[3]),
                 "+v"(gw[0]), "+v"(gw[1]), "+v"(gw[2]), "+v"(gw[3]) :: "memory");
    DIAG_T(5);
    float xv[16]; float s = 0.f;
#pragma unroll
    for (int g = 0; g < 4; ++g) {
        float xin[4] = {bflo(xi[g][0]), bfhi(xi[g][0]), bflo(xi[g][1]), bfhi(xi[g][1])};
        u32x2 o;
        float t0 = xin[0] + rbf(ga[g][0] * rbf(r[4 * g])), t1 = xin[1] + rbf(ga[g][1] * rbf(r[4 * g + 1]));
        float t2 = xin[2] + rbf(ga[g][2] * rbf(r[4 * g + 2])), t3 = xin[3] + rbf(ga[g][3] * rbf(r[4 * g + 3]));
        o[0] = pk2(t0, t1); o[1] = pk2(t2, t3);
        *(u32x2*)(x1 + (size_t)row * D_ + n0 + 8 * g) = o;
        xv[4 * g] = bflo(o[0]); xv[4 * g + 1] = bfhi(o[0]); xv[4 * g + 2] = bflo(o[1]); xv[4 * g + 3] = bfhi(o[1]);
        s += xv[4 * g] + xv[4 * g + 1] + xv[4 * g + 2] + xv[4 * g + 3];
    }
    s = sw32sum(s, s);
    const float mt = s * (1.f / 32.f);
    float m2 = 0.f, dt = 0.f, gs = 0.f;
#pragma unroll
    for (int g = 0; g < 4; ++g)
#pragma unroll
        for (int e = 0; e < 4; ++e) {
            const float d = xv[4 * g + e] - mt;
            m2 += d * d; dt += d * gw[g][e]; gs += gw[g][e];
        }
    m2 = sw32sum(m2, m2); dt = sw32sum(dt, dt); gs = sw32sum(gs, gs);
    __attribute__((address_space(3))) f32x4* st = (__attribute__((address_space(3))) f32x4*)((lds_ptr)lds + 8 * 16384);
    if (lane < 32) st[(nh * 2 + (kg & 1)) * 64 + (kg >> 1) * 32 + lane] = f32x4{mt, m2, dt, gs};
    LDS_BAR();
    DIAG_T(6);
    if (tid < 64) {
        f32x4 p[4];
#pragma unroll
        for (int i = 0; i < 4; ++i) p[i] = st[i * 64 + tid];
        const float mean = 0.25f * (p[0][0] + p[1][0] + p[2][0] + p[3][0]);
        float M2 = 0.f, DT = 0.f, GS = 0.f;
#pragma unroll
        for (int i = 0; i < 4; ++i) { const float d = p[i][0] - mean; M2 += p[i][1] + 32.f * d * d; DT += p[i][2] + d * p[i][3]; GS += p[i][3]; }
        float* pp = part + (size_t)cp * 3 * S_ + rp * 64 + tid;
        pp[0] = mean; pp[S_] = M2; pp[2 * S_] = DT;
        if (rp == 0 && tid == 0) Gp[cp] = GS;
    }
    DIAG_END(4);
}

constexpr int NFB = 224;
typedef __attribute__((address_space(1))) unsigned long long gu64;
#define RLX_AGENT __ATOMIC_RELAXED, __HIP_MEMORY_SCOPE_AGENT
constexpr int XOR7 = 0x141;
#define POLL_SLEEP 4
DI float oct_sum(float v) { v += dpp<XOR1>(v); v += dpp<XOR2>(v); v += dpp<XOR7>(v); return v; }
DI void row_stats(float pm, float p2, float pd, float G, float& mean, float& rstd, float& rv) {
    mean = oct_sum(pm) * 0.125f;
    const float dd = pm - mean;
    const float M2 = oct_sum(p2 + 128.f * dd * dd), DT = oct_sum(pd + dd * G);
    rstd = rsqrtf(M2 * (1.f / D_) + LN_EPS);
    const float r = DT * rstd;
    rv = r == r ? r : -INFINITY;
}
__global__ __launch_bounds__(512, 2) void k_ffn(const float* __restrict__ part, const float* __restrict__ Gp, const u16* __restrict__ x1,
                                                const u16* __restrict__ modb, const u16* __restrict__ wr, const u16* __restrict__ wg,
                                                const u16* __restrict__ wu, const u16* __restrict__ wd, float* __restrict__ yp,
                                                float* __restrict__ sel, float* __restrict__ meta, unsigned long long* __restrict__ gran,
                                                u16* __restrict__ out DIAG_ARG) {
    DIAG_BEGIN
    __shared__ __attribute__((aligned(16))) float sh_h[1024];
    __shared__ __attribute__((aligned(16))) float sh_red[4][1024];
    __shared__ __attribute__((aligned(16))) float sh_gu[8][32];
    __shared__ __attribute__((aligned(16))) f32x4 sh_best[8];
    __shared__ float sh_r[8];
    __shared__ int sh_to[8];
    const int b = blockIdx.x, t = threadIdx.x, lane = t & 63, hh = lane >> 5;
    const int w = __builtin_amdgcn_readfirstlane(t >> 6);
    const bool router = b < 32;
    const int fb = b - 32;
    const int chunk = router ? 0 : (fb >> 5) * 32 + (fb & 7) * 4 + ((fb >> 3) & 3);
    gu64* gr = (gu64*)gran + (b & 31) * 512 + t;
    const int myrow = b * 8 + (lane >> 3), c0 = w * 128 + (lane & 7) * 8;
    float pm0, p20, pd0, G0;
    u32x4 xo[2], eg[2], es[2], eb[2];
#define FFN_ELEM_LOAD() do {                                                                                                     \
        const unsigned o_ = (unsigned)(((lane & 7) * 3 * S_ + myrow) * 4);                                                       \
        GLD1(pm0, o_, part); GLD1(p20, o_, part + S_); GLD1(pd0, o_, part + 2 * S_); GLD1(G0, (unsigned)((lane & 7) * 4), Gp);   \
        const unsigned xoff_ = (unsigned)((myrow * D_ + c0) * 2), voff_ = (unsigned)(c0 * 2);                                    \
        _Pragma("unroll") for (int i = 0; i < 2; ++i) { GLD4(xo[i], xoff_, x1 + 64 * i); GLD4(eg[i], voff_, modb + M_G2 + 64 * i); \
            GLD4(es[i], voff_, modb + M_SB + 64 * i); GLD4(eb[i], voff_, modb + M_GB + 64 * i); }                                \
    } while (0)
#define FFN_ELEM(N) do {                                                                                                         \
        asm volatile("s_waitcnt vmcnt(" #N ")" : "+v"(pm0), "+v"(p20), "+v"(pd0), "+v"(G0), "+v"(xo[0]), "+v"(xo[1]), "+v"(eg[0]), "+v"(eg[1]), \
                     "+v"(es[0]), "+v"(es[1]), "+v"(eb[0]), "+v"(eb[1]) :: "memory");                                            \
        float mean_, rstd_, rv_;                                                                                                 \
        row_stats(pm0, p20, pd0, G0, mean_, rstd_, rv_);                                                                         \
        _Pragma("unroll") for (int i = 0; i < 2; ++i) {                                                                          \
            u32x4 o;                                                                                                             \
            _Pragma("unroll") for (int j = 0; j < 4; ++j) {                                                                      \
                const float x0 = bflo(xo[i][j]), x1v = bfhi(xo[i][j]);                                                           \
                const float h0 = rbf((x0 - mean_) * rstd_ * bflo(eg[i][j]) + bflo(es[i][j]));                                    \
                const float h1 = rbf((x1v - mean_) * rstd_ * bfhi(eg[i][j]) + bfhi(es[i][j]));                                   \
                o[j] = pk2(x0 + rbf(bflo(eb[i][j]) * h0), x1v + rbf(bfhi(eb[i][j]) * h1));                                       \
            }                                                                                                                    \
            *(u32x4*)(out + (size_t)myrow * D_ + c0 + 64 * i) = o;                                                               \
        }                                                                                                                        \
    } while (0)
    u32x4 g[4], u[4], d[4];
    bool need = router;
    if (!router) {
        FFN_ELEM_LOAD();
        const unsigned go = (unsigned)(((t >> 1) * F_ + (t & 1) * 8) * 2);
#pragma unroll
        for (int i = 0; i < 4; ++i) { GLD4(g[i], go, wg + (size_t)256 * i * F_ + chunk * 16); GLD4(u[i], go, wu + (size_t)256 * i * F_ + chunk * 16); }
#pragma unroll
        for (int i = 0; i < 4; ++i) GLD4(d[i], (unsigned)((t & 127) * 16), wd + (size_t)(chunk * 16 + (w >> 1) * 4 + i) * D_);
        DIAG_T(0);
        FFN_ELEM(12);
        DIAG_T(1);
        asm volatile("s_waitcnt vmcnt(2)" : "+v"(g[0]), "+v"(g[1]), "+v"(g[2]), "+v"(g[3]), "+v"(u[0]), "+v"(u[1]), "+v"(u[2]), "+v"(u[3]),
                     "+v"(d[0]), "+v"(d[1]), "+v"(d[2]), "+v"(d[3]) :: "memory");
        DIAG_T(2);
        int to = 1;
        for (int spins = 0; spins < 20000; ++spins) {
            const unsigned long long v = __hip_atomic_load(gr, RLX_AGENT);
            if (__all((v >> 32) == 0x5EEDu)) { *(f32x2*)&sh_h[2 * t] = f32x2{bflo((unsigned)v), bfhi((unsigned)v)}; to = 0; break; }
            __builtin_amdgcn_s_sleep(POLL_SLEEP);
        }
        if (lane == 0) sh_to[w] = to;
        DIAG_T(3);
        LDS_BAR();
        DIAG_T(4);
        need = __builtin_amdgcn_readfirstlane(__any(sh_to[lane & 7] != 0));
    }
    if (need) {
        f32x4 pm[8], p2[8], pd[8];
        float Gs[8];
#pragma unroll
        for (int i = 0; i < 8; ++i) Gs[i] = Gp[i];
        const unsigned po = (unsigned)(t * 16);
#pragma unroll
        for (int i = 0; i < 8; ++i) GLD4(pm[i], po, part + (i * 3) * S_);
#pragma unroll
        for (int i = 0; i < 8; ++i) { GLD4(p2[i], po, part + (i * 3 + 1) * S_); GLD4(pd[i], po, part + (i * 3 + 2) * S_); }
        unsigned g2t, sbt, wrt;
        GLD1(g2t, (unsigned)(t * 4), modb + M_G2); GLD1(sbt, (unsigned)(t * 4), modb + M_SB); GLD1(wrt, (unsigned)(t * 4), wr);
        DIAG_T(0);
        asm volatile("s_waitcnt vmcnt(19)" : "+v"(pm[0]), "+v"(pm[1]), "+v"(pm[2]), "+v"(pm[3]), "+v"(pm[4]), "+v"(pm[5]), "+v"(pm[6]), "+v"(pm[7]) :: "memory");
        const f32x4 mean = (((pm[0] + pm[1]) + (pm[2] + pm[3])) + ((pm[4] + pm[5]) + (pm[6] + pm[7]))) * 0.125f;
#pragma unroll
        for (int i = 0; i < 8; ++i) pm[i] -= mean;
        asm volatile("s_waitcnt vmcnt(3)" : "+v"(p2[0]), "+v"(p2[1]), "+v"(p2[2]), "+v"(p2[3]), "+v"(p2[4]), "+v"(p2[5]), "+v"(p2[6]), "+v"(p2[7]),
                     "+v"(pd[0]), "+v"(pd[1]), "+v"(pd[2]), "+v"(pd[3]), "+v"(pd[4]), "+v"(pd[5]), "+v"(pd[6]), "+v"(pd[7]) :: "memory");
        DIAG_T(1);
#pragma unroll
        for (int i = 0; i < 8; ++i) { p2[i] += 128.f * pm[i] * pm[i]; pd[i] += pm[i] * Gs[i]; }
        const f32x4 M2 = ((p2[0] + p2[1]) + (p2[2] + p2[3])) + ((p2[4] + p2[5]) + (p2[6] + p2[7]));
        const f32x4 DT = ((pd[0] + pd[1]) + (pd[2] + pd[3])) + ((pd[4] + pd[5]) + (pd[6] + pd[7]));
        float best = -INFINITY, bm = mean[0], br = 0.f; int bidx = t * 4;
#pragma unroll
        for (int e = 0; e < 4; ++e) {
            const float rs = rsqrtf(M2[e] * (1.f / D_) + LN_EPS);
            const float rv = DT[e] * rs;
            if (rv > best || e == 0) { if (rv == rv) best = rv; bidx = t * 4 + e; bm = mean[e]; br = rs; }
        }
        {
            const float m = wmax(best);
            const int src = (int)__builtin_ctzll(__ballot(best == m) | (1ull << 63));
            if (lane == src) sh_best[w] = f32x4{m, __int_as_float(bidx), bm, br};
        }
        DIAG_T(2);
        LDS_BAR();
        {
            const f32x4 c = sh_best[lane & 7];
            const float m = row_max16(c[0]);
            const int src = (int)__builtin_ctzll(__ballot(c[0] == m) | (1ull << 63));
            bidx = __builtin_amdgcn_readlane(__float_as_int(c[1]), src) & (S_ - 1);
            bm = __int_as_float(__builtin_amdgcn_readlane(__float_as_int(c[2]), src));
            br = __int_as_float(__builtin_amdgcn_readlane(__float_as_int(c[3]), src));
        }
        DIAG_T(3);
        unsigned xs;
        GLD1(xs, (unsigned)(t * 4), x1 + (size_t)bidx * D_);
        asm volatile("s_waitcnt vmcnt(0)" : "+v"(xs), "+v"(g2t), "+v"(sbt), "+v"(wrt) :: "memory");
        DIAG_T(4);
        const float h0 = rbf((bflo(xs) - bm) * br * bflo(g2t) + bflo(sbt));
        const float h1 = rbf((bfhi(xs) - bm) * br * bfhi(g2t) + bfhi(sbt));
        if (router) {
            __hip_atomic_store(gr, (0x5EEDull << 32) | pk2(h0, h1), RLX_AGENT);
            FFN_ELEM_LOAD();
            if (b == 0) {
                *(f32x2*)&sel[2 * t] = f32x2{h0, h1}; *(f32x2*)&sel[1024 + 2 * t] = f32x2{bflo(xs), bfhi(xs)};
                const float rr = wsum(h0 * bflo(wrt) + h1 * bfhi(wrt));
                if (lane == 0) sh_r[w] = rr;
                LDS_BAR();
                if (t == 0) {
                    float r2 = 0.f;
#pragma unroll
                    for (int i = 0; i < 8; ++i) r2 += sh_r[i];
                    r2 = rbf(r2);
                    *(f32x2*)meta = f32x2{__int_as_float(bidx), rbf(1.f / (1.f + __expf(-r2)))};
                }
            }
            FFN_ELEM(0);
            DIAG_END(7);
            return;
        }
        *(f32x2*)&sh_h[2 * t] = f32x2{h0, h1};
        LDS_BAR();
    }
    {
        float v[16];
#pragma unroll
        for (int j = 0; j < 16; ++j) v[j] = 0.f;
#pragma unroll
        for (int i = 0; i < 4; ++i) {
            const float hv = sh_h[(t >> 1) + 256 * i];
#pragma unroll
            for (int j = 0; j < 4; ++j) {
                v[2 * j] += hv * bflo(g[i][j]); v[2 * j + 1] += hv * bfhi(g[i][j]);
                v[8 + 2 * j] += hv * bflo(u[i][j]); v[9 + 2 * j] += hv * bfhi(u[i][j]);
            }
        }
#pragma unroll
        for (int j = 0; j < 8; ++j) v[j] = sw32sum(v[j], v[8 + j]);
        f32x4 z;
#pragma unroll
        for (int j = 0; j < 4; ++j) {
            float q = sw16sum(v[j], v[4 + j]);
            q += dpp<XOR2>(q); q += dpp<ROR4>(q); q += dpp<ROR8>(q);
            z[j] = q;
        }
        if ((lane & 14) == 0) *(f32x4*)&sh_gu[w][hh * 16 + (lane & 1) * 8 + ((lane >> 4) & 1) * 4] = z;
        DIAG_T(5);
        LDS_BAR();
        float s = 0.f;
#pragma unroll
        for (int i = 0; i < 4; ++i) s += sh_gu[hh * 4 + i][lane & 31];
        s = sw32sum(s, s);
        auto p = __builtin_amdgcn_permlane16_swap(__float_as_uint(s), __float_as_uint(s), false, false);
        const float sg = rbf(__uint_as_float(p[0])), su = rbf(__uint_as_float(p[1]));
        const float act = rbf(rbf(sg / (1.f + __expf(-sg))) * su);
        DIAG_T(6);
        f32x4 y0 = f32x4{}, y1 = f32x4{};
#pragma unroll
        for (int i = 0; i < 4; ++i) {
            const float a = __int_as_float(__builtin_amdgcn_readlane(__float_as_int(act), (w >> 1) * 4 + i));
#pragma unroll
            for (int j = 0; j < 2; ++j) {
                y0[2 * j] += a * bflo(d[i][j]); y0[2 * j + 1] += a * bfhi(d[i][j]);
                y1[2 * j] += a * bflo(d[i][2 + j]); y1[2 * j + 1] += a * bfhi(d[i][2 + j]);
            }
        }
        *(f32x4*)&sh_red[w >> 1][(t & 127) * 8] = y0;
        *(f32x4*)&sh_red[w >> 1][(t & 127) * 8 + 4] = y1;
        LDS_BAR();
        DIAG_T(7);
        float s0 = 0.f, s1 = 0.f;
#pragma unroll
        for (int i = 0; i < 4; ++i) { const f32x2 v2 = *(const f32x2*)&sh_red[i][2 * t]; s0 += v2[0]; s1 += v2[1]; }
        *(f32x2*)(yp + (size_t)chunk * 1024 + 2 * t) = f32x2{s0, s1};
    }
    DIAG_END(5);
}

__global__ __launch_bounds__(256) void k_fin(const float* __restrict__ yp, const float* __restrict__ sel, const float* __restrict__ meta,
                                             const float* __restrict__ mod, u16* __restrict__ out DIAG_ARG) {
    DIAG_BEGIN
    __shared__ f32x4 red[4][4];
    const int t = threadIdx.x, c4 = t & 3, rg = t >> 2;
    const int col = blockIdx.x * 16 + c4 * 4;
    f32x4 v[4];
    const unsigned yo = (unsigned)((rg * 1024 + col) * 4), yo3 = (unsigned)(((rg & 31) * 1024 + col) * 4);
    GLD4(v[0], yo, yp); GLD4(v[1], yo, yp + 64 * 1024); GLD4(v[2], yo, yp + 128 * 1024); GLD4(v[3], yo3, yp + 192 * 1024);
    const int n = blockIdx.x * 16 + (t & 15);
    float hs, xs, gb; f32x2 mt;
    GLD1(hs, (unsigned)(n * 4), sel); GLD1(xs, (unsigned)(n * 4), sel + 1024); GLD1(gb, (unsigned)(n * 4), mod + M_GB); GLD2(mt, 0u, meta);
    asm volatile("s_waitcnt vmcnt(0)" : "+v"(v[0]), "+v"(v[1]), "+v"(v[2]), "+v"(v[3]), "+v"(hs), "+v"(xs), "+v"(gb), "+v"(mt) :: "memory");
    if (rg >= 32) v[3] = f32x4{};
    const float sig = mt[1];
    const int idx = __float_as_int(mt[0]) & (S_ - 1);
    f32x4 s = (v[0] + v[1]) + (v[2] + v[3]);
#pragma unroll
    for (int e = 0; e < 4; ++e) { float a = s[e]; a += dpp<ROR4>(a); a += dpp<ROR8>(a); a = sw16sum(a, a); s[e] = sw32sum(a, a); }
    if ((t & 63) < 4) red[t >> 6][c4] = s;
    LDS_BAR();
    if (t < 16) {
        const float y = (red[0][t >> 2][t & 3] + red[1][t >> 2][t & 3]) + (red[2][t >> 2][t & 3] + red[3][t >> 2][t & 3]);
        const float yy = rbf(rbf(y) * sig);
        const float hb = rbf(hs + yy);
        const float o = xs + rbf(gb * hb);
        out[(size_t)idx * D_ + n] = (u16)(pk2(o, o) & 0xffffu);
    }
    DIAG_END(6);
}

extern "C" void kernel_launch(void* const* d_in, const int* in_sizes, int n_in, void* d_out, int out_size, void* d_ws, size_t ws_size,
                              hipStream_t stream) {
    const u16* x = (const u16*)d_in[0];
    const u16* wqkv = (const u16*)d_in[1];
    const u16* wo = (const u16*)d_in[2];
    const u16* wr = (const u16*)d_in[4];
    const u16* wg = (const u16*)d_in[5];
    const u16* wu = (const u16*)d_in[6];
    const u16* wd = (const u16*)d_in[7];
    const u16* c = (const u16*)d_in[8];
    const u16* wmod = (const u16*)d_in[9];
    char* ws = (char*)d_ws;
    float* mod = (float*)(ws + WS_MOD);
    u16* modb = (u16*)(ws + WS_MODB);
    unsigned long long* gran = (unsigned long long*)(ws + WS_GRAN);
    float* kn2 = (float*)(ws + WS_KN2);
    float* sel = (float*)(ws + WS_SEL);
    float* meta = (float*)(ws + WS_META);
    float* Gp = (float*)(ws + WS_GP);
    float* part = (float*)(ws + WS_PART);
    float* yp = (float*)(ws + WS_YP);
    f32x2* stat = (f32x2*)(ws + WS_STAT);
    u16* Qf = (u16*)(ws + WS_QF);
    u16* KVf = (u16*)(ws + WS_KVF);
    u16* Of = (u16*)(ws + WS_OF);
    u16* x1 = (u16*)(ws + WS_X1);
    u16* out = (u16*)d_out;
    k_mod<<<512, 512, 0, stream>>>(c, wmod, wr, x, mod, modb, stat, gran DIAG_PASS);
    k_qkv<<<256, 768, 0, stream>>>(x, stat, mod, wqkv, Qf, KVf, kn2 DIAG_PASS);
    k_attn<<<256, 512, 0, stream>>>(Qf, KVf, kn2, Of DIAG_PASS);
    k_oproj<<<256, 512, 0, stream>>>(Of, wo, x, mod, x1, part, Gp DIAG_PASS);
    k_ffn<<<256, 512, 0, stream>>>(part, Gp, x1, modb, wr, wg, wu, wd, yp, sel, meta, gran, out DIAG_PASS);
    k_fin<<<64, 256, 0, stream>>>(yp, sel, meta, mod, out DIAG_PASS);
}
```

```cpp
#include <hip/hip_runtime.h>
#include <hip/hip_bf16.h>
#include <stdint.h>

typedef __attribute__((ext_vector_type(8))) short bf16x8;
typedef __attribute__((ext_vector_type(4))) short s16x4;
typedef __attribute__((ext_vector_type(16))) float f32x16;
typedef __attribute__((ext_vector_type(4))) float f32x4;
typedef __attribute__((ext_vector_type(2))) float f32x2;
typedef __attribute__((ext_vector_type(4))) unsigned u32x4;
typedef __attribute__((ext_vector_type(2))) unsigned u32x2;
typedef __bf16 bf16x2_t __attribute__((ext_vector_type(2)));
typedef unsigned short u16;
typedef __attribute__((address_space(3))) const char* lds_cptr;
typedef __attribute__((address_space(3))) char* lds_ptr;

#define DI __device__ __forceinline__
#define MFMA32(a, b, c) __builtin_amdgcn_mfma_f32_32x32x16_bf16((a), (b), (c), 0, 0, 0)
#define MFMA16(a, b, c) __builtin_amdgcn_mfma_f32_16x16x32_bf16((a), (b), (c), 0, 0, 0)
#define SBAR() __builtin_amdgcn_sched_barrier(0)

constexpr int S_ = 2048, D_ = 1024, H_ = 32, F_ = 3584;
constexpr float LN_EPS = 1e-5f;
constexpr float QSCALE = 0.17677669529663687f * 1.4426950408889634f;

constexpr size_t WS_MOD = 0;
constexpr size_t WS_KN2 = 32768;
constexpr size_t WS_SC = 65536;
constexpr size_t WS_STAT = 98304;
constexpr size_t WS_MEAN = 114688;
constexpr size_t WS_GRAN = 131072;
#define NREP 8
constexpr int G_H2 = 32 * 12288, G_BEST = G_H2 + 32 * 4096, G_Y = G_BEST + NREP * 4096, G_END = G_Y + 225 * 8192;
constexpr size_t WS_QF = 6u << 20;
constexpr size_t WS_KVF = WS_QF + (4u << 20);
constexpr size_t WS_OF = WS_KVF + (8u << 20);
constexpr size_t WS_WT = WS_OF + (4u << 20);

#define DIAG_ARG
#define DIAG_BEGIN
#define DIAG_T(i)
#define DIAG_END(kid)
#define DIAG_PASS
DI float bflo(unsigned u) { return __uint_as_float(u << 16); }
DI float bfhi(unsigned u) { return __uint_as_float(u & 0xffff0000u); }
DI unsigned pk2(float lo, float hi) {
    f32x2 v = {lo, hi};
    return __builtin_bit_cast(unsigned, __builtin_convertvector(v, bf16x2_t));
}
DI float rbf(float x) { return bflo(pk2(x, x) & 0xffffu); }
DI float bf2f(u16 v) { return __uint_as_float((unsigned)v << 16); }
DI s16x4 trread(lds_cptr p) {
    typedef short v4i16_t __attribute__((ext_vector_type(4)));
    return __builtin_bit_cast(s16x4, __builtin_amdgcn_ds_read_tr16_b64_v4i16((__attribute__((address_space(3))) v4i16_t*)p));
}
DI bf16x8 ldsfrag(lds_cptr p) { return *(const __attribute__((address_space(3))) bf16x8*)p; }
DI bf16x8 cat4(s16x4 a, s16x4 b) { return __builtin_shufflevector(a, b, 0, 1, 2, 3, 4, 5, 6, 7); }
DI void glds16(const void* sbase, unsigned voff, unsigned lds_base) {
    unsigned sv;
    asm volatile("s_mov_b32 %0, m0\n\ts_mov_b32 m0, %3\n\ts_nop 0\n\tglobal_load_lds_dwordx4 %1, %2\n\ts_mov_b32 m0, %0"
                 : "=&s"(sv) : "v"(voff), "s"(sbase), "s"(lds_base) : "memory");
}
#define GLD4(dst, voff, sbase) asm volatile("global_load_dwordx4 %0, %1, %2" : "=v"(dst) : "v"(voff), "s"(sbase) : "memory")
#define GLD2(dst, voff, sbase) asm volatile("global_load_dwordx2 %0, %1, %2" : "=v"(dst) : "v"(voff), "s"(sbase) : "memory")
#define GLD1(dst, voff, sbase) asm volatile("global_load_dword %0, %1, %2" : "=v"(dst) : "v"(voff), "s"(sbase) : "memory")
#define GLDH(dst, voff, sbase) asm volatile("global_load_ushort %0, %1, %2" : "=v"(dst) : "v"(voff), "s"(sbase) : "memory")
#define WAIT_VM(n) asm volatile("s_waitcnt vmcnt(" #n ")" ::: "memory")
#define WAIT_BAR(n) asm volatile("s_waitcnt vmcnt(" #n ") lgkmcnt(0)\n\ts_barrier" ::: "memory")
#define LDS_BAR() asm volatile("s_waitcnt lgkmcnt(0)\n\ts_barrier" ::: "memory")
DI float xor32(float v) {
    auto r = __builtin_amdgcn_permlane32_swap(__float_as_uint(v), __float_as_uint(v), false, false);
    return (threadIdx.x & 32) ? __uint_as_float(r[0]) : __uint_as_float(r[1]);
}
DI float wave_sum(float v) {
#pragma unroll
    for (int o = 1; o < 64; o <<= 1) v += __shfl_xor(v, o);
    return v;
}
DI float wave_max(float v) {
#pragma unroll
    for (int o = 1; o < 64; o <<= 1) v = fmaxf(v, __shfl_xor(v, o));
    return v;
}

template <int CTRL> DI float dpp(float x) { return __builtin_bit_cast(float, __builtin_amdgcn_mov_dpp(__builtin_bit_cast(int, x), CTRL, 0xf, 0xf, true)); }
constexpr int XOR1 = 0xB1, XOR2 = 0x4E, ROR4 = 0x124, ROR8 = 0x128;
DI float sw32sum(float a, float b) {
    auto r = __builtin_amdgcn_permlane32_swap(__float_as_uint(a), __float_as_uint(b), false, false);
    return __uint_as_float(r[0]) + __uint_as_float(r[1]);
}
DI float sw16sum(float a, float b) {
    auto r = __builtin_amdgcn_permlane16_swap(__float_as_uint(a), __float_as_uint(b), false, false);
    return __uint_as_float(r[0]) + __uint_as_float(r[1]);
}
DI float row_sum16(float v) { v += dpp<XOR1>(v); v += dpp<XOR2>(v); v += dpp<ROR4>(v); v += dpp<ROR8>(v); return v; }
DI float row_max16(float v) { v = fmaxf(v, dpp<XOR1>(v)); v = fmaxf(v, dpp<XOR2>(v)); v = fmaxf(v, dpp<ROR4>(v)); v = fmaxf(v, dpp<ROR8>(v)); return v; }
DI float wsum(float v) { v = row_sum16(v); v = sw16sum(v, v); return sw32sum(v, v); }
DI float wmax(float m) {
    m = row_max16(m);
    { auto s_ = __builtin_amdgcn_permlane16_swap(__float_as_uint(m), __float_as_uint(m), false, false); m = fmaxf(__uint_as_float(s_[0]), __uint_as_float(s_[1])); }
    { auto s_ = __builtin_amdgcn_permlane32_swap(__float_as_uint(m), __float_as_uint(m), false, false); m = fmaxf(__uint_as_float(s_[0]), __uint_as_float(s_[1])); }
    return m;
}
DI float sfma(float a, float b, float c) { float r; asm("v_fma_f32 %0, %1, %2, %3" : "=v"(r) : "v"(a), "v"(b), "v"(c)); return r; }
#define LAS __attribute__((address_space(3)))
constexpr int M_SA = 0, M_G1 = 1024, M_GA = 2048, M_SB = 3072, M_G2 = 4096, M_GB = 5120, M_GW = 6144, M_SW = 7168;

__global__ __launch_bounds__(512) void k_mod(const u16* __restrict__ c, const u16* __restrict__ wmod, u16* __restrict__ scb,
                                             const u16* __restrict__ x, float* __restrict__ mod, float* __restrict__ mean1,
                                             f32x2* __restrict__ stat DIAG_ARG) {
    DIAG_BEGIN
    __shared__ __attribute__((aligned(16))) float red[8][16];
    const int t = threadIdx.x, lane = t & 63;
    if (blockIdx.x < 128) {
        const int row = blockIdx.x * 16 + (t >> 6) * 2;
        u32x4 v[4];
#pragma unroll
        for (int i = 0; i < 4; ++i) v[i] = *(const u32x4*)(x + (size_t)row * D_ + i * 512 + lane * 8);
#pragma unroll
        for (int r = 0; r < 2; ++r) {
            float f[16], s = 0.f;
#pragma unroll
            for (int j = 0; j < 8; ++j) { f[2 * j] = bflo(v[2 * r + (j >> 2)][j & 3]); f[2 * j + 1] = bfhi(v[2 * r + (j >> 2)][j & 3]); s += f[2 * j] + f[2 * j + 1]; }
            const float mean = wsum(s) * (1.f / D_);
            float q = 0.f;
#pragma unroll
            for (int j = 0; j < 16; ++j) { const float d = f[j] - mean; q += d * d; }
            const float rstd = rsqrtf(wsum(q) * (1.f / D_) + LN_EPS);
            if (lane == 0) { stat[row + r] = f32x2{rstd, -mean * rstd}; mean1[row + r] = mean; }
        }
        DIAG_END(1);
        return;
    }
    const int b = blockIdx.x - 128;
    const int chunk = (b >> 5) * 32 + (b & 7) * 4 + ((b >> 3) & 3);
    const int col = chunk * 16 + (t & 1) * 8, rg = t >> 1;
    u32x4 w[4]; unsigned cv[4];
#pragma unroll
    for (int i = 0; i < 4; ++i) GLDH(cv[i], (unsigned)(rg * 2), c + 256 * i);
#pragma unroll
    for (int i = 0; i < 4; ++i) GLD4(w[i], (unsigned)((rg * 6144 + col) * 2), wmod + (size_t)256 * i * 6144);
    asm volatile("s_waitcnt vmcnt(4)" : "+v"(cv[0]), "+v"(cv[1]), "+v"(cv[2]), "+v"(cv[3]) :: "memory");
    float sc[4];
#pragma unroll
    for (int i = 0; i < 4; ++i) { const float cf = bflo(cv[i]); sc[i] = rbf(cf / (1.f + __expf(-cf))); }
    if (b == 0 && (t & 1) == 0) {
#pragma unroll
        for (int i = 0; i < 4; ++i) scb[256 * i + (rg & ~31) + ((rg >> 2) & 3) * 8 + ((rg >> 4) & 1) * 4 + (rg & 3)] = (u16)(__float_as_uint(sc[i]) >> 16);
    }
    asm volatile("s_waitcnt vmcnt(0)" : "+v"(w[0]), "+v"(w[1]), "+v"(w[2]), "+v"(w[3]) :: "memory");
    float acc[8];
#pragma unroll
    for (int j = 0; j < 8; ++j) acc[j] = 0.f;
#pragma unroll
    for (int i = 0; i < 4; ++i)
#pragma unroll
        for (int j = 0; j < 4; ++j) { acc[2 * j] += sc[i] * bflo(w[i][j]); acc[2 * j + 1] += sc[i] * bfhi(w[i][j]); }
#pragma unroll
    for (int j = 0; j < 4; ++j) acc[j] = sw32sum(acc[j], acc[4 + j]);
    f32x2 z;
#pragma unroll
    for (int j = 0; j < 2; ++j) {
        float q = sw16sum(acc[j], acc[2 + j]);
        q += dpp<XOR2>(q); q += dpp<ROR4>(q); q += dpp<ROR8>(q);
        z[j] = q;
    }
    if ((lane & 14) == 0) *(f32x2*)&red[t >> 6][(lane & 1) * 8 + (lane >> 5) * 4 + ((lane >> 4) & 1) * 2] = z;
    LDS_BAR();
    if (t < 16) {
        float s = ((red[0][t] + red[1][t]) + (red[2][t] + red[3][t])) + ((red[4][t] + red[5][t]) + (red[6][t] + red[7][t]));
        const int n = chunk * 16 + t, sec = n >> 10;
        s = rbf(s);
        if (sec == 1) s = rbf(1.f + s);
        mod[n] = s;
    }
    DIAG_END(0);
}

#define QSC1 "sc1"
#define QABL 0
constexpr int QA_SLOT = 8576, QB_SLOT = 12288, QB_OFF = 5 * QA_SLOT, QG_OFF = QB_OFF + 9 * QB_SLOT, QK_LDS = QG_OFF + 8192;
__global__ __launch_bounds__(768, 3) void k_qkv(const u16* __restrict__ x, const f32x2* __restrict__ stat, const float* __restrict__ mod,
                                                const u16* __restrict__ W, u16* __restrict__ Qf, u16* __restrict__ KVf,
                                                float* __restrict__ kn2 DIAG_ARG) {
    DIAG_BEGIN
    __shared__ __attribute__((aligned(16))) char lds[QK_LDS];
    const int b = blockIdx.x, tid = threadIdx.x, lane = tid & 63;
    const int w = __builtin_amdgcn_readfirstlane(tid >> 6);
    const int xcd = b & 7, idx = b >> 3;
    const int rp = (xcd & 1) * 8 + (idx & 7), cp = (xcd >> 1) * 4 + (idx >> 3);
    const unsigned lds0 = (unsigned)(uintptr_t)lds;
    if (w >= 8) {
        const int lw = w - 8;
        const unsigned boff = (unsigned)(((lw * 8 + (lane >> 3)) * 3072 + cp * 64 + ((lane & 7) ^ ((lane >> 2) & 4)) * 8) * 2);
        glds16((const char*)mod + lw * 2048, lane * 16, lds0 + QG_OFF + lw * 2048);
        glds16((const char*)mod + lw * 2048 + 1024, lane * 16, lds0 + QG_OFF + lw * 2048 + 1024);
        const char* src = (const char*)W;
        unsigned dst = lds0 + QB_OFF + lw * 1024;
        const unsigned dend = lds0 + QB_OFF + 9 * QB_SLOT;
#define QB_UNIT() do { if (!(QABL & 4)) { glds16(src, boff, dst); glds16(src + 2048, boff, dst + 4096); glds16(src + 4096, boff, dst + 8192); }  \
                       src += 32 * 3072 * 2; dst += QB_SLOT; if (dst >= dend) dst -= 9 * QB_SLOT; } while (0)
#pragma unroll 1
        for (int u = 0; u < 6; ++u) QB_UNIT();
        WAIT_BAR(18);
#pragma unroll 1
        for (int j = 0; j < 13; ++j) { WAIT_BAR(12); QB_UNIT(); QB_UNIT(); }
        WAIT_BAR(12); WAIT_BAR(6); WAIT_BAR(0);
    } else if (w >= 4) {
        const int p = tid - 256, c = p & 7, r = p >> 3;
        const unsigned xoff = (unsigned)(((rp * 128 + r) * D_ + c * 8) * 2);
        u32x4 xa[4], xb[4]; f32x2 st[4];
#define QX_LOAD(dst, j) do { _Pragma("unroll") for (int i = 0; i < 4; ++i) GLD4(dst[i], xoff, x + (size_t)i * 32 * D_ + (j) * 64); } while (0)
#pragma unroll
        for (int i = 0; i < 4; ++i) GLD2(st[i], (unsigned)((rp * 128 + r) * 8), stat + 32 * i);
        QX_LOAD(xa, 0); QX_LOAD(xb, 1);
        const lds_cptr gp = (lds_cptr)lds + QG_OFF + c * 32;
        const unsigned wbase = (c & 3) * 528 + r * 16 + (c >> 2) * 64;
        int slot = c >> 2;
        asm volatile("s_waitcnt vmcnt(8)" : "+v"(st[0]), "+v"(st[1]), "+v"(st[2]), "+v"(st[3]) :: "memory");
        LDS_BAR();
#define QA_STEP(xs, j) do {                                                                                       \
            const lds_cptr g_ = gp + (j) * 256;                                                                   \
            const f32x4 s0 = *(const LAS f32x4*)g_, s1 = *(const LAS f32x4*)(g_ + 16);                            \
            const f32x4 g0 = *(const LAS f32x4*)(g_ + 4096), g1 = *(const LAS f32x4*)(g_ + 4096 + 16);            \
            asm volatile("s_waitcnt vmcnt(4)" : "+v"(xs[0]), "+v"(xs[1]), "+v"(xs[2]), "+v"(xs[3]) :: "memory");  \
            const lds_ptr d_ = (lds_ptr)lds + slot * QA_SLOT + wbase;                                             \
            _Pragma("unroll") for (int i = 0; i < 4; ++i) {                                                       \
                u32x4 o;                                                                                          \
                _Pragma("unroll") for (int e = 0; e < 4; ++e) {                                                   \
                    const float gl = e < 2 ? g0[2 * e] : g1[2 * e - 4], gh = e < 2 ? g0[2 * e + 1] : g1[2 * e - 3];   \
                    const float sl = e < 2 ? s0[2 * e] : s1[2 * e - 4], sh = e < 2 ? s0[2 * e + 1] : s1[2 * e - 3];   \
                    o[e] = pk2(sfma(sfma(bflo(xs[i][e]), st[i][0], st[i][1]), gl, sl), sfma(sfma(bfhi(xs[i][e]), st[i][0], st[i][1]), gh, sh)); \
                }                                                                                                 \
                *(LAS u32x4*)(d_ + i * 2112) = (QABL & 1) ? xs[i] : o;                                                                 \
            }                                                                                                     \
            QX_LOAD(xs, ((j) + 2) & 15);                                   \
            slot += 2; if (slot >= 5) slot -= 5;                                                                  \
            LDS_BAR();                                                                                            \
        } while (0)
#pragma unroll 1
        for (int j = 0; j < 16; j += 2) { QA_STEP(xa, j); QA_STEP(xb, j + 1); }
        asm volatile("s_waitcnt vmcnt(0)" : "+v"(xa[0]), "+v"(xa[1]), "+v"(xa[2]), "+v"(xa[3]), "+v"(xb[0]), "+v"(xb[1]), "+v"(xb[2]), "+v"(xb[3]) :: "memory");
    } else {
        const int rh = w & 1, hd = w >> 1, G = lane >> 4, hh = lane >> 5;
        const lds_cptr ap = (lds_cptr)lds + rh * 4224 + hh * 528 + (lane & 31) * 16;
        const lds_cptr bp = (lds_cptr)lds + QB_OFF + hh * 1024 + ((lane & 15) >> 2) * 128 + ((hd ^ (lane >> 3)) & 1) * 64 + (G & 1) * 32 + (lane & 3) * 8;
        f32x16 acc[2][3];
#pragma unroll
        for (int i = 0; i < 2; ++i)
#pragma unroll
            for (int j = 0; j < 3; ++j) acc[i][j] = f32x16{};
        bf16x8 a0[2], b0[3], a1[2], b1[3];
#define QC_READ(af, bf, a_, b_, s) do {                                                                           \
            af[0] = ldsfrag((a_) + (s) * 1056); af[1] = ldsfrag((a_) + (s) * 1056 + 2112);                        \
            _Pragma("unroll") for (int j = 0; j < 3; ++j) bf[j] = cat4(trread((b_) + j * 4096 + (s) * 2048), trread((b_) + j * 4096 + (s) * 2048 + 512)); \
        } while (0)
#define QC_RB(bf, j, b_, s) do { if (!(QABL & 2)) bf[j] = cat4(trread((b_) + (j) * 4096 + (s) * 2048), trread((b_) + (j) * 4096 + (s) * 2048 + 512)); } while (0)
#define QC_STEP(af, bf, an, bn, a_, b_, s) do {                                                                   \
            acc[0][0] = MFMA32(bf[0], af[0], acc[0][0]); SBAR();                                                  \
            if (!(QABL & 2)) { an[0] = ldsfrag((a_) + (s) * 1056); an[1] = ldsfrag((a_) + (s) * 1056 + 2112); } SBAR();   \
            acc[0][1] = MFMA32(bf[1], af[0], acc[0][1]); SBAR();                                                  \
            QC_RB(bn, 0, b_, s); SBAR();                                                                          \
            acc[0][2] = MFMA32(af[0], bf[2], acc[0][2]); SBAR();                                                  \
            QC_RB(bn, 1, b_, s); SBAR();                                                                          \
            acc[1][0] = MFMA32(bf[0], af[1], acc[1][0]); SBAR();                                                  \
            QC_RB(bn, 2, b_, s); SBAR();                                                                          \
            acc[1][1] = MFMA32(bf[1], af[1], acc[1][1]);                                                          \
            acc[1][2] = MFMA32(af[1], bf[2], acc[1][2]); SBAR();                                                  \
        } while (0)
        asm volatile("s_barrier" ::: "memory");
        asm volatile("s_barrier" ::: "memory");
        DIAG_T(0);
        int as = 0, bs = 0;
        lds_cptr a_ = ap, b_ = bp;
        QC_READ(a0, b0, a_, b_, 0);
        if (QABL & 2) QC_READ(a1, b1, a_, b_, 1);
        SBAR(); __builtin_amdgcn_s_waitcnt(0xC07F); SBAR();
#define QC_NEXT(odd) do { as += QA_SLOT; if (as >= 5 * QA_SLOT) as = 0; bs += QB_SLOT; if (bs >= 9 * QB_SLOT) bs = 0;   \
                          a_ = ap + as + (odd) * 64; b_ = bp + bs; } while (0)
#pragma unroll 1
        for (int j = 0; j < 16; ++j) {
            QC_STEP(a0, b0, a1, b1, a_, b_, 1);
            QC_NEXT(1);
            QC_STEP(a1, b1, a0, b0, a_, b_, 0);
            if (j < 15) asm volatile("s_barrier" ::: "memory");
            QC_STEP(a0, b0, a1, b1, a_, b_, 1);
            QC_NEXT(0);
            QC_STEP(a1, b1, a0, b0, a_, b_, 0);
        }
        DIAG_T(1);
        const int head = cp * 2 + hd;
#pragma unroll
        for (int i = 0; i < 2; ++i) {
            const int tile = rp * 4 + rh * 2 + i;
            u32x4* qo = (u32x4*)((char*)Qf + ((size_t)(head * 64 + tile) * 2) * 1024) + lane;
            u32x4* kvo = (u32x4*)((char*)KVf + ((size_t)(head * 64 + tile) * 4) * 1024) + lane;
            float n2a = 0.f, n2b = 0.f;
#pragma unroll
            for (int s = 0; s < 2; ++s) {
                u32x4 q, k, v;
#pragma unroll
                for (int j = 0; j < 4; ++j) {
                    const float k0 = acc[i][1][8 * s + 2 * j], k1 = acc[i][1][8 * s + 2 * j + 1];
                    q[j] = pk2(acc[i][0][8 * s + 2 * j] * QSCALE, acc[i][0][8 * s + 2 * j + 1] * QSCALE);
                    k[j] = pk2(k0, k1);
                    v[j] = pk2(acc[i][2][8 * s + 2 * j], acc[i][2][8 * s + 2 * j + 1]);
                    n2a += k0 * k0; n2b += k1 * k1;
                }
                {
                    const char* qb = (const char*)Qf + ((size_t)(head * 64 + tile) * 2 + s) * 1024;
                    const char* kb = (const char*)KVf + ((size_t)(head * 64 + tile) * 4 + s) * 1024;
                    asm volatile("global_store_dwordx4 %0, %1, %2 " QSC1 "\n\ts_nop 1" :: "v"(lane * 16), "v"(q), "s"(qb) : "memory");
                    asm volatile("global_store_dwordx4 %0, %1, %2 " QSC1 "\n\ts_nop 1" :: "v"(lane * 16), "v"(k), "s"(kb) : "memory");
                    asm volatile("global_store_dwordx4 %0, %1, %2 offset:2048 " QSC1 "\n\ts_nop 1" :: "v"(lane * 16), "v"(v), "s"(kb) : "memory");
                }
            }
            float n2 = n2a + n2b;
            n2 = wmax(sw32sum(n2, n2));
            if (lane == 0) kn2[head * 64 + tile] = n2;
        }
    }
    DIAG_END(2);
}

constexpr int AT_SLOT = 32768;
#define HABL 0
#define WT_ST " sc1"
#define GST4W(voff, val, sbase) asm volatile("global_store_dwordx4 %0, %1, %2" WT_ST "\n\ts_nop 1" :: "v"(voff), "v"(val), "s"(sbase) : "memory")
DI int ffn_chunk(int b) { const int fb = b - 32; return (fb >> 5) * 32 + (fb & 7) * 4 + ((fb >> 3) & 3); }
__global__ __launch_bounds__(768, 3) void k_attn(const u16* __restrict__ Qf, const u16* __restrict__ KVf, const float* __restrict__ kn2,
                                                 u16* __restrict__ Of, const u16* __restrict__ wg, const u16* __restrict__ wu,
                                                 const u16* __restrict__ wd, char* __restrict__ wt, const u16* __restrict__ wmod,
                                                 const u16* __restrict__ scb, const u16* __restrict__ wr, float* __restrict__ mod,
                                                 const u16* __restrict__ Wo, char* __restrict__ gran DIAG_ARG) {
    DIAG_BEGIN
    __shared__ __attribute__((aligned(16))) char lds[5 * AT_SLOT];
    const int b = blockIdx.x, tid = threadIdx.x, lane = tid & 63;
    const int w = __builtin_amdgcn_readfirstlane(tid >> 6);
    if (w >= 8) {
        const int hw = w - 8;
        const unsigned stg = (unsigned)(uintptr_t)lds + 4 * AT_SLOT + hw * 8192;
        const lds_cptr sp = (lds_cptr)lds + 4 * AT_SLOT + hw * 8192;
        const int chunk = ffn_chunk(b < 32 ? b + 32 : b);
        const int mc = (b >> 5) * 32 + (b & 7) * 4 + ((b >> 3) & 3);
        const unsigned l16 = lane * 16;
        const unsigned goff = (unsigned)(((lane >> 1) * F_ + (lane & 1) * 8) * 2), moff = (unsigned)(((lane >> 1) * 6144 + (lane & 1) * 8) * 2);
        const char* pm = (const char*)wmod + ((size_t)hw * 256 * 6144 + 2048 + mc * 16) * 2;
        const char* pg = (const char*)wg + ((size_t)hw * 256 * F_ + chunk * 16) * 2;
        const char* pu = (const char*)wu + ((size_t)hw * 256 * F_ + chunk * 16) * 2;
        char* o = wt + (size_t)(b * 4 + hw) * 16384;
        u32x4 af[8], gv[4][4]; unsigned wrb;
#define H_BAR() asm volatile("s_waitcnt lgkmcnt(0)\n\ts_barrier" ::: "memory")
#define H_LD(r, p) do { _Pragma("unroll") for (int q = 0; q < 4; ++q) GLD4(gv[r][q], goff, (p) + (size_t)q * 32 * F_ * 2); } while (0)
#define H_ST(r, N) do { asm volatile("s_waitcnt vmcnt(" #N ")" : "+v"(gv[r][0]), "+v"(gv[r][1]), "+v"(gv[r][2]), "+v"(gv[r][3]) :: "memory");   \
                        _Pragma("unroll") for (int q = 0; q < 4; ++q) GST4W(l16, gv[r][q], o + (r) * 4096 + q * 1024); } while (0)
        const char* pd = (const char*)wd + (size_t)chunk * 32768 + hw * 8192;
        H_BAR();
        H_BAR();
        {
            const u32x4 z = u32x4{};
            const unsigned zo = (unsigned)((b * 256 + tid - 512) * 16);
            GST4W(zo, z, gran); GST4W(zo, z, gran + (1 << 20));
            if (zo + (2u << 20) < (unsigned)G_END) GST4W(zo, z, gran + (2 << 20));
        }
#pragma unroll
        for (int q = 0; q < 8; ++q) GLD4(af[q], (unsigned)((lane >> 4) * 16), scb + hw * 256 + q * 32);
        GLDH(wrb, (unsigned)(((mc * 16 + (lane & 15)) & 1023) * 2), wr);
#pragma unroll 1
        for (int q = 0; q < 5; ++q) glds16(pm + (size_t)q * 32 * 6144 * 2, moff, stg + q * 1024);
        H_BAR();
#pragma unroll 1
        for (int q = 5; q < 8; ++q) glds16(pm + (size_t)q * 32 * 6144 * 2, moff, stg + q * 1024);
        H_LD(0, pg);
        H_BAR();
        H_LD(1, pu);
        H_BAR();
        H_LD(2, pg + (size_t)128 * F_ * 2);
        asm volatile("s_waitcnt vmcnt(12)" : "+v"(af[0]), "+v"(af[1]), "+v"(af[2]), "+v"(af[3]), "+v"(af[4]), "+v"(af[5]), "+v"(af[6]), "+v"(af[7]), "+v"(wrb) :: "memory");
        {
            f32x4 m0 = f32x4{}, m1 = f32x4{};
#pragma unroll
            for (int q = 0; q < 8; q += 2) {
                m0 = MFMA16(__builtin_bit_cast(bf16x8, af[q]), cat4(trread(sp + q * 1024 + lane * 8), trread(sp + q * 1024 + 512 + lane * 8)), m0);
                m1 = MFMA16(__builtin_bit_cast(bf16x8, af[q + 1]), cat4(trread(sp + q * 1024 + 1024 + lane * 8), trread(sp + q * 1024 + 1536 + lane * 8)), m1);
            }
            if (lane < 16) *(LAS float*)((lds_ptr)lds + 4 * AT_SLOT + hw * 8192 + lane * 4) = m0[0] + m1[0];
        }
        H_ST(0, 8);
        H_BAR();
        H_LD(3, pu + (size_t)128 * F_ * 2);
        H_ST(1, 12);
        if (hw == 0 && lane < 16) {
            const LAS float* p = (const LAS float*)((lds_cptr)lds + 4 * AT_SLOT) + lane;
            float s = rbf((p[0] + p[2048]) + (p[4096] + p[6144]));
            const int n = 2048 + mc * 16 + lane, sec = n >> 10;
            if (sec == 4) s = rbf(1.f + s);
            mod[n] = s;
            if (sec == 4) mod[M_GW + (n & 1023)] = s * bflo(wrb);
            if (sec == 3) mod[M_SW + (n & 1023)] = s * bflo(wrb);
        }
        H_BAR();
        H_ST(2, 12);
#pragma unroll 1
        for (int i = 0; i < 8; ++i) glds16(pd + i * 1024, l16, stg + 1024);
        H_BAR();
        H_ST(3, 16);
        {
            const char* po = (const char*)Wo + (size_t)((b >> 3) * 32 + hw * 8) * 2048 + (b & 1) * 1024;
#pragma unroll 1
            for (int i = 0; i < 8; ++i) glds16(po + i * 2048, l16, stg + 1024);
        }
        H_BAR();
        WAIT_VM(0);
        return;
    }
    const int head = (b & 7) * 4 + ((b >> 3) & 3), qt = (b >> 5) * 8 + w;
    const unsigned lds0 = (unsigned)(uintptr_t)lds;
    const char* kvbase = (const char*)KVf + (size_t)head * 262144;
    const unsigned voff = (unsigned)(w * 4096 + lane * 16);
#define AT_LOAD(c)                                                                  \
    do {                                                                            \
        const char* g_ = kvbase + (c) * AT_SLOT;                                    \
        const unsigned d_ = lds0 + ((c) & 3) * AT_SLOT + w * 4096;                  \
        glds16(g_, voff, d_); glds16(g_ + 1024, voff, d_ + 1024);                   \
        glds16(g_ + 2048, voff, d_ + 2048); glds16(g_ + 3072, voff, d_ + 3072);     \
    } while (0)
    const bf16x8* qp = (const bf16x8*)((const char*)Qf + (size_t)(head * 64 + qt) * 2048) + lane;
    const bf16x8 q0 = qp[0], q1 = qp[64];
    const float kmx = wave_max(kn2[head * 64 + lane]);
    float qn = 0.f;
#pragma unroll
    for (int j = 0; j < 8; ++j) { float a = bf2f((u16)q0[j]), c = bf2f((u16)q1[j]); qn += a * a + c * c; }
    qn += xor32(qn);
    float mref = sqrtf(qn * kmx) * 1.01f;
    bf16x8 ones;
    { const short o = (((lane >> 4) & 1) == (((lane & 15) >> 2) & 1)) ? (short)0x3f80 : (short)0;
#pragma unroll
      for (int j = 0; j < 8; ++j) ones[j] = o; }
    const lds_cptr fp = (lds_cptr)lds + lane * 16;
    volatile __attribute__((address_space(3))) int* flag = (volatile __attribute__((address_space(3))) int*)((lds_ptr)lds + AT_SLOT);
    f32x16 o; f32x4 ls;
    for (int attempt = 0; attempt < 2; ++attempt) {
        f32x16 negm;
#pragma unroll
        for (int i = 0; i < 16; ++i) negm[i] = -mref;
        o = f32x16{}; ls = f32x4{};
        AT_LOAD(0); AT_LOAD(1); AT_LOAD(2);
        f32x16 sA, sB;
#define AT_S(dst, p_) do { const bf16x8 k0_ = ldsfrag(p_), k1_ = ldsfrag((p_) + 1024); dst = MFMA32(k0_, q0, negm); dst = MFMA32(k1_, q1, dst); } while (0)
#define AT_PV(src, p_) do {                                                                             \
            const bf16x8 v0_ = ldsfrag((p_) + 2048), v1_ = ldsfrag((p_) + 3072);                        \
            u32x4 p0_, p1_;                                                                             \
            _Pragma("unroll") for (int j = 0; j < 4; ++j) {                                             \
                p0_[j] = pk2(__builtin_amdgcn_exp2f(src[2 * j]), __builtin_amdgcn_exp2f(src[2 * j + 1]));         \
                p1_[j] = pk2(__builtin_amdgcn_exp2f(src[8 + 2 * j]), __builtin_amdgcn_exp2f(src[9 + 2 * j])); }   \
            const bf16x8 pa_ = __builtin_bit_cast(bf16x8, p0_), pb_ = __builtin_bit_cast(bf16x8, p1_);  \
            o = MFMA32(v0_, pa_, o); ls = MFMA16(ones, pa_, ls);                                        \
            o = MFMA32(v1_, pb_, o); ls = MFMA16(ones, pb_, ls);                                        \
        } while (0)
        WAIT_BAR(8);
        AT_S(sA, fp);
        for (int c = 0; c < 8; ++c) {
            if (c < 6) { WAIT_BAR(4); } else { WAIT_BAR(0); }
            if (c + 3 < 8) AT_LOAD(c + 3);
            if (c == 6 && tid == 0) *flag = 0;
            const lds_cptr p = fp + (c & 3) * AT_SLOT;
            const lds_cptr pn = fp + ((c + 1) & 3) * AT_SLOT;
#pragma unroll
            for (int t = 0; t < 8; t += 2) {
                AT_S(sB, p + (t + 1) * 4096);
                AT_PV(sA, p + t * 4096);
                if (t + 2 < 8) AT_S(sA, p + (t + 2) * 4096); else AT_S(sA, pn);
                AT_PV(sB, p + (t + 1) * 4096);
            }
        }
        const bool bad = !(ls[0] >= 8.6736174e-19f);
        if (attempt == 0 && __any(bad)) { if (lane == 0) *flag = 1; }
        LDS_BAR();
        const int redo = __builtin_amdgcn_readfirstlane(*flag);
        if (attempt == 1 || !redo) break;
        float mx = -INFINITY;
        const bf16x8* kp = (const bf16x8*)kvbase + lane;
        for (int t = 0; t < 64; ++t) {
            const bf16x8 k0 = kp[t * 256], k1 = kp[t * 256 + 64];
            f32x16 s = MFMA32(k0, q0, f32x16{}); s = MFMA32(k1, q1, s);
#pragma unroll
            for (int i = 0; i < 16; ++i) mx = fmaxf(mx, s[i]);
        }
        mx = fmaxf(mx, xor32(mx));
        mref = mx;
        LDS_BAR();
    }
    const float rl = 1.f / ls[0];
    u32x4* op = (u32x4*)((char*)Of + ((size_t)(qt * 32 + head) * 2) * 1024) + lane;
#pragma unroll
    for (int s = 0; s < 2; ++s) {
        u32x4 v;
#pragma unroll
        for (int j = 0; j < 4; ++j) v[j] = pk2(o[8 * s + 2 * j] * rl, o[8 * s + 2 * j + 1] * rl);
        op[s * 64] = v;
    }
    DIAG_END(3);
}

constexpr int OS = 24576, OV = 6 * OS, OST = OV + 3072, OBN = OST + 3072, OPT = OBN + 2048, OGS = OPT + 1024, O_LDS = OGS + 128;
#define OMAP 0
#define OROT 1
#define FF0 2
#define OSL3 16
#define OSLP 24
#define OSL2 0
#define OSPEC 1
#define OABL 0
constexpr int XOR7 = 0x141;
constexpr unsigned TAG = 0x5EED0000u;
constexpr int SPIN_MAX = 1 << 18;
DI float oct_sum(float v) { v += dpp<XOR1>(v); v += dpp<XOR2>(v); v += dpp<XOR7>(v); return v; }
#define GLD2C(dst, voff, sbase) asm volatile("global_load_dwordx2 %0, %1, %2 sc1" : "=v"(dst) : "v"(voff), "s"(sbase) : "memory")
#define GLD4C(dst, voff, sbase) asm volatile("global_load_dwordx4 %0, %1, %2 sc1" : "=v"(dst) : "v"(voff), "s"(sbase) : "memory")
#define GST2C(voff, val, sbase) asm volatile("global_store_dwordx2 %0, %1, %2 sc1" :: "v"(voff), "v"(val), "s"(sbase) : "memory")
#define GST4C(voff, val, sbase) asm volatile("global_store_dwordx4 %0, %1, %2 sc1\n\ts_nop 1" :: "v"(voff), "v"(val), "s"(sbase) : "memory")
DI float dot2(unsigned a, unsigned b, float c) { return __builtin_amdgcn_fdot2_f32_bf16(__builtin_bit_cast(bf16x2_t, a), __builtin_bit_cast(bf16x2_t, b), c, false); }
__global__ __launch_bounds__(768, 3) void k_out(const u16* __restrict__ Of, const u16* __restrict__ Wo, const u16* __restrict__ x,
                                                const float* __restrict__ mean1, const float* __restrict__ mod,
                                                const char* __restrict__ wt, const u16* __restrict__ wd, char* __restrict__ gran,
                                                u16* __restrict__ out DIAG_ARG) {
    DIAG_BEGIN
    __shared__ __attribute__((aligned(16))) char lds[O_LDS];
    const int b = blockIdx.x, tid = threadIdx.x, lane = tid & 63;
    const int w = __builtin_amdgcn_readfirstlane(tid >> 6);
    const int xcd = b & 7, idx = b >> 3;
    const int rp = (xcd >> 1) * 8 + (idx & 7), cp = (xcd & 1) * 4 + (idx >> 3);
    const int mt = w & 1, nt = w >> 1, blk = lane >> 4, hh = lane >> 5;
    const unsigned lds0 = (unsigned)(uintptr_t)lds;
    const lds_ptr L = (lds_ptr)lds;
    if (w >= 8) {
        const int lw = w - 8;
        const char* pa = (const char*)Of + (size_t)(rp * 2 + (lw >> 1)) * 65536 + (lw & 1) * 2048;
        const char* pb = (const char*)Wo + ((size_t)lw * 16 * 1024 + cp * 128) * 2;
        const unsigned aoff = lane * 16;
        const unsigned boff = (unsigned)(((lane >> 3) * 1024 + ((lane & 7) ^ ((lane >> 2) & 4)) * 8) * 2);
        unsigned dst = lds0;
        int si = OROT * ((idx & 7) * 2 + (idx >> 3) * 4) & 15;
#define O_STAGE() do { const char* a_ = pa + si * 4096; const char* b_ = pb + (size_t)si * 131072; const unsigned d_ = dst + 8192 + lw * 4096;    \
                       glds16(a_, aoff, dst + lw * 2048); glds16(a_ + 1024, aoff, dst + lw * 2048 + 1024);                                        \
                       glds16(b_, boff, d_); glds16(b_ + 128, boff, d_ + 1024); glds16(b_ + 16384, boff, d_ + 2048); glds16(b_ + 16384 + 128, boff, d_ + 3072);  \
                       si = (si + 1) & 15; dst += OS; if (dst >= lds0 + OV) dst = lds0; } while (0)
        if (lw < 3) glds16(mod + M_GA + lw * 2048 + cp * 128, (unsigned)(hh * 4096 + (lane & 31) * 16), lds0 + OV + lw * 1024);
#pragma unroll 1
        for (int s = 0; s < 3; ++s) O_STAGE();
        WAIT_BAR(12);
#pragma unroll 1
        for (int s = 0; s < 2; ++s) O_STAGE();
        WAIT_BAR(18);
        O_STAGE();
#pragma unroll 1
        for (int s = 1; s < 11; ++s) { O_STAGE(); if (s < 10) WAIT_BAR(24); }
        WAIT_BAR(24); WAIT_BAR(18); WAIT_BAR(12); WAIT_BAR(6); WAIT_BAR(0);
        asm volatile("s_barrier" ::: "memory");
        DIAG_T(0);
        {
            u32x2 v[6];
            const char* gp = gran + (size_t)rp * 12288 + lw * 3072;
#pragma unroll 1
            for (int spin = 0; spin < SPIN_MAX; ++spin) {
#pragma unroll
                for (int i = 0; i < 6; ++i) GLD2C(v[i], (unsigned)(lane * 8), gp + i * 512);
                asm volatile("s_waitcnt vmcnt(0)" : "+v"(v[0]), "+v"(v[1]), "+v"(v[2]), "+v"(v[3]), "+v"(v[4]), "+v"(v[5]) :: "memory");
                if (__all((v[0][1] & v[1][1] & v[2][1] & v[3][1] & v[4][1] & v[5][1]) == TAG)) break;
            }
            DIAG_T(1);
            LAS u32x2* p = (LAS u32x2*)(L + 2 * OS + lane * 112 + lw * 8);
            p[0] = u32x2{v[0][0], v[3][0]}; p[4] = u32x2{v[1][0], v[4][0]}; p[8] = u32x2{v[2][0], v[5][0]};
        }
        LDS_BAR();
        DIAG_T(2);
        if (lw == 0) {
            const char* g2 = gran + G_H2;
            u32x2 v; u32x4 hv[4];
            int cur = 63; bool have = false;
            hv[0] = u32x4{}; hv[1] = u32x4{}; hv[2] = u32x4{}; hv[3] = u32x4{};
            __builtin_amdgcn_s_sleep(OSLP);
#pragma unroll 1
            for (int spin = 0; spin < SPIN_MAX; ++spin) {
                GLD2C(v, (unsigned)((lane & 31) * 128), g2 + 32 * 4096 + (b & (NREP - 1)) * 4096);
                if (!have && cur < 32) {
#pragma unroll
                    for (int i = 0; i < 4; ++i) GLD4C(hv[i], (unsigned)(lane * 16), g2 + cur * 4096 + i * 1024);
                }
                asm volatile("s_waitcnt vmcnt(0)" : "+v"(v), "+v"(hv[0]), "+v"(hv[1]), "+v"(hv[2]), "+v"(hv[3]) :: "memory");
                const bool ok = (v[1] & 0xffff0000u) == TAG;
                const float r = ok ? __uint_as_float(v[0]) : -INFINITY;
                const float wbest = wmax(r);
                const int win = (int)__builtin_ctzll(__ballot(ok && r == wbest) | (1ull << 63));
                have = win == cur && __all((hv[0][1] & hv[0][3] & hv[1][1] & hv[1][3] & hv[2][1] & hv[2][3] & hv[3][1] & hv[3][3]) == TAG);
                cur = __builtin_popcountll(__ballot(ok)) >= 2 * OSPEC ? win : 63;
                if (have && __all(ok)) break;
                if (OSL2) __builtin_amdgcn_s_sleep(OSL2);
            }
            if (lane == cur) *(LAS u32x2*)(L + OGS + 64) = u32x2{v[0], (unsigned)(cur * 64) + (v[1] & 63)};
            LAS u32x2* p = (LAS u32x2*)(L + OBN + (lane >> 3) * 64 + (lane & 3) * 16 + ((lane >> 2) & 1) * 8);
#pragma unroll
            for (int i = 0; i < 4; ++i) p[i * 64] = u32x2{hv[i][0], hv[i][2]};
        }
        DIAG_T(6);
        LDS_BAR();
        return;
    }
    const int row = rp * 64 + mt * 32 + (lane & 31);
    const unsigned xoff = (unsigned)((row * D_ + cp * 128 + nt * 32 + 8 * hh) * 2);
    u32x4 xi[2]; float ref; f32x2 gws, sws, gbv;
    const int ycol = w * 128 + (lane & 1) * 63 + lane;
    GLD4(xi[0], xoff, x); GLD4(xi[1], xoff, x + 16);
    GLD1(ref, (unsigned)(row * 4), mean1);
    GLD2(gws, (unsigned)(tid * 8), mod + M_GW); GLD2(sws, (unsigned)(tid * 8), mod + M_SW); GLD2(gbv, (unsigned)(ycol * 4), mod + M_GB);
    u32x4 fr[12];
    const char* pw = (OABL & 1) ? wt : wt + (size_t)(b * 8 + w) * 8192;
    const char* pd = (OABL & 1) ? wt : (const char*)wd + (size_t)ffn_chunk(b < 32 ? b + 32 : b) * 32768 + w * 4096 - 8192;
    const lds_cptr ap = (lds_cptr)lds + mt * 4096 + lane * 16;
    const lds_cptr bp = (lds_cptr)lds + 8192 + (nt >> 1) * 1024 + hh * 512 + ((lane & 15) >> 2) * 128 + ((nt ^ (lane >> 3)) & 1) * 64 + (blk & 1) * 32 + (lane & 3) * 8;
    f32x16 acc = f32x16{};
    asm volatile("s_waitcnt vmcnt(0)" : "+v"(xi[0]), "+v"(xi[1]), "+v"(ref), "+v"(gws), "+v"(sws), "+v"(gbv) :: "memory");
    DIAG_T(0);
    {
        const float a = wsum(gws[0] + gws[1]), c = wsum(sws[0] + sws[1]);
        if (lane == 0) *(LAS f32x2*)(L + OGS + w * 8) = f32x2{a, c};
    }
    float d[16];
#pragma unroll
    for (int j = 0; j < 2; ++j)
#pragma unroll
        for (int e = 0; e < 2; ++e) {
            const auto r = __builtin_amdgcn_permlane32_swap(xi[j][e], xi[j][2 + e], false, false);
            d[8 * j + 2 * e] = bflo(r[0]) - ref; d[8 * j + 2 * e + 1] = bfhi(r[0]) - ref;
            d[8 * j + 4 + 2 * e] = bflo(r[1]) - ref; d[8 * j + 5 + 2 * e] = bfhi(r[1]) - ref;
        }
    bf16x8 fa[2][4], fb[2][4];
    f32x4 va[4], vb[4];
    const lds_cptr vp = (lds_cptr)lds + OV + (nt * 32 + 4 * hh) * 4;
#pragma unroll
    for (int s = 0; s < 17; ++s) {
        if (s < 16) {
            LDS_BAR();
            int so = (s % 6) * OS;
            asm volatile("" : "+s"(so));
#pragma unroll
            for (int ks = 0; ks < 4; ++ks) { fa[s & 1][ks] = ldsfrag(ap + so + ks * 1024); fb[s & 1][ks] = cat4(trread(bp + so + ks * 4096), trread(bp + so + ks * 4096 + 2048)); }
            if (s >= FF0 && s < FF0 + 12) GLD4(fr[s - FF0], (unsigned)((OABL & 2) ? 0 : lane * 16), (s - FF0 < 8 ? pw : pd) + (s - FF0) * 1024);
            SBAR();
        }
        if (s == 16) {
#pragma unroll
            for (int g = 0; g < 4; ++g) { va[g] = *(const LAS f32x4*)(vp + g * 32); vb[g] = *(const LAS f32x4*)(vp + 2048 + g * 32); }
            SBAR();
        }
        if (s > 0) {
#pragma unroll
            for (int ks = 0; ks < 4; ++ks) acc = MFMA32(fb[(s - 1) & 1][ks], fa[(s - 1) & 1][ks], acc);
            SBAR();
        }
    }
    DIAG_T(2);
    {
        float s1a = 0.f, s1b = 0.f, s2a = 0.f, s2b = 0.f, sda = 0.f, sdb = 0.f;
#pragma unroll
        for (int g = 0; g < 4; ++g)
#pragma unroll
            for (int e = 0; e < 4; e += 2) {
                const float v0 = __builtin_fmaf(va[g][e], acc[4 * g + e], d[4 * g + e]), v1 = __builtin_fmaf(va[g][e + 1], acc[4 * g + e + 1], d[4 * g + e + 1]);
                d[4 * g + e] = v0; d[4 * g + e + 1] = v1;
                s1a += v0; s1b += v1; s2a = __builtin_fmaf(v0, v0, s2a); s2b = __builtin_fmaf(v1, v1, s2b);
                sda = __builtin_fmaf(v0, vb[g][e], sda); sdb = __builtin_fmaf(v1, vb[g][e + 1], sdb);
            }
        const float q12 = sw32sum(s1a + s1b, s2a + s2b), sd = sda + sdb, q3 = sw32sum(sd, sd);
        const int o = OST + nt * 256 + (mt * 32 + (lane & 31)) * 4;
        *(LAS float*)(L + o + hh * 1024) = q12;
        *(LAS float*)(L + o + 2048) = q3;
    }
    asm volatile("s_waitcnt vmcnt(0)" : "+v"(fr[0]), "+v"(fr[1]), "+v"(fr[2]), "+v"(fr[3]), "+v"(fr[4]), "+v"(fr[5]), "+v"(fr[6]), "+v"(fr[7]), "+v"(fr[8]), "+v"(fr[9]), "+v"(fr[10]), "+v"(fr[11]) :: "memory");
    LDS_BAR();
    if (w < 3) {
        const LAS float* p = (const LAS float*)(L + OST + w * 1024) + lane;
        const float s = (p[0] + p[64]) + (p[128] + p[192]);
        const u32x2 v = {__float_as_uint(s), TAG};
        GST2C((unsigned)(cp * 1536 + w * 512 + lane * 8), v, gran + (size_t)rp * 12288);
    }
    DIAG_T(3);
    {
        LAS u32x4* p = (LAS u32x4*)(L + (w < 6 ? 4 * OS + w * 8192 : (w - 6) * 8192)) + lane;
#pragma unroll
        for (int j = 0; j < 8; ++j) p[j * 64] = fr[j];
        LAS u32x4* q = (LAS u32x4*)(L + 16384 + w * 4096) + lane;
#pragma unroll
        for (int j = 0; j < 4; ++j) q[j * 64] = fr[8 + j];
    }
    float gtot, ctot;
    {
        const f32x2 t = *(const LAS f32x2*)(L + OGS + (lane & 7) * 8);
        gtot = oct_sum(t[0]); ctot = oct_sum(t[1]);
    }
    f32x4 vc[4];
#pragma unroll
    for (int g = 0; g < 4; ++g) { va[g] = *(const LAS f32x4*)(vp + 512 + g * 32); vb[g] = *(const LAS f32x4*)(vp + 1024 + g * 32); vc[g] = *(const LAS f32x4*)(vp + 1536 + g * 32); }
    bf16x8 fw[12];
    {
        const lds_cptr p = (lds_cptr)lds + (w < 6 ? 4 * OS + w * 8192 : (w - 6) * 8192) + lane * 8;
#pragma unroll
        for (int j = 0; j < 8; ++j) fw[j] = cat4(trread(p + j * 1024), trread(p + j * 1024 + 512));
    }
    DIAG_T(4);
    LDS_BAR();
    DIAG_T(5);
    int brow; f32x2 fs;
    char* g2 = gran + G_H2;
    {
        const LAS f32x4* p = (const LAS f32x4*)(L + 2 * OS + lane * 112);
        const f32x4 a = p[0] + p[1], q = p[2] + p[3], c = p[4] + p[5];
        const float md = ((a[0] + a[1]) + (a[2] + a[3])) * (1.f / D_), s2 = ((q[0] + q[1]) + (q[2] + q[3])) * (1.f / D_), sd = (c[0] + c[1]) + (c[2] + c[3]);
        const float rstd = __builtin_amdgcn_rsqf(fmaxf(s2 - md * md, 0.f) + LN_EPS);
        float r = (sd - md * gtot) * rstd;
        r = r == r ? r : -INFINITY;
        const float best = wmax(r);
        brow = (int)__builtin_ctzll(__ballot(r == best) | (1ull << 63));
        if (w == 0 && cp == 0 && lane < NREP) { const u32x2 v = {__float_as_uint(best), TAG | (unsigned)brow}; GST2C((unsigned)(lane * 4096 + rp * 128), v, g2 + 32 * 4096); }
        const auto r0 = __builtin_amdgcn_permlane32_swap(__float_as_uint(rstd), __float_as_uint(rstd), false, false);
        const auto r1 = __builtin_amdgcn_permlane32_swap(__float_as_uint(-md * rstd), __float_as_uint(-md * rstd), false, false);
        fs = f32x2{__uint_as_float(mt ? r0[1] : r0[0]), __uint_as_float(mt ? r1[1] : r1[0])};
    }
    float h[16];
#pragma unroll
    for (int i = 0; i < 16; ++i) h[i] = __builtin_fmaf(__builtin_fmaf(d[i], fs[0], fs[1]), vb[i >> 2][i & 3], va[i >> 2][i & 3]);
    const bool mine = mt * 32 + (lane & 31) == brow;
    if ((brow >> 5) == mt) {
#pragma unroll
        for (int g = 0; g < 4; ++g) {
            const u32x4 v = {pk2(h[4 * g], h[4 * g + 1]), TAG, pk2(h[4 * g + 2], h[4 * g + 3]), TAG};
            if (mine) GST4C((unsigned)(rp * 4096 + (cp * 128 + nt * 32 + 8 * g + 4 * hh) * 4), v, g2);
        }
    }
    DIAG_T(6);
#pragma unroll
    for (int i = 0; i < 16; ++i) h[i] = __builtin_fmaf(vc[i >> 2][i & 3], h[i], d[i]) + ref;
    u32x4 o[2];
#pragma unroll
    for (int j = 0; j < 2; ++j) {
#pragma unroll
        for (int e = 0; e < 2; ++e) {
            const auto r = __builtin_amdgcn_permlane32_swap(pk2(h[8 * j + 2 * e], h[8 * j + 2 * e + 1]), pk2(h[8 * j + 4 + 2 * e], h[8 * j + 5 + 2 * e]), false, false);
            o[j][e] = r[0]; o[j][2 + e] = r[1];
        }
        if (!mine) *(u32x4*)((char*)(out + 16 * j) + xoff) = o[j];
    }
    {
        const lds_cptr q = (lds_cptr)lds + 16384 + ((lane & 15) >> 2) * 2048 + w * 256 + blk * 32 + (lane & 3) * 8;
#pragma unroll
        for (int j = 0; j < 4; ++j) fw[8 + j] = cat4(trread(q + (j & 1) * 16384 + (j >> 1) * 128), trread(q + (j & 1) * 16384 + (j >> 1) * 128 + 8192));
    }
    LDS_BAR();
    DIAG_T(7);
    const u32x2 wi = *(const LAS u32x2*)(L + OGS + 64);
    char* gy = gran + G_Y;
    if ((brow >> 5) == mt) {
        if ((int)(wi[1] >> 6) == rp) {
#pragma unroll
            for (int g = 0; g < 8; ++g) {
                const u32x4 v = {__float_as_uint(h[2 * g]), TAG, __float_as_uint(h[2 * g + 1]), TAG};
                if (mine) GST4C((unsigned)(224 * 8192 + (cp * 128 + nt * 32 + 8 * (g >> 1) + 4 * hh + 2 * (g & 1)) * 8), v, gy);
            }
        } else if (mine) {
            *(u32x4*)((char*)out + xoff) = o[0]; *(u32x4*)((char*)(out + 16) + xoff) = o[1];
        }
    }
    if (b >= 32) {
        f32x4 ag = f32x4{}, au = f32x4{};
#pragma unroll
        for (int q = 0; q < 4; ++q) {
            const bf16x8 a = ldsfrag((lds_cptr)lds + OBN + w * 256 + q * 64 + blk * 16);
            ag = MFMA16(a, fw[q], ag); au = MFMA16(a, fw[4 + q], au);
        }
        if (lane < 32) *(LAS float*)(L + OPT + (lane & 31) * 32 + w * 4) = lane < 16 ? ag[0] : au[0];
        const float sig = __builtin_amdgcn_rcpf(1.f + __expf(-(__uint_as_float(wi[0]) + ctot)));
        LDS_BAR();
        const LAS f32x4* pp = (const LAS f32x4*)(L + OPT + (lane & 15) * 32);
        const f32x4 a0 = pp[0] + pp[1], u0 = pp[32] + pp[33];
        const float sg = (a0[0] + a0[1]) + (a0[2] + a0[3]), su = (u0[0] + u0[1]) + (u0[2] + u0[3]);
        const float act = sg * su * __builtin_amdgcn_rcpf(1.f + __expf(-sg));
        const unsigned pr = pk2(act, dpp<XOR1>(act));
        float y0 = 0.f, y1 = 0.f, y2 = 0.f, y3 = 0.f;
#pragma unroll
        for (int i = 0; i < 8; i += 2) {
            const unsigned s0 = __builtin_amdgcn_readlane(pr, 2 * i), s1 = __builtin_amdgcn_readlane(pr, 2 * i + 2);
            const u32x4 w0 = __builtin_bit_cast(u32x4, fw[8 + (i >> 2)]), w1 = __builtin_bit_cast(u32x4, fw[10 + (i >> 2)]);
            y0 = dot2(s0, w0[i & 3], y0); y1 = dot2(s0, w1[i & 3], y1); y2 = dot2(s1, w0[(i & 3) + 1], y2); y3 = dot2(s1, w1[(i & 3) + 1], y3);
        }
        y0 += y2; y1 += y3;
        const float t = dpp<XOR1>((lane & 1) ? y0 : y1);
        const u32x4 v = {__float_as_uint(((lane & 1) ? t : y0) * (gbv[0] * sig)), TAG, __float_as_uint(((lane & 1) ? y1 : t) * (gbv[1] * sig)), TAG};
        GST4C((unsigned)((b - 32) * 8192 + ycol * 8), v, gy);
    } else __builtin_amdgcn_s_sleep(OSL3 + 10);
    DIAG_T(1);
    {
        const int cg = (b & 7) * 32 + (b >> 3), r = w * 32 + (lane >> 1);
        u32x4 v;
        __builtin_amdgcn_s_sleep(OSL3);
#pragma unroll 1
        for (int spin = 0; spin < SPIN_MAX; ++spin) {
            GLD4C(v, (unsigned)((r < 224 ? r : 224) * 8192 + (lane & 1) * 16), gy + cg * 32);
            asm volatile("s_waitcnt vmcnt(0)" : "+v"(v) :: "memory");
            if (__all((v[1] & v[3]) == TAG)) break;
        }
        float a = r > 224 ? 0.f : __uint_as_float(v[0]), c = r > 224 ? 0.f : __uint_as_float(v[2]);
        a = sw32sum(a, c);
        a = sw16sum(a, a); a += dpp<XOR2>(a); a += dpp<ROR4>(a); a += dpp<ROR8>(a);
        if ((lane & 30) == 0) *(LAS float*)(L + OST + w * 16 + (lane & 1) * 8 + hh * 4) = a;
        LDS_BAR();
        if (tid < 8) {
            f32x4 q = *(const LAS f32x4*)(L + OST + lane * 16);
#pragma unroll
            for (int e = 0; e < 4; ++e) q[e] = oct_sum(q[e]);
            if (tid == 0) *(u32x2*)(out + (size_t)(wi[1] & (S_ - 1)) * D_ + cg * 4) = u32x2{pk2(q[0], q[1]), pk2(q[2], q[3])};
        }
    }
    DIAG_END(4);
}

extern "C" void kernel_launch(void* const* d_in, const int* in_sizes, int n_in, void* d_out, int out_size, void* d_ws, size_t ws_size,
                              hipStream_t stream) {
    const u16* x = (const u16*)d_in[0];
    const u16* wqkv = (const u16*)d_in[1];
    const u16* wo = (const u16*)d_in[2];
    const u16* wr = (const u16*)d_in[4];
    const u16* wg = (const u16*)d_in[5];
    const u16* wu = (const u16*)d_in[6];
    const u16* wd = (const u16*)d_in[7];
    const u16* c = (const u16*)d_in[8];
    const u16* wmod = (const u16*)d_in[9];
    char* ws = (char*)d_ws;
    float* mod = (float*)(ws + WS_MOD);
    char* gran = ws + WS_GRAN;
    float* kn2 = (float*)(ws + WS_KN2);
    f32x2* stat = (f32x2*)(ws + WS_STAT);
    float* mean1 = (float*)(ws + WS_MEAN);
    u16* Qf = (u16*)(ws + WS_QF);
    u16* KVf = (u16*)(ws + WS_KVF);
    u16* Of = (u16*)(ws + WS_OF);
    u16* out = (u16*)d_out;
    u16* scb = (u16*)(ws + WS_SC);
    char* wt = ws + WS_WT;
    k_mod<<<256, 512, 0, stream>>>(c, wmod, scb, x, mod, mean1, stat DIAG_PASS);
    k_qkv<<<256, 768, 0, stream>>>(x, stat, mod, wqkv, Qf, KVf, kn2 DIAG_PASS);
    k_attn<<<256, 768, 0, stream>>>(Qf, KVf, kn2, Of, wg, wu, wd, wt, wmod, scb, wr, mod, wo, gran DIAG_PASS);
    k_out<<<256, 768, 0, stream>>>(Of, wo, x, mean1, mod, wt, wd, gran, out DIAG_PASS);
}
```

```cpp
#include <hip/hip_runtime.h>
#include <hip/hip_bf16.h>
#include <stdint.h>

typedef __attribute__((ext_vector_type(8))) short bf16x8;
typedef __attribute__((ext_vector_type(4))) short s16x4;
typedef __attribute__((ext_vector_type(16))) float f32x16;
typedef __attribute__((ext_vector_type(4))) float f32x4;
typedef __attribute__((ext_vector_type(2))) float f32x2;
typedef __attribute__((ext_vector_type(4))) unsigned u32x4;
typedef __attribute__((ext_vector_type(2))) unsigned u32x2;
typedef __bf16 bf16x2_t __attribute__((ext_vector_type(2)));
typedef unsigned short u16;
typedef __attribute__((address_space(3))) const char* lds_cptr;
typedef __attribute__((address_space(3))) char* lds_ptr;

#define DI __device__ __forceinline__
#define MFMA32(a, b, c) __builtin_amdgcn_mfma_f32_32x32x16_bf16((a), (b), (c), 0, 0, 0)
#define MFMA16(a, b, c) __builtin_amdgcn_mfma_f32_16x16x32_bf16((a), (b), (c), 0, 0, 0)
#define SBAR() __builtin_amdgcn_sched_barrier(0)

constexpr int S_ = 2048, D_ = 1024, H_ = 32, F_ = 3584;
constexpr float LN_EPS = 1e-5f;
constexpr float QSCALE = 0.17677669529663687f * 1.4426950408889634f;

constexpr size_t WS_MOD = 0;
constexpr size_t WS_KN2 = 32768;
constexpr size_t WS_SC = 65536;
constexpr size_t WS_STAT = 98304;
constexpr size_t WS_MEAN = 114688;
constexpr size_t WS_GRAN = 131072;
#define NREP 8
constexpr int G_H2 = 32 * 12288, G_BEST = G_H2 + 32 * 4096, G_Y = G_BEST + NREP * 4096, G_END = G_Y + 225 * 8192;
constexpr size_t WS_MODP = 5u << 20;
constexpr size_t WS_GM = 4u << 20;
constexpr unsigned TAG = 0x5EED0000u;
constexpr int SPIN_MAX = 1 << 18;
constexpr size_t WS_QF = 6u << 20;
constexpr size_t WS_KVF = WS_QF + (4u << 20);
constexpr size_t WS_OF = WS_KVF + (8u << 20);
constexpr size_t WS_WT = WS_OF + (4u << 20);

#define DIAG_ARG
#define DIAG_BEGIN
#define DIAG_T(i)
#define DIAG_END(kid)
#define DIAG_PASS
DI float bflo(unsigned u) { return __uint_as_float(u << 16); }
DI float bfhi(unsigned u) { return __uint_as_float(u & 0xffff0000u); }
DI unsigned pk2(float lo, float hi) {
    f32x2 v = {lo, hi};
    return __builtin_bit_cast(unsigned, __builtin_convertvector(v, bf16x2_t));
}
DI float rbf(float x) { return bflo(pk2(x, x) & 0xffffu); }
DI float bf2f(u16 v) { return __uint_as_float((unsigned)v << 16); }
DI s16x4 trread(lds_cptr p) {
    typedef short v4i16_t __attribute__((ext_vector_type(4)));
    return __builtin_bit_cast(s16x4, __builtin_amdgcn_ds_read_tr16_b64_v4i16((__attribute__((address_space(3))) v4i16_t*)p));
}
DI bf16x8 ldsfrag(lds_cptr p) { return *(const __attribute__((address_space(3))) bf16x8*)p; }
DI bf16x8 cat4(s16x4 a, s16x4 b) { return __builtin_shufflevector(a, b, 0, 1, 2, 3, 4, 5, 6, 7); }
DI void glds16(const void* sbase, unsigned voff, unsigned lds_base) {
    unsigned sv;
    asm volatile("s_mov_b32 %0, m0\n\ts_mov_b32 m0, %3\n\ts_nop 0\n\tglobal_load_lds_dwordx4 %1, %2\n\ts_mov_b32 m0, %0"
                 : "=&s"(sv) : "v"(voff), "s"(sbase), "s"(lds_base) : "memory");
}
#define GLD4(dst, voff, sbase) asm volatile("global_load_dwordx4 %0, %1, %2" : "=v"(dst) : "v"(voff), "s"(sbase) : "memory")
#define GLD2(dst, voff, sbase) asm volatile("global_load_dwordx2 %0, %1, %2" : "=v"(dst) : "v"(voff), "s"(sbase) : "memory")
#define GLD1(dst, voff, sbase) asm volatile("global_load_dword %0, %1, %2" : "=v"(dst) : "v"(voff), "s"(sbase) : "memory")
#define GLDH(dst, voff, sbase) asm volatile("global_load_ushort %0, %1, %2" : "=v"(dst) : "v"(voff), "s"(sbase) : "memory")
#define GLD2C(dst, voff, sbase) asm volatile("global_load_dwordx2 %0, %1, %2 sc1" : "=v"(dst) : "v"(voff), "s"(sbase) : "memory")
#define GLD4C(dst, voff, sbase) asm volatile("global_load_dwordx4 %0, %1, %2 sc1" : "=v"(dst) : "v"(voff), "s"(sbase) : "memory")
#define GST2C(voff, val, sbase) asm volatile("global_store_dwordx2 %0, %1, %2 sc1" :: "v"(voff), "v"(val), "s"(sbase) : "memory")
#define GST4C(voff, val, sbase) asm volatile("global_store_dwordx4 %0, %1, %2 sc1\n\ts_nop 1" :: "v"(voff), "v"(val), "s"(sbase) : "memory")
#define WAIT_VM(n) asm volatile("s_waitcnt vmcnt(" #n ")" ::: "memory")
#define WAIT_BAR(n) asm volatile("s_waitcnt vmcnt(" #n ") lgkmcnt(0)\n\ts_barrier" ::: "memory")
#define LDS_BAR() asm volatile("s_waitcnt lgkmcnt(0)\n\ts_barrier" ::: "memory")
DI float xor32(float v) {
    auto r = __builtin_amdgcn_permlane32_swap(__float_as_uint(v), __float_as_uint(v), false, false);
    return (threadIdx.x & 32) ? __uint_as_float(r[0]) : __uint_as_float(r[1]);
}
DI float wave_sum(float v) {
#pragma unroll
    for (int o = 1; o < 64; o <<= 1) v += __shfl_xor(v, o);
    return v;
}
DI float wave_max(float v) {
#pragma unroll
    for (int o = 1; o < 64; o <<= 1) v = fmaxf(v, __shfl_xor(v, o));
    return v;
}

template <int CTRL> DI float dpp(float x) { return __builtin_bit_cast(float, __builtin_amdgcn_mov_dpp(__builtin_bit_cast(int, x), CTRL, 0xf, 0xf, true)); }
constexpr int XOR1 = 0xB1, XOR2 = 0x4E, ROR4 = 0x124, ROR8 = 0x128;
DI float sw32sum(float a, float b) {
    auto r = __builtin_amdgcn_permlane32_swap(__float_as_uint(a), __float_as_uint(b), false, false);
    return __uint_as_float(r[0]) + __uint_as_float(r[1]);
}
DI float sw16sum(float a, float b) {
    auto r = __builtin_amdgcn_permlane16_swap(__float_as_uint(a), __float_as_uint(b), false, false);
    return __uint_as_float(r[0]) + __uint_as_float(r[1]);
}
DI float row_sum16(float v) { v += dpp<XOR1>(v); v += dpp<XOR2>(v); v += dpp<ROR4>(v); v += dpp<ROR8>(v); return v; }
DI float row_max16(float v) { v = fmaxf(v, dpp<XOR1>(v)); v = fmaxf(v, dpp<XOR2>(v)); v = fmaxf(v, dpp<ROR4>(v)); v = fmaxf(v, dpp<ROR8>(v)); return v; }
DI float wsum(float v) { v = row_sum16(v); v = sw16sum(v, v); return sw32sum(v, v); }
DI float wmax(float m) {
    m = row_max16(m);
    { auto s_ = __builtin_amdgcn_permlane16_swap(__float_as_uint(m), __float_as_uint(m), false, false); m = fmaxf(__uint_as_float(s_[0]), __uint_as_float(s_[1])); }
    { auto s_ = __builtin_amdgcn_permlane32_swap(__float_as_uint(m), __float_as_uint(m), false, false); m = fmaxf(__uint_as_float(s_[0]), __uint_as_float(s_[1])); }
    return m;
}
DI float sfma(float a, float b, float c) { float r; asm("v_fma_f32 %0, %1, %2, %3" : "=v"(r) : "v"(a), "v"(b), "v"(c)); return r; }
#define LAS __attribute__((address_space(3)))
constexpr int M_SA = 0, M_G1 = 1024, M_GA = 2048, M_SB = 3072, M_G2 = 4096, M_GB = 5120, M_GW = 6144, M_SW = 7168;

#define MPF 1
__global__ __launch_bounds__(512) void k_mod(const u16* __restrict__ c, const u16* __restrict__ wmod, u16* __restrict__ scb,
                                             const u16* __restrict__ x, float* __restrict__ modp, float* __restrict__ mean1,
                                             f32x2* __restrict__ stat, char* __restrict__ gm, const u16* __restrict__ W DIAG_ARG) {
    DIAG_BEGIN
    __shared__ __attribute__((aligned(16))) float red[8][32];
    const int t = threadIdx.x, lane = t & 63, b = blockIdx.x;
    const int w = __builtin_amdgcn_readfirstlane(t >> 6);
    const int xcd = b & 7, i = b >> 3;
    const int cg = (xcd * 4 + (i >> 3)) * 2 + (i & 1), ks = (i >> 1) & 3;
    const int rg = ks * 256 + (t >> 2), row = b * 8 + w;
    u32x4 wv[2], v[2], pf[3]; unsigned cv[2];
    GLDH(cv[0], (unsigned)(rg * 2), c); GLDH(cv[1], (unsigned)(rg * 2), c + 128);
    GLD4(wv[0], (unsigned)((rg * 6144 + cg * 32 + (t & 3) * 8) * 2), wmod); GLD4(wv[1], (unsigned)((rg * 6144 + cg * 32 + (t & 3) * 8) * 2), wmod + 128 * 6144);
    GLD4(v[0], (unsigned)(lane * 16), x + (size_t)row * D_); GLD4(v[1], (unsigned)(lane * 16), x + (size_t)row * D_ + 512);
    {
        const int r2 = (i * 8 + w) * 2 + (lane >> 5), k = (r2 >> 5) * 64 + (xcd & 1) * 32 + (r2 & 31);
        const unsigned po = (unsigned)(k * 6144 + (xcd >> 1) * 512 + (lane & 31) * 16);
        if (MPF) { GLD4(pf[0], po, W); GLD4(pf[1], po, W + 1024); GLD4(pf[2], po, W + 2048); }
        else { GLD4(pf[0], 0u, W); GLD4(pf[1], 0u, W); GLD4(pf[2], 0u, W); }
    }
    if (t < 32) ((u32x4*)gm)[b * 32 + t] = u32x4{};
    asm volatile("s_waitcnt vmcnt(7)" : "+v"(cv[0]), "+v"(cv[1]) :: "memory");
    float sc[2];
#pragma unroll
    for (int j = 0; j < 2; ++j) { const float cf = bflo(cv[j]); sc[j] = rbf(cf / (1.f + __expf(-cf))); }
    if (cg == 0 && (t & 3) == 0) {
#pragma unroll
        for (int j = 0; j < 2; ++j) scb[128 * j + (rg & ~31) + ((rg >> 2) & 3) * 8 + ((rg >> 4) & 1) * 4 + (rg & 3)] = (u16)(__float_as_uint(sc[j]) >> 16);
    }
    asm volatile("s_waitcnt vmcnt(5)" : "+v"(wv[0]), "+v"(wv[1]) :: "memory");
    DIAG_T(0);
    {
        float acc[8];
#pragma unroll
        for (int j = 0; j < 4; ++j) { acc[2 * j] = sc[0] * bflo(wv[0][j]) + sc[1] * bflo(wv[1][j]); acc[2 * j + 1] = sc[0] * bfhi(wv[0][j]) + sc[1] * bfhi(wv[1][j]); }
#pragma unroll
        for (int j = 0; j < 4; ++j) acc[j] = sw32sum(acc[j], acc[4 + j]);
        f32x2 z;
#pragma unroll
        for (int j = 0; j < 2; ++j) {
            float q = sw16sum(acc[j], acc[2 + j]);
            q += dpp<ROR4>(q); q += dpp<ROR8>(q);
            z[j] = q;
        }
        if ((lane & 12) == 0) *(f32x2*)&red[w][(lane & 3) * 8 + (lane >> 5) * 4 + ((lane >> 4) & 1) * 2] = z;
    }
    LDS_BAR();
    if (t < 32) modp[ks * 2048 + cg * 32 + t] = ((red[0][t] + red[1][t]) + (red[2][t] + red[3][t])) + ((red[4][t] + red[5][t]) + (red[6][t] + red[7][t]));
    asm volatile("s_waitcnt vmcnt(3)" : "+v"(v[0]), "+v"(v[1]) :: "memory");
    DIAG_T(1);
    {
        float f[16], s = 0.f;
#pragma unroll
        for (int j = 0; j < 8; ++j) { f[2 * j] = bflo(v[j >> 2][j & 3]); f[2 * j + 1] = bfhi(v[j >> 2][j & 3]); s += f[2 * j] + f[2 * j + 1]; }
        const float mean = wsum(s) * (1.f / D_);
        float q = 0.f;
#pragma unroll
        for (int j = 0; j < 16; ++j) { const float d = f[j] - mean; q += d * d; }
        const float rstd = rsqrtf(wsum(q) * (1.f / D_) + LN_EPS);
        if (lane == 0) { stat[row] = f32x2{rstd, -mean * rstd}; mean1[row] = mean; }
    }
    DIAG_T(2);
    asm volatile("s_waitcnt vmcnt(0)" : "+v"(pf[0]), "+v"(pf[1]), "+v"(pf[2]) :: "memory");
    DIAG_END(0);
}

#define QSC1 "sc1"
#define QABL 0
constexpr int QA_SLOT = 8576, QB_SLOT = 12288, QB_OFF = 5 * QA_SLOT, QG_OFF = QB_OFF + 9 * QB_SLOT, QK_LDS = QG_OFF + 8192;
__global__ __launch_bounds__(768, 3) void k_qkv(const u16* __restrict__ x, const f32x2* __restrict__ stat, const float* __restrict__ mod,
                                                const u16* __restrict__ W, u16* __restrict__ Qf, u16* __restrict__ KVf,
                                                float* __restrict__ kn2 DIAG_ARG) {
    DIAG_BEGIN
    __shared__ __attribute__((aligned(16))) char lds[QK_LDS];
    const int b = blockIdx.x, tid = threadIdx.x, lane = tid & 63;
    const int w = __builtin_amdgcn_readfirstlane(tid >> 6);
    const int xcd = b & 7, idx = b >> 3;
    const int rp = (xcd & 1) * 8 + (idx & 7), cp = (xcd >> 1) * 4 + (idx >> 3);
    const unsigned lds0 = (unsigned)(uintptr_t)lds;
    if (w >= 8) {
        const int lw = w - 8;
        const unsigned boff = (unsigned)(((lw * 8 + (lane >> 3)) * 3072 + cp * 64 + ((lane & 7) ^ ((lane >> 2) & 4)) * 8) * 2);
        const char* src = (const char*)W;
        unsigned dst = lds0 + QB_OFF + lw * 1024;
        const unsigned dend = lds0 + QB_OFF + 9 * QB_SLOT;
#define QB_UNIT() do { if (!(QABL & 4)) { glds16(src, boff, dst); glds16(src + 2048, boff, dst + 4096); glds16(src + 4096, boff, dst + 8192); }  \
                       src += 32 * 3072 * 2; dst += QB_SLOT; if (dst >= dend) dst -= 9 * QB_SLOT; } while (0)
#pragma unroll 1
        for (int u = 0; u < 6; ++u) QB_UNIT();
        asm volatile("s_barrier" ::: "memory");
#pragma unroll 1
        for (int j = 0; j < 13; ++j) { WAIT_BAR(12); QB_UNIT(); QB_UNIT(); }
        WAIT_BAR(12); WAIT_BAR(6); WAIT_BAR(0);
    } else if (w >= 4) {
        const int p = tid - 256, c = p & 7, r = p >> 3;
        const unsigned xoff = (unsigned)(((rp * 128 + r) * D_ + c * 8) * 2);
        u32x4 xa[4], xb[4]; f32x2 st[4];
#define QX_LOAD(dst, j) do { _Pragma("unroll") for (int i = 0; i < 4; ++i) GLD4(dst[i], xoff, x + (size_t)i * 32 * D_ + (j) * 64); } while (0)
#pragma unroll
        for (int i = 0; i < 4; ++i) GLD2(st[i], (unsigned)((rp * 128 + r) * 8), stat + 32 * i);
        QX_LOAD(xa, 0); QX_LOAD(xb, 1);
        const lds_cptr gp = (lds_cptr)lds + QG_OFF + c * 32;
        const unsigned wbase = (c & 3) * 528 + r * 16 + (c >> 2) * 64;
        int slot = c >> 2;
        asm volatile("s_waitcnt vmcnt(8)" : "+v"(st[0]), "+v"(st[1]), "+v"(st[2]), "+v"(st[3]) :: "memory");
        LDS_BAR();
#define QA_STEP(xs, j) do {                                                                                       \
            const lds_cptr g_ = gp + (j) * 256;                                                                   \
            const f32x4 s0 = *(const LAS f32x4*)g_, s1 = *(const LAS f32x4*)(g_ + 16);                            \
            const f32x4 g0 = *(const LAS f32x4*)(g_ + 4096), g1 = *(const LAS f32x4*)(g_ + 4096 + 16);            \
            asm volatile("s_waitcnt vmcnt(4)" : "+v"(xs[0]), "+v"(xs[1]), "+v"(xs[2]), "+v"(xs[3]) :: "memory");  \
            const lds_ptr d_ = (lds_ptr)lds + slot * QA_SLOT + wbase;                                             \
            _Pragma("unroll") for (int i = 0; i < 4; ++i) {                                                       \
                u32x4 o;                                                                                          \
                _Pragma("unroll") for (int e = 0; e < 4; ++e) {                                                   \
                    const float gl = e < 2 ? g0[2 * e] : g1[2 * e - 4], gh = e < 2 ? g0[2 * e + 1] : g1[2 * e - 3];   \
                    const float sl = e < 2 ? s0[2 * e] : s1[2 * e - 4], sh = e < 2 ? s0[2 * e + 1] : s1[2 * e - 3];   \
                    o[e] = pk2(sfma(sfma(bflo(xs[i][e]), st[i][0], st[i][1]), gl, sl), sfma(sfma(bfhi(xs[i][e]), st[i][0], st[i][1]), gh, sh)); \
                }                                                                                                 \
                *(LAS u32x4*)(d_ + i * 2112) = (QABL & 1) ? xs[i] : o;                                                                 \
            }                                                                                                     \
            QX_LOAD(xs, ((j) + 2) & 15);                                   \
            slot += 2; if (slot >= 5) slot -= 5;                                                                  \
            LDS_BAR();                                                                                            \
        } while (0)
#pragma unroll 1
        for (int j = 0; j < 16; j += 2) { QA_STEP(xa, j); QA_STEP(xb, j + 1); }
        asm volatile("s_waitcnt vmcnt(0)" : "+v"(xa[0]), "+v"(xa[1]), "+v"(xa[2]), "+v"(xa[3]), "+v"(xb[0]), "+v"(xb[1]), "+v"(xb[2]), "+v"(xb[3]) :: "memory");
    } else {
        const int rh = w & 1, hd = w >> 1, G = lane >> 4, hh = lane >> 5;
        const lds_cptr ap = (lds_cptr)lds + rh * 4224 + hh * 528 + (lane & 31) * 16;
        const lds_cptr bp = (lds_cptr)lds + QB_OFF + hh * 1024 + ((lane & 15) >> 2) * 128 + ((hd ^ (lane >> 3)) & 1) * 64 + (G & 1) * 32 + (lane & 3) * 8;
        f32x16 acc[2][3];
#pragma unroll
        for (int i = 0; i < 2; ++i)
#pragma unroll
            for (int j = 0; j < 3; ++j) acc[i][j] = f32x16{};
        bf16x8 a0[2], b0[3], a1[2], b1[3];
#define QC_READ(af, bf, a_, b_, s) do {                                                                           \
            af[0] = ldsfrag((a_) + (s) * 1056); af[1] = ldsfrag((a_) + (s) * 1056 + 2112);                        \
            _Pragma("unroll") for (int j = 0; j < 3; ++j) bf[j] = cat4(trread((b_) + j * 4096 + (s) * 2048), trread((b_) + j * 4096 + (s) * 2048 + 512)); \
        } while (0)
#define QC_RB(bf, j, b_, s) do { if (!(QABL & 2)) bf[j] = cat4(trread((b_) + (j) * 4096 + (s) * 2048), trread((b_) + (j) * 4096 + (s) * 2048 + 512)); } while (0)
#define QC_STEP(af, bf, an, bn, a_, b_, s) do {                                                                   \
            acc[0][0] = MFMA32(bf[0], af[0], acc[0][0]); SBAR();                                                  \
            if (!(QABL & 2)) { an[0] = ldsfrag((a_) + (s) * 1056); an[1] = ldsfrag((a_) + (s) * 1056 + 2112); } SBAR();   \
            acc[0][1] = MFMA32(bf[1], af[0], acc[0][1]); SBAR();                                                  \
            QC_RB(bn, 0, b_, s); SBAR();                                                                          \
            acc[0][2] = MFMA32(af[0], bf[2], acc[0][2]); SBAR();                                                  \
            QC_RB(bn, 1, b_, s); SBAR();                                                                          \
            acc[1][0] = MFMA32(bf[0], af[1], acc[1][0]); SBAR();                                                  \
            QC_RB(bn, 2, b_, s); SBAR();                                                                          \
            acc[1][1] = MFMA32(bf[1], af[1], acc[1][1]);                                                          \
            acc[1][2] = MFMA32(af[1], bf[2], acc[1][2]); SBAR();                                                  \
        } while (0)
        {
            f32x4 p[8];
#pragma unroll
            for (int j = 0; j < 8; ++j) GLD4(p[j], (unsigned)(tid * 32), mod + (j >> 1) * 2048 + (j & 1) * 4);
            asm volatile("s_waitcnt vmcnt(0)" : "+v"(p[0]), "+v"(p[1]), "+v"(p[2]), "+v"(p[3]), "+v"(p[4]), "+v"(p[5]), "+v"(p[6]), "+v"(p[7]) :: "memory");
#pragma unroll
            for (int j = 0; j < 2; ++j) {
                f32x4 q = (p[j] + p[2 + j]) + (p[4 + j] + p[6 + j]);
#pragma unroll
                for (int e = 0; e < 4; ++e) { q[e] = rbf(q[e]); if (w >= 2) q[e] = rbf(1.f + q[e]); }
                *(LAS f32x4*)((lds_ptr)lds + QG_OFF + tid * 32 + j * 16) = q;
            }
        }
        LDS_BAR();
        asm volatile("s_barrier" ::: "memory");
        DIAG_T(0);
        int as = 0, bs = 0;
        lds_cptr a_ = ap, b_ = bp;
        QC_READ(a0, b0, a_, b_, 0);
        if (QABL & 2) QC_READ(a1, b1, a_, b_, 1);
        SBAR(); __builtin_amdgcn_s_waitcnt(0xC07F); SBAR();
#define QC_NEXT(odd) do { as += QA_SLOT; if (as >= 5 * QA_SLOT) as = 0; bs += QB_SLOT; if (bs >= 9 * QB_SLOT) bs = 0;   \
                          a_ = ap + as + (odd) * 64; b_ = bp + bs; } while (0)
#pragma unroll 1
        for (int j = 0; j < 16; ++j) {
            QC_STEP(a0, b0, a1, b1, a_, b_, 1);
            QC_NEXT(1);
            QC_STEP(a1, b1, a0, b0, a_, b_, 0);
            if (j < 15) asm volatile("s_barrier" ::: "memory");
            QC_STEP(a0, b0, a1, b1, a_, b_, 1);
            QC_NEXT(0);
            QC_STEP(a1, b1, a0, b0, a_, b_, 0);
        }
        DIAG_T(1);
        const int head = cp * 2 + hd;
#pragma unroll
        for (int i = 0; i < 2; ++i) {
            const int tile = rp * 4 + rh * 2 + i;
            u32x4* qo = (u32x4*)((char*)Qf + ((size_t)(head * 64 + tile) * 2) * 1024) + lane;
            u32x4* kvo = (u32x4*)((char*)KVf + ((size_t)(head * 64 + tile) * 4) * 1024) + lane;
            float n2a = 0.f, n2b = 0.f;
#pragma unroll
            for (int s = 0; s < 2; ++s) {
                u32x4 q, k, v;
#pragma unroll
                for (int j = 0; j < 4; ++j) {
                    const float k0 = acc[i][1][8 * s + 2 * j], k1 = acc[i][1][8 * s + 2 * j + 1];
                    q[j] = pk2(acc[i][0][8 * s + 2 * j] * QSCALE, acc[i][0][8 * s + 2 * j + 1] * QSCALE);
                    k[j] = pk2(k0, k1);
                    v[j] = pk2(acc[i][2][8 * s + 2 * j], acc[i][2][8 * s + 2 * j + 1]);
                    n2a += k0 * k0; n2b += k1 * k1;
                }
                {
                    const char* qb = (const char*)Qf + ((size_t)(head * 64 + tile) * 2 + s) * 1024;
                    const char* kb = (const char*)KVf + ((size_t)(head * 64 + tile) * 4 + s) * 1024;
                    asm volatile("global_store_dwordx4 %0, %1, %2 " QSC1 "\n\ts_nop 1" :: "v"(lane * 16), "v"(q), "s"(qb) : "memory");
                    asm volatile("global_store_dwordx4 %0, %1, %2 " QSC1 "\n\ts_nop 1" :: "v"(lane * 16), "v"(k), "s"(kb) : "memory");
                    asm volatile("global_store_dwordx4 %0, %1, %2 offset:2048 " QSC1 "\n\ts_nop 1" :: "v"(lane * 16), "v"(v), "s"(kb) : "memory");
                }
            }
            float n2 = n2a + n2b;
            n2 = wmax(sw32sum(n2, n2));
            if (lane == 0) kn2[head * 64 + tile] = n2;
        }
    }
    DIAG_END(2);
}

constexpr int AT_SLOT = 32768, AT_STG = 4 * AT_SLOT, AT_RED = AT_STG + 4 * 4224, AT_DUM = AT_RED + 1024, AT_CNT = AT_DUM + 1024, AT_LDS = AT_CNT + 64;
#define HABL 0
#define HSL0 16
#define HP 8
#define ABAL 0
#define HCUT 9
#define HW0 3
#define HP4 4
#define LP 3
#define HPRIO 0
#define LPRIO 3
#define GST4W GST4C
typedef volatile LAS unsigned* lds_cnt;
DI unsigned cnt_wait(lds_cnt p, unsigned target) {
    unsigned v;
    do { v = *p; } while (__any((v & 0xffffu) < target));
    asm volatile("" ::: "memory");
    return v;
}
__global__ __launch_bounds__(832) void k_attn(const u16* __restrict__ Qf, const u16* __restrict__ KVf, const float* __restrict__ kn2,
                                                 u16* __restrict__ Of, const u16* __restrict__ wg, const u16* __restrict__ wu,
                                                 const u16* __restrict__ wd, char* __restrict__ wt, const u16* __restrict__ wmod,
                                                 const u16* __restrict__ scb, const u16* __restrict__ wr, float* __restrict__ mod,
                                                 const u16* __restrict__ Wo, char* __restrict__ gran, char* __restrict__ gm DIAG_ARG) {
    DIAG_BEGIN
    __shared__ __attribute__((aligned(16))) char lds[AT_LDS];
    const int b = blockIdx.x, tid = threadIdx.x, lane = tid & 63;
    const int w = __builtin_amdgcn_readfirstlane(tid >> 6);
    const unsigned lds0 = (unsigned)(uintptr_t)lds;
    const lds_cnt cnt = (lds_cnt)((lds_ptr)lds + AT_CNT);
    const int head = (b & 7) * 4 + ((b >> 3) & 3);
    const char* kvbase = (const char*)KVf + (size_t)head * 262144;
    const int qt = (b >> 5) * 8 + (w & 7);
    const unsigned voff = (unsigned)(w * 4096 + lane * 16);
#define AT_LOAD(c)                                                                  \
    do {                                                                            \
        const char* g_ = kvbase + (c) * AT_SLOT;                                    \
        const unsigned d_ = lds0 + ((c) & 3) * AT_SLOT + w * 4096;                  \
        glds16(g_, voff, d_); glds16(g_ + 1024, voff, d_ + 1024);                   \
        glds16(g_ + 2048, voff, d_ + 2048); glds16(g_ + 3072, voff, d_ + 3072);     \
    } while (0)
    u32x4 qa, qb; float kmx;
    if (w < 8) {
        const char* qp = (const char*)Qf + (size_t)(head * 64 + qt) * 2048;
        GLD4(qa, (unsigned)(lane * 16), qp); GLD4(qb, (unsigned)(lane * 16), qp + 1024); GLD1(kmx, (unsigned)(lane * 4), kn2 + head * 64);
        AT_LOAD(0);
    } else if (tid < 528) cnt[lane] = 0;
    LDS_BAR();
    if (w == 12) {
        __builtin_amdgcn_s_setprio(LPRIO);
#pragma unroll 1
        for (int c = 1; c < 8; ++c) {
            if (c > 3) while (__any(cnt[lane & 7] < (unsigned)(c - 1))) __builtin_amdgcn_s_sleep(2);
#pragma unroll 1
            for (int i = 0; i < 32; ++i) glds16(kvbase + c * AT_SLOT + i * 1024, lane * 16, lds0 + (c & 3) * AT_SLOT + i * 1024);
            WAIT_VM(32);
            cnt[12] = c;
        }
        WAIT_VM(0);
        cnt[12] = 8;
        const char* po = (const char*)Wo + (size_t)(b >> 3) * 65536 + (b & 1) * 1024;
#pragma unroll 1
        for (int i = 0; i < 32; ++i) { __builtin_amdgcn_s_sleep(LP); if (!(HABL & 2)) glds16(po + i * 2048, lane * 16, lds0 + AT_DUM); }
        WAIT_VM(0);
        return;
    }
    if (w >= 8) {
        const int hw = w - 8;
        if (HABL & 1) return;
        __builtin_amdgcn_s_setprio(HPRIO);
        if (HABL & 64) {
#pragma unroll 1
            for (int i = 0; i < 70; ++i) __builtin_amdgcn_s_sleep(HP);
            return;
        }
        if (HABL & 128) {
#pragma unroll 1
            for (int i = 0; i < 5; ++i) __builtin_amdgcn_s_sleep(112);
            return;
        }
        const lds_ptr sp = (lds_ptr)lds + AT_STG + hw * 4224;
        const int fb = b < 32 ? b : b - 32;
        const unsigned l16 = lane * 16;
        const unsigned goff = (unsigned)(((lane >> 3) * F_ + (lane & 7) * 8) * 2), moff = (unsigned)(((lane >> 3) * 6144 + (lane & 7) * 8) * 2);
        const unsigned toff = (unsigned)(((lane >> 1) & 3) * 65536 + (lane >> 3) * 32 + (lane & 1) * 16);
        const char* pm = (const char*)wmod + ((size_t)((b & 3) * 256 + hw * 64) * 6144 + 2048 + (b >> 2) * 64) * 2;
        const size_t go = ((size_t)((fb & 3) * 256 + hw * 64) * F_ + (fb >> 2) * 64) * 2;
        const char* pg = (const char*)wg + go;
        const char* pu = (const char*)wu + go;
        char* o = wt + (size_t)(fb >> 2) * 262144 + ((fb & 3) * 2 + (hw >> 1)) * 8192 + (hw & 1) * 2048;
        u32x4 af[2], ra[8], rb[8]; unsigned wrb; u32x2 gv;
#define PACE() __builtin_amdgcn_s_sleep(HP)
#define H_LD1(r, j, h) GLD4(r[j], (HABL & 16) ? 0u : goff, ((j) & 1 ? pu : pg) + (size_t)((h) * 4 + ((j) >> 1)) * 8 * F_ * 2)
#define H_ST1(r, j, h, N) do { asm volatile("s_waitcnt vmcnt(" #N ")" : "+v"(r[j]) :: "memory"); if (HABL & 8) glds16((const char*)scb, 0u, lds0 + AT_STG + hw * 4224); else GST4W(toff, r[j], o + ((j) & 1) * 4096 + ((h) * 4 + ((j) >> 1)) * 256); } while (0)
#define H_WAIT(r, N) asm volatile("s_waitcnt vmcnt(" #N ")" : "+v"(r[0]), "+v"(r[1]), "+v"(r[2]), "+v"(r[3]), "+v"(r[4]), "+v"(r[5]), "+v"(r[6]), "+v"(r[7]) :: "memory")
        __builtin_amdgcn_s_sleep(HSL0);
        {
            const u32x4 z = u32x4{};
            const unsigned zo = (unsigned)((b * 256 + tid - 512) * 16);
            GST4W(zo, z, gran); PACE(); GST4W(zo, z, gran + (1 << 20)); PACE();
            if (zo + (2u << 20) < (unsigned)G_END) GST4W(zo, z, gran + (2 << 20));
        }
        if (HW0) while (cnt[12] < HW0) __builtin_amdgcn_s_sleep(2);
#pragma unroll
        for (int q = 0; q < 2; ++q) GLD4(af[q], (unsigned)((lane >> 4) * 16), scb + (b & 3) * 256 + hw * 64 + q * 32);
        GLDH(wrb, (unsigned)(((b * 16 + (lane & 15)) & 1023) * 2), wr);
#pragma unroll
        for (int q = 0; q < 8; ++q) { GLD4(ra[q], (HABL & 32) ? 0u : moff, pm + (size_t)q * 8 * 6144 * 2); PACE(); }
#pragma unroll
        for (int j = 0; j < 8; ++j) { H_LD1(rb, j, 0); PACE(); }
        DIAG_T(0);
        if (HCUT == 0) { WAIT_VM(0); return; }
        H_WAIT(ra, 8);
        DIAG_T(1);
        if (HCUT == 1) { WAIT_VM(0); return; }
        asm volatile("" : "+v"(wrb), "+v"(af[0]), "+v"(af[1]));
        {
            f32x4 m[4] = {f32x4{}, f32x4{}, f32x4{}, f32x4{}};
            const lds_ptr wp = sp + ((lane >> 1) & 3) * 1056 + (lane >> 3) * 32 + (lane & 1) * 16;
#pragma unroll
            for (int h = 0; h < 2; ++h) {
#pragma unroll
                for (int q = 0; q < 4; ++q) *(LAS u32x4*)(wp + q * 256) = ra[4 * h + q];
#pragma unroll
                for (int q = 0; q < 4; ++q)
                    m[q] = MFMA16(__builtin_bit_cast(bf16x8, af[h]), cat4(trread(sp + q * 1056 + lane * 8), trread(sp + q * 1056 + 512 + lane * 8)), m[q]);
                asm volatile("" ::: "memory");
            }
            const float lo = (lane & 16) ? m[1][0] : m[0][0], hi = (lane & 16) ? m[3][0] : m[2][0];
            *(LAS float*)((lds_ptr)lds + AT_RED + hw * 256 + lane * 4) = (lane & 32) ? hi : lo;
            cnt[w] = 1;
        }
        DIAG_T(2);
        if (HCUT == 2) { WAIT_VM(0); return; }
#define H_P3(j, N) do { H_LD1(ra, j, 1); PACE(); H_ST1(rb, j, 0, N); PACE(); } while (0)
        H_P3(0, 8); H_P3(1, 9);
        if (hw == 0) {
            cnt_wait(cnt + 8 + (lane & 3), 1);
            const LAS float* p = (const LAS float*)((lds_cptr)lds + AT_RED) + lane;
            const u32x2 v = {__float_as_uint((p[0] + p[64]) + (p[128] + p[192])), TAG};
            GST2C((unsigned)(lane * 8), v, gm + b * 512);
        }
        H_P3(2, 10); H_P3(3, 11); H_P3(4, 12); H_P3(5, 13); H_P3(6, 14); H_P3(7, 15);
        DIAG_T(3);
        if (HCUT == 3) { WAIT_VM(0); return; }
        const char* pd = (const char*)wd + (size_t)fb * 32768 + hw * 8192;
#define H_P4(j) do { glds16((HABL & 4) ? (const char*)scb : pd + (j) * 1024, (HABL & 4) ? 0u : l16, lds0 + AT_STG + hw * 4224); __builtin_amdgcn_s_sleep(HP4); H_ST1(ra, j, 1, 16); __builtin_amdgcn_s_sleep(HP4); } while (0)
        H_P4(0); H_P4(1); H_P4(2); H_P4(3); H_P4(4); H_P4(5); H_P4(6); H_P4(7);
        DIAG_T(4);
        if (HCUT == 4) { WAIT_VM(0); return; }
        const unsigned gmo = (unsigned)((lane >> 4) * 512 + ((b & 3) * 16 + (lane & 15)) * 8);
        GLD2C(gv, gmo, gm + (b >> 2) * 2048);
        DIAG_T(5);
        if (HCUT == 5) { WAIT_VM(0); return; }
        asm volatile("s_waitcnt vmcnt(0)" : "+v"(gv) :: "memory");
        if (hw == 0) {
#pragma unroll 1
            for (int spin = 0; spin < SPIN_MAX && !__all(gv[1] == TAG); ++spin) {
                GLD2C(gv, gmo, gm + (b >> 2) * 2048);
                asm volatile("s_waitcnt vmcnt(0)" : "+v"(gv) :: "memory");
            }
            float s = __uint_as_float(gv[0]);
            s = sw16sum(s, s); s = rbf(sw32sum(s, s));
            if (lane < 16) {
                const int n = 2048 + b * 16 + lane, sec = n >> 10;
                if (sec == 4) s = rbf(1.f + s);
                mod[n] = s;
                if (sec == 4) mod[M_GW + (n & 1023)] = s * bflo(wrb);
                if (sec == 3) mod[M_SW + (n & 1023)] = s * bflo(wrb);
            }
        }
        return;
    }
    if (ABAL) { if (w < 4) __builtin_amdgcn_s_setprio(2); else __builtin_amdgcn_s_setprio(1); }
    asm volatile("s_waitcnt vmcnt(4)" : "+v"(qa), "+v"(qb), "+v"(kmx) :: "memory");
    const bf16x8 q0 = __builtin_bit_cast(bf16x8, qa), q1 = __builtin_bit_cast(bf16x8, qb);
    kmx = wmax(kmx);
    float qn = 0.f;
#pragma unroll
    for (int j = 0; j < 4; ++j) { const float a = bflo(qa[j]), c = bfhi(qa[j]), d = bflo(qb[j]), e = bfhi(qb[j]); qn += (a * a + c * c) + (d * d + e * e); }
    qn = sw32sum(qn, qn);
    float mref = sqrtf(qn * kmx) * 1.01f;
    bf16x8 ones;
    { const short o = (((lane >> 4) & 1) == (((lane & 15) >> 2) & 1)) ? (short)0x3f80 : (short)0;
#pragma unroll
      for (int j = 0; j < 8; ++j) ones[j] = o; }
    const lds_cptr fp = (lds_cptr)lds + lane * 16;
    const lds_cnt call = cnt + (lane & 7);
    unsigned target = 0;
    for (int attempt = 0; attempt < 2; ++attempt) {
        f32x16 negm;
#pragma unroll
        for (int i = 0; i < 16; ++i) negm[i] = -mref;
        f32x16 o = f32x16{}; f32x4 ls = f32x4{};
        f32x16 sA, sB;
#define AT_S(dst, p_) do { const bf16x8 k0_ = ldsfrag(p_), k1_ = ldsfrag((p_) + 1024); dst = MFMA32(k0_, q0, negm); dst = MFMA32(k1_, q1, dst); } while (0)
#define AT_PV(src, p_) do {                                                                             \
            const bf16x8 v0_ = ldsfrag((p_) + 2048), v1_ = ldsfrag((p_) + 3072);                        \
            u32x4 p0_, p1_;                                                                             \
            _Pragma("unroll") for (int j = 0; j < 4; ++j) {                                             \
                p0_[j] = pk2(__builtin_amdgcn_exp2f(src[2 * j]), __builtin_amdgcn_exp2f(src[2 * j + 1]));         \
                p1_[j] = pk2(__builtin_amdgcn_exp2f(src[8 + 2 * j]), __builtin_amdgcn_exp2f(src[9 + 2 * j])); }   \
            const bf16x8 pa_ = __builtin_bit_cast(bf16x8, p0_), pb_ = __builtin_bit_cast(bf16x8, p1_);  \
            o = MFMA32(v0_, pa_, o); ls = MFMA16(ones, pa_, ls);                                        \
            o = MFMA32(v1_, pb_, o); ls = MFMA16(ones, pb_, ls);                                        \
        } while (0)
        if (attempt) { WAIT_VM(8); } else { WAIT_VM(0); }
        cnt[w] = ++target;
        cnt_wait(call, target);
        DIAG_T(0);
        AT_S(sA, fp);
        for (int c = 0; c < 8; ++c) {
            if (attempt && c < 6) { WAIT_VM(4); } else { WAIT_VM(0); }
            cnt[w] = ++target;
            const lds_cptr p = fp + (c & 3) * AT_SLOT;
            const lds_cptr pn = fp + ((c + 1) & 3) * AT_SLOT;
            const bool own = attempt;
            const lds_cnt cw = own ? call : cnt + 12;
            const unsigned need = own ? target : (c < 7 ? c + 2 : 0);
            unsigned cv;
#pragma unroll
            for (int t = 0; t < 8; t += 2) {
                if (t == 2) cv = *cw;
                if (t == 4) {
                    if (__any((cv & 0xffffu) < need)) cnt_wait(cw, need);
                    asm volatile("" ::: "memory");
                    if (attempt && c + 3 < 8) AT_LOAD(c + 3);
                    if (ABAL && c == ABAL && w < 4) __builtin_amdgcn_s_setprio(0);
                }
                AT_S(sB, p + (t + 1) * 4096);
                AT_PV(sA, p + t * 4096);
                if (t + 2 < 8) AT_S(sA, p + (t + 2) * 4096); else AT_S(sA, pn);
                AT_PV(sB, p + (t + 1) * 4096);
            }
        }
        DIAG_T(1);
        const bool bad = !(ls[0] >= 8.6736174e-19f);
        ++target;
        cnt[w] = target + (__any(bad) ? 0x10000u : 0u);
        const float rl = 1.f / ls[0];
        u32x4* op = (u32x4*)((char*)Of + ((size_t)(qt * 32 + head) * 2) * 1024) + lane;
#pragma unroll
        for (int s = 0; s < 2; ++s) {
            u32x4 v;
#pragma unroll
            for (int j = 0; j < 4; ++j) v[j] = pk2(o[8 * s + 2 * j] * rl, o[8 * s + 2 * j + 1] * rl);
            op[s * 64] = v;
        }
        if (attempt == 1 || !__any(cnt_wait(call, target) != target)) break;
        float mx = -INFINITY;
        const bf16x8* kp = (const bf16x8*)kvbase + lane;
        for (int t = 0; t < 64; ++t) {
            const bf16x8 k0 = kp[t * 256], k1 = kp[t * 256 + 64];
            f32x16 s = MFMA32(k0, q0, f32x16{}); s = MFMA32(k1, q1, s);
#pragma unroll
            for (int i = 0; i < 16; ++i) mx = fmaxf(mx, s[i]);
        }
        mx = fmaxf(mx, xor32(mx));
        mref = mx;
        WAIT_VM(0);
        AT_LOAD(0); AT_LOAD(1); AT_LOAD(2);
    }
    DIAG_END(3);
}

constexpr int OS = 24576, OV = 6 * OS, OST = OV + 3072, OBN = OST + 3072, OPT = OBN + 2048, OGS = OPT + 1024, O_LDS = OGS + 128;
#define OMAP 0
#define OROT 1
#define FF0 2
#define OSL3 16
#define OSLP 24
#define OSL2 0
#define OSPEC 1
#define OABL 0
constexpr int XOR7 = 0x141;
DI float oct_sum(float v) { v += dpp<XOR1>(v); v += dpp<XOR2>(v); v += dpp<XOR7>(v); return v; }
DI float dot2(unsigned a, unsigned b, float c) { return __builtin_amdgcn_fdot2_f32_bf16(__builtin_bit_cast(bf16x2_t, a), __builtin_bit_cast(bf16x2_t, b), c, false); }
__global__ __launch_bounds__(768, 3) void k_out(const u16* __restrict__ Of, const u16* __restrict__ Wo, const u16* __restrict__ x,
                                                const float* __restrict__ mean1, const float* __restrict__ mod,
                                                const char* __restrict__ wt, const u16* __restrict__ wd, char* __restrict__ gran,
                                                u16* __restrict__ out DIAG_ARG) {
    DIAG_BEGIN
    __shared__ __attribute__((aligned(16))) char lds[O_LDS];
    const int b = blockIdx.x, tid = threadIdx.x, lane = tid & 63;
    const int w = __builtin_amdgcn_readfirstlane(tid >> 6);
    const int xcd = b & 7, idx = b >> 3;
    const int rp = (xcd >> 1) * 8 + (idx & 7), cp = (xcd & 1) * 4 + (idx >> 3);
    const int mt = w & 1, nt = w >> 1, blk = lane >> 4, hh = lane >> 5;
    const unsigned lds0 = (unsigned)(uintptr_t)lds;
    const lds_ptr L = (lds_ptr)lds;
    if (w >= 8) {
        const int lw = w - 8;
        const char* pa = (const char*)Of + (size_t)(rp * 2 + (lw >> 1)) * 65536 + (lw & 1) * 2048;
        const char* pb = (const char*)Wo + ((size_t)lw * 16 * 1024 + cp * 128) * 2;
        const unsigned aoff = lane * 16;
        const unsigned boff = (unsigned)(((lane >> 3) * 1024 + ((lane & 7) ^ ((lane >> 2) & 4)) * 8) * 2);
        unsigned dst = lds0;
        int si = OROT * ((idx & 7) * 2 + (idx >> 3) * 4) & 15;
#define O_STAGE() do { const char* a_ = pa + si * 4096; const char* b_ = pb + (size_t)si * 131072; const unsigned d_ = dst + 8192 + lw * 4096;    \
                       glds16(a_, aoff, dst + lw * 2048); glds16(a_ + 1024, aoff, dst + lw * 2048 + 1024);                                        \
                       glds16(b_, boff, d_); glds16(b_ + 128, boff, d_ + 1024); glds16(b_ + 16384, boff, d_ + 2048); glds16(b_ + 16384 + 128, boff, d_ + 3072);  \
                       si = (si + 1) & 15; dst += OS; if (dst >= lds0 + OV) dst = lds0; } while (0)
        if (lw < 3) glds16(mod + M_GA + lw * 2048 + cp * 128, (unsigned)(hh * 4096 + (lane & 31) * 16), lds0 + OV + lw * 1024);
#pragma unroll 1
        for (int s = 0; s < 3; ++s) O_STAGE();
        WAIT_BAR(12);
#pragma unroll 1
        for (int s = 0; s < 2; ++s) O_STAGE();
        WAIT_BAR(18);
        O_STAGE();
#pragma unroll 1
        for (int s = 1; s < 11; ++s) { O_STAGE(); if (s < 10) WAIT_BAR(24); }
        WAIT_BAR(24); WAIT_BAR(18); WAIT_BAR(12); WAIT_BAR(6); WAIT_BAR(0);
        asm volatile("s_barrier" ::: "memory");
        DIAG_T(0);
        {
            u32x2 v[6];
            const char* gp = gran + (size_t)rp * 12288 + lw * 3072;
#pragma unroll 1
            for (int spin = 0; spin < SPIN_MAX; ++spin) {
#pragma unroll
                for (int i = 0; i < 6; ++i) GLD2C(v[i], (unsigned)(lane * 8), gp + i * 512);
                asm volatile("s_waitcnt vmcnt(0)" : "+v"(v[0]), "+v"(v[1]), "+v"(v[2]), "+v"(v[3]), "+v"(v[4]), "+v"(v[5]) :: "memory");
                if (__all((v[0][1] & v[1][1] & v[2][1] & v[3][1] & v[4][1] & v[5][1]) == TAG)) break;
            }
            DIAG_T(1);
            LAS u32x2* p = (LAS u32x2*)(L + 2 * OS + lane * 112 + lw * 8);
            p[0] = u32x2{v[0][0], v[3][0]}; p[4] = u32x2{v[1][0], v[4][0]}; p[8] = u32x2{v[2][0], v[5][0]};
        }
        LDS_BAR();
        DIAG_T(2);
        if (lw == 0) {
            const char* g2 = gran + G_H2;
            u32x2 v; u32x4 hv[4];
            int cur = 63; bool have = false;
            hv[0] = u32x4{}; hv[1] = u32x4{}; hv[2] = u32x4{}; hv[3] = u32x4{};
            __builtin_amdgcn_s_sleep(OSLP);
#pragma unroll 1
            for (int spin = 0; spin < SPIN_MAX; ++spin) {
                GLD2C(v, (unsigned)((lane & 31) * 128), g2 + 32 * 4096 + (b & (NREP - 1)) * 4096);
                if (!have && cur < 32) {
#pragma unroll
                    for (int i = 0; i < 4; ++i) GLD4C(hv[i], (unsigned)(lane * 16), g2 + cur * 4096 + i * 1024);
                }
                asm volatile("s_waitcnt vmcnt(0)" : "+v"(v), "+v"(hv[0]), "+v"(hv[1]), "+v"(hv[2]), "+v"(hv[3]) :: "memory");
                const bool ok = (v[1] & 0xffff0000u) == TAG;
                const float r = ok ? __uint_as_float(v[0]) : -INFINITY;
                const float wbest = wmax(r);
                const int win = (int)__builtin_ctzll(__ballot(ok && r == wbest) | (1ull << 63));
                have = win == cur && __all((hv[0][1] & hv[0][3] & hv[1][1] & hv[1][3] & hv[2][1] & hv[2][3] & hv[3][1] & hv[3][3]) == TAG);
                cur = __builtin_popcountll(__ballot(ok)) >= 2 * OSPEC ? win : 63;
                if (have && __all(ok)) break;
                if (OSL2) __builtin_amdgcn_s_sleep(OSL2);
            }
            if (lane == cur) *(LAS u32x2*)(L + OGS + 64) = u32x2{v[0], (unsigned)(cur * 64) + (v[1] & 63)};
            LAS u32x2* p = (LAS u32x2*)(L + OBN + (lane >> 3) * 64 + (lane & 3) * 16 + ((lane >> 2) & 1) * 8);
#pragma unroll
            for (int i = 0; i < 4; ++i) p[i * 64] = u32x2{hv[i][0], hv[i][2]};
        }
        DIAG_T(6);
        LDS_BAR();
        return;
    }
    const int row = rp * 64 + mt * 32 + (lane & 31);
    const unsigned xoff = (unsigned)((row * D_ + cp * 128 + nt * 32 + 8 * hh) * 2);
    u32x4 xi[2]; float ref; f32x2 gws, sws, gbv;
    const int ycol = w * 128 + (lane & 1) * 63 + lane;
    GLD4(xi[0], xoff, x); GLD4(xi[1], xoff, x + 16);
    GLD1(ref, (unsigned)(row * 4), mean1);
    GLD2(gws, (unsigned)(tid * 8), mod + M_GW); GLD2(sws, (unsigned)(tid * 8), mod + M_SW); GLD2(gbv, (unsigned)(ycol * 4), mod + M_GB);
    u32x4 fr[12];
    const int fc = b < 32 ? b : b - 32;
    const char* pw = (OABL & 1) ? wt : wt + (size_t)(fc * 8 + w) * 8192;
    const char* pd = (OABL & 1) ? wt : (const char*)wd + (size_t)fc * 32768 + w * 4096 - 8192;
    const lds_cptr ap = (lds_cptr)lds + mt * 4096 + lane * 16;
    const lds_cptr bp = (lds_cptr)lds + 8192 + (nt >> 1) * 1024 + hh * 512 + ((lane & 15) >> 2) * 128 + ((nt ^ (lane >> 3)) & 1) * 64 + (blk & 1) * 32 + (lane & 3) * 8;
    f32x16 acc = f32x16{};
    asm volatile("s_waitcnt vmcnt(0)" : "+v"(xi[0]), "+v"(xi[1]), "+v"(ref), "+v"(gws), "+v"(sws), "+v"(gbv) :: "memory");
    DIAG_T(0);
    {
        const float a = wsum(gws[0] + gws[1]), c = wsum(sws[0] + sws[1]);
        if (lane == 0) *(LAS f32x2*)(L + OGS + w * 8) = f32x2{a, c};
    }
    float d[16];
#pragma unroll
    for (int j = 0; j < 2; ++j)
#pragma unroll
        for (int e = 0; e < 2; ++e) {
            const auto r = __builtin_amdgcn_permlane32_swap(xi[j][e], xi[j][2 + e], false, false);
            d[8 * j + 2 * e] = bflo(r[0]) - ref; d[8 * j + 2 * e + 1] = bfhi(r[0]) - ref;
            d[8 * j + 4 + 2 * e] = bflo(r[1]) - ref; d[8 * j + 5 + 2 * e] = bfhi(r[1]) - ref;
        }
    bf16x8 fa[2][4], fb[2][4];
    f32x4 va[4], vb[4];
    const lds_cptr vp = (lds_cptr)lds + OV + (nt * 32 + 4 * hh) * 4;
#pragma unroll
    for (int s = 0; s < 17; ++s) {
        if (s < 16) {
            LDS_BAR();
            int so = (s % 6) * OS;
            asm volatile("" : "+s"(so));
#pragma unroll
            for (int ks = 0; ks < 4; ++ks) { fa[s & 1][ks] = ldsfrag(ap + so + ks * 1024); fb[s & 1][ks] = cat4(trread(bp + so + ks * 4096), trread(bp + so + ks * 4096 + 2048)); }
            if (s >= FF0 && s < FF0 + 12) GLD4(fr[s - FF0], (unsigned)((OABL & 2) ? 0 : lane * 16), (s - FF0 < 8 ? pw : pd) + (s - FF0) * 1024);
            SBAR();
        }
        if (s == 16) {
#pragma unroll
            for (int g = 0; g < 4; ++g) { va[g] = *(const LAS f32x4*)(vp + g * 32); vb[g] = *(const LAS f32x4*)(vp + 2048 + g * 32); }
            SBAR();
        }
        if (s > 0) {
#pragma unroll
            for (int ks = 0; ks < 4; ++ks) acc = MFMA32(fb[(s - 1) & 1][ks], fa[(s - 1) & 1][ks], acc);
            SBAR();
        }
    }
    DIAG_T(2);
    {
        float s1a = 0.f, s1b = 0.f, s2a = 0.f, s2b = 0.f, sda = 0.f, sdb = 0.f;
#pragma unroll
        for (int g = 0; g < 4; ++g)
#pragma unroll
            for (int e = 0; e < 4; e += 2) {
                const float v0 = __builtin_fmaf(va[g][e], acc[4 * g + e], d[4 * g + e]), v1 = __builtin_fmaf(va[g][e + 1], acc[4 * g + e + 1], d[4 * g + e + 1]);
                d[4 * g + e] = v0; d[4 * g + e + 1] = v1;
                s1a += v0; s1b += v1; s2a = __builtin_fmaf(v0, v0, s2a); s2b = __builtin_fmaf(v1, v1, s2b);
                sda = __builtin_fmaf(v0, vb[g][e], sda); sdb = __builtin_fmaf(v1, vb[g][e + 1], sdb);
            }
        const float q12 = sw32sum(s1a + s1b, s2a + s2b), sd = sda + sdb, q3 = sw32sum(sd, sd);
        const int o = OST + nt * 256 + (mt * 32 + (lane & 31)) * 4;
        *(LAS float*)(L + o + hh * 1024) = q12;
        *(LAS float*)(L + o + 2048) = q3;
    }
    asm volatile("s_waitcnt vmcnt(0)" : "+v"(fr[0]), "+v"(fr[1]), "+v"(fr[2]), "+v"(fr[3]), "+v"(fr[4]), "+v"(fr[5]), "+v"(fr[6]), "+v"(fr[7]), "+v"(fr[8]), "+v"(fr[9]), "+v"(fr[10]), "+v"(fr[11]) :: "memory");
    LDS_BAR();
    if (w < 3) {
        const LAS float* p = (const LAS float*)(L + OST + w * 1024) + lane;
        const float s = (p[0] + p[64]) + (p[128] + p[192]);
        const u32x2 v = {__float_as_uint(s), TAG};
        GST2C((unsigned)(cp * 1536 + w * 512 + lane * 8), v, gran + (size_t)rp * 12288);
    }
    DIAG_T(3);
    {
        LAS u32x4* p = (LAS u32x4*)(L + (w < 6 ? 4 * OS + w * 8192 : (w - 6) * 8192)) + lane;
#pragma unroll
        for (int j = 0; j < 8; ++j) p[j * 64] = fr[j];
        LAS u32x4* q = (LAS u32x4*)(L + 16384 + w * 4096) + lane;
#pragma unroll
        for (int j = 0; j < 4; ++j) q[j * 64] = fr[8 + j];
    }
    float gtot, ctot;
    {
        const f32x2 t = *(const LAS f32x2*)(L + OGS + (lane & 7) * 8);
        gtot = oct_sum(t[0]); ctot = oct_sum(t[1]);
    }
    f32x4 vc[4];
#pragma unroll
    for (int g = 0; g < 4; ++g) { va[g] = *(const LAS f32x4*)(vp + 512 + g * 32); vb[g] = *(const LAS f32x4*)(vp + 1024 + g * 32); vc[g] = *(const LAS f32x4*)(vp + 1536 + g * 32); }
    bf16x8 fw[12];
    {
        const lds_cptr p = (lds_cptr)lds + (w < 6 ? 4 * OS + w * 8192 : (w - 6) * 8192) + lane * 8;
#pragma unroll
        for (int j = 0; j < 8; ++j) fw[j] = cat4(trread(p + j * 1024), trread(p + j * 1024 + 512));
    }
    DIAG_T(4);
    LDS_BAR();
    DIAG_T(5);
    int brow; f32x2 fs;
    char* g2 = gran + G_H2;
    {
        const LAS f32x4* p = (const LAS f32x4*)(L + 2 * OS + lane * 112);
        const f32x4 a = p[0] + p[1], q = p[2] + p[3], c = p[4] + p[5];
        const float md = ((a[0] + a[1]) + (a[2] + a[3])) * (1.f / D_), s2 = ((q[0] + q[1]) + (q[2] + q[3])) * (1.f / D_), sd = (c[0] + c[1]) + (c[2] + c[3]);
        const float rstd = __builtin_amdgcn_rsqf(fmaxf(s2 - md * md, 0.f) + LN_EPS);
        float r = (sd - md * gtot) * rstd;
        r = r == r ? r : -INFINITY;
        const float best = wmax(r);
        brow = (int)__builtin_ctzll(__ballot(r == best) | (1ull << 63));
        if (w == 0 && cp == 0 && lane < NREP) { const u32x2 v = {__float_as_uint(best), TAG | (unsigned)brow}; GST2C((unsigned)(lane * 4096 + rp * 128), v, g2 + 32 * 4096); }
        const auto r0 = __builtin_amdgcn_permlane32_swap(__float_as_uint(rstd), __float_as_uint(rstd), false, false);
        const auto r1 = __builtin_amdgcn_permlane32_swap(__float_as_uint(-md * rstd), __float_as_uint(-md * rstd), false, false);
        fs = f32x2{__uint_as_float(mt ? r0[1] : r0[0]), __uint_as_float(mt ? r1[1] : r1[0])};
    }
    float h[16];
#pragma unroll
    for (int i = 0; i < 16; ++i) h[i] = __builtin_fmaf(__builtin_fmaf(d[i], fs[0], fs[1]), vb[i >> 2][i & 3], va[i >> 2][i & 3]);
    const bool mine = mt * 32 + (lane & 31) == brow;
    if ((brow >> 5) == mt) {
#pragma unroll
        for (int g = 0; g < 4; ++g) {
            const u32x4 v = {pk2(h[4 * g], h[4 * g + 1]), TAG, pk2(h[4 * g + 2], h[4 * g + 3]), TAG};
            if (mine) GST4C((unsigned)(rp * 4096 + (cp * 128 + nt * 32 + 8 * g + 4 * hh) * 4), v, g2);
        }
    }
    DIAG_T(6);
#pragma unroll
    for (int i = 0; i < 16; ++i) h[i] = __builtin_fmaf(vc[i >> 2][i & 3], h[i], d[i]) + ref;
    u32x4 o[2];
#pragma unroll
    for (int j = 0; j < 2; ++j) {
#pragma unroll
        for (int e = 0; e < 2; ++e) {
            const auto r = __builtin_amdgcn_permlane32_swap(pk2(h[8 * j + 2 * e], h[8 * j + 2 * e + 1]), pk2(h[8 * j + 4 + 2 * e], h[8 * j + 5 + 2 * e]), false, false);
            o[j][e] = r[0]; o[j][2 + e] = r[1];
        }
        if (!mine) *(u32x4*)((char*)(out + 16 * j) + xoff) = o[j];
    }
    {
        const lds_cptr q = (lds_cptr)lds + 16384 + ((lane & 15) >> 2) * 2048 + w * 256 + blk * 32 + (lane & 3) * 8;
#pragma unroll
        for (int j = 0; j < 4; ++j) fw[8 + j] = cat4(trread(q + (j & 1) * 16384 + (j >> 1) * 128), trread(q + (j & 1) * 16384 + (j >> 1) * 128 + 8192));
    }
    LDS_BAR();
    DIAG_T(7);
    const u32x2 wi = *(const LAS u32x2*)(L + OGS + 64);
    char* gy = gran + G_Y;
    if ((brow >> 5) == mt) {
        if ((int)(wi[1] >> 6) == rp) {
#pragma unroll
            for (int g = 0; g < 8; ++g) {
                const u32x4 v = {__float_as_uint(h[2 * g]), TAG, __float_as_uint(h[2 * g + 1]), TAG};
                if (mine) GST4C((unsigned)(224 * 8192 + (cp * 128 + nt * 32 + 8 * (g >> 1) + 4 * hh + 2 * (g & 1)) * 8), v, gy);
            }
        } else if (mine) {
            *(u32x4*)((char*)out + xoff) = o[0]; *(u32x4*)((char*)(out + 16) + xoff) = o[1];
        }
    }
    if (b >= 32) {
        f32x4 ag = f32x4{}, au = f32x4{};
#pragma unroll
        for (int q = 0; q < 4; ++q) {
            const bf16x8 a = ldsfrag((lds_cptr)lds + OBN + w * 256 + q * 64 + blk * 16);
            ag = MFMA16(a, fw[q], ag); au = MFMA16(a, fw[4 + q], au);
        }
        if (lane < 32) *(LAS float*)(L + OPT + (lane & 31) * 32 + w * 4) = lane < 16 ? ag[0] : au[0];
        const float sig = __builtin_amdgcn_rcpf(1.f + __expf(-(__uint_as_float(wi[0]) + ctot)));
        LDS_BAR();
        const LAS f32x4* pp = (const LAS f32x4*)(L + OPT + (lane & 15) * 32);
        const f32x4 a0 = pp[0] + pp[1], u0 = pp[32] + pp[33];
        const float sg = (a0[0] + a0[1]) + (a0[2] + a0[3]), su = (u0[0] + u0[1]) + (u0[2] + u0[3]);
        const float act = sg * su * __builtin_amdgcn_rcpf(1.f + __expf(-sg));
        const unsigned pr = pk2(act, dpp<XOR1>(act));
        float y0 = 0.f, y1 = 0.f, y2 = 0.f, y3 = 0.f;
#pragma unroll
        for (int i = 0; i < 8; i += 2) {
            const unsigned s0 = __builtin_amdgcn_readlane(pr, 2 * i), s1 = __builtin_amdgcn_readlane(pr, 2 * i + 2);
            const u32x4 w0 = __builtin_bit_cast(u32x4, fw[8 + (i >> 2)]), w1 = __builtin_bit_cast(u32x4, fw[10 + (i >> 2)]);
            y0 = dot2(s0, w0[i & 3], y0); y1 = dot2(s0, w1[i & 3], y1); y2 = dot2(s1, w0[(i & 3) + 1], y2); y3 = dot2(s1, w1[(i & 3) + 1], y3);
        }
        y0 += y2; y1 += y3;
        const float t = dpp<XOR1>((lane & 1) ? y0 : y1);
        const u32x4 v = {__float_as_uint(((lane & 1) ? t : y0) * (gbv[0] * sig)), TAG, __float_as_uint(((lane & 1) ? y1 : t) * (gbv[1] * sig)), TAG};
        GST4C((unsigned)((b - 32) * 8192 + ycol * 8), v, gy);
    } else __builtin_amdgcn_s_sleep(OSL3 + 10);
    DIAG_T(1);
    {
        const int cg = (b & 7) * 32 + (b >> 3), r = w * 32 + (lane >> 1);
        u32x4 v;
        __builtin_amdgcn_s_sleep(OSL3);
#pragma unroll 1
        for (int spin = 0; spin < SPIN_MAX; ++spin) {
            GLD4C(v, (unsigned)((r < 224 ? r : 224) * 8192 + (lane & 1) * 16), gy + cg * 32);
            asm volatile("s_waitcnt vmcnt(0)" : "+v"(v) :: "memory");
            if (__all((v[1] & v[3]) == TAG)) break;
        }
        float a = r > 224 ? 0.f : __uint_as_float(v[0]), c = r > 224 ? 0.f : __uint_as_float(v[2]);
        a = sw32sum(a, c);
        a = sw16sum(a, a); a += dpp<XOR2>(a); a += dpp<ROR4>(a); a += dpp<ROR8>(a);
        if ((lane & 30) == 0) *(LAS float*)(L + OST + w * 16 + (lane & 1) * 8 + hh * 4) = a;
        LDS_BAR();
        if (tid < 8) {
            f32x4 q = *(const LAS f32x4*)(L + OST + lane * 16);
#pragma unroll
            for (int e = 0; e < 4; ++e) q[e] = oct_sum(q[e]);
            if (tid == 0) *(u32x2*)(out + (size_t)(wi[1] & (S_ - 1)) * D_ + cg * 4) = u32x2{pk2(q[0], q[1]), pk2(q[2], q[3])};
        }
    }
    DIAG_END(4);
}

extern "C" void kernel_launch(void* const* d_in, const int* in_sizes, int n_in, void* d_out, int out_size, void* d_ws, size_t ws_size,
                              hipStream_t stream) {
    const u16* x = (const u16*)d_in[0];
    const u16* wqkv = (const u16*)d_in[1];
    const u16* wo = (const u16*)d_in[2];
    const u16* wr = (const u16*)d_in[4];
    const u16* wg = (const u16*)d_in[5];
    const u16* wu = (const u16*)d_in[6];
    const u16* wd = (const u16*)d_in[7];
    const u16* c = (const u16*)d_in[8];
    const u16* wmod = (const u16*)d_in[9];
    char* ws = (char*)d_ws;
    float* mod = (float*)(ws + WS_MOD);
    char* gran = ws + WS_GRAN;
    float* modp = (float*)(ws + WS_MODP);
    float* kn2 = (float*)(ws + WS_KN2);
    f32x2* stat = (f32x2*)(ws + WS_STAT);
    float* mean1 = (float*)(ws + WS_MEAN);
    u16* Qf = (u16*)(ws + WS_QF);
    u16* KVf = (u16*)(ws + WS_KVF);
    u16* Of = (u16*)(ws + WS_OF);
    u16* out = (u16*)d_out;
    u16* scb = (u16*)(ws + WS_SC);
    char* wt = ws + WS_WT;
    k_mod<<<256, 512, 0, stream>>>(c, wmod, scb, x, modp, mean1, stat, ws + WS_GM, wqkv DIAG_PASS);
    k_qkv<<<256, 768, 0, stream>>>(x, stat, modp, wqkv, Qf, KVf, kn2 DIAG_PASS);
    k_attn<<<256, 832, 0, stream>>>(Qf, KVf, kn2, Of, wg, wu, wd, wt, wmod, scb, wr, mod, wo, gran, ws + WS_GM DIAG_PASS);
    k_out<<<256, 768, 0, stream>>>(Of, wo, x, mean1, mod, wt, wd, gran, out DIAG_PASS);
}
```

```cpp
#include <hip/hip_runtime.h>
#include <hip/hip_bf16.h>
#include <stdint.h>

typedef __attribute__((ext_vector_type(8))) short bf16x8;
typedef __attribute__((ext_vector_type(4))) short s16x4;
typedef __attribute__((ext_vector_type(16))) float f32x16;
typedef __attribute__((ext_vector_type(4))) float f32x4;
typedef __attribute__((ext_vector_type(2))) float f32x2;
typedef __attribute__((ext_vector_type(4))) unsigned u32x4;
typedef __attribute__((ext_vector_type(2))) unsigned u32x2;
typedef __bf16 bf16x2_t __attribute__((ext_vector_type(2)));
typedef unsigned short u16;
typedef __attribute__((address_space(3))) const char* lds_cptr;
typedef __attribute__((address_space(3))) char* lds_ptr;

#define DI __device__ __forceinline__
#define MFMA32(a, b, c) __builtin_amdgcn_mfma_f32_32x32x16_bf16((a), (b), (c), 0, 0, 0)
#define MFMA16(a, b, c) __builtin_amdgcn_mfma_f32_16x16x32_bf16((a), (b), (c), 0, 0, 0)
#define SBAR() __builtin_amdgcn_sched_barrier(0)

constexpr int S_ = 2048, D_ = 1024, H_ = 32, F_ = 3584;
constexpr float LN_EPS = 1e-5f;
constexpr float QSCALE = 0.17677669529663687f * 1.4426950408889634f;

constexpr size_t WS_MOD = 0;
constexpr size_t WS_KN2 = 32768;
constexpr size_t WS_SC = 65536;
constexpr size_t WS_MEAN = 114688;
constexpr size_t WS_GRAN = 131072;
#define NREP 8
constexpr int G_H2 = 32 * 12288, G_BEST = G_H2 + 32 * 4096, G_Y = G_BEST + NREP * 4096, G_END = G_Y + 225 * 8192;
constexpr size_t WS_MODP = 5u << 20;
constexpr size_t WS_GM = 4u << 20;
constexpr unsigned TAG = 0x5EED0000u;
constexpr int SPIN_MAX = 1 << 18;
constexpr size_t WS_QF = 6u << 20;
constexpr size_t WS_KVF = WS_QF + (4u << 20);
constexpr size_t WS_OF = WS_KVF + (8u << 20);
constexpr size_t WS_WT = WS_OF + (4u << 20);
constexpr size_t WS_XN = WS_WT + (16u << 20);

#define DIAG_ARG
#define DIAG_BEGIN
#define DIAG_T(i)
#define DIAG_END(kid)
#define DIAG_PASS
#define DIAG_A(i)
#define DIAG_A0
DI float bflo(unsigned u) { return __uint_as_float(u << 16); }
DI float bfhi(unsigned u) { return __uint_as_float(u & 0xffff0000u); }
DI unsigned pk2(float lo, float hi) {
    f32x2 v = {lo, hi};
    return __builtin_bit_cast(unsigned, __builtin_convertvector(v, bf16x2_t));
}
DI float rbf(float x) { return bflo(pk2(x, x) & 0xffffu); }
DI float bf2f(u16 v) { return __uint_as_float((unsigned)v << 16); }
DI s16x4 trread(lds_cptr p) {
    typedef short v4i16_t __attribute__((ext_vector_type(4)));
    return __builtin_bit_cast(s16x4, __builtin_amdgcn_ds_read_tr16_b64_v4i16((__attribute__((address_space(3))) v4i16_t*)p));
}
DI bf16x8 ldsfrag(lds_cptr p) { return *(const __attribute__((address_space(3))) bf16x8*)p; }
DI bf16x8 cat4(s16x4 a, s16x4 b) { return __builtin_shufflevector(a, b, 0, 1, 2, 3, 4, 5, 6, 7); }
DI void glds16(const void* sbase, unsigned voff, unsigned lds_base) {
    unsigned sv;
    asm volatile("s_mov_b32 %0, m0\n\ts_mov_b32 m0, %3\n\ts_nop 0\n\tglobal_load_lds_dwordx4 %1, %2\n\ts_mov_b32 m0, %0"
                 : "=&s"(sv) : "v"(voff), "s"(sbase), "s"(lds_base) : "memory");
}
#define GLD4(dst, voff, sbase) asm volatile("global_load_dwordx4 %0, %1, %2" : "=v"(dst) : "v"(voff), "s"(sbase) : "memory")
#define GLD2(dst, voff, sbase) asm volatile("global_load_dwordx2 %0, %1, %2" : "=v"(dst) : "v"(voff), "s"(sbase) : "memory")
#define GLD1(dst, voff, sbase) asm volatile("global_load_dword %0, %1, %2" : "=v"(dst) : "v"(voff), "s"(sbase) : "memory")
#define GLDH(dst, voff, sbase) asm volatile("global_load_ushort %0, %1, %2" : "=v"(dst) : "v"(voff), "s"(sbase) : "memory")
#define GLD2C(dst, voff, sbase) asm volatile("global_load_dwordx2 %0, %1, %2 sc1" : "=v"(dst) : "v"(voff), "s"(sbase) : "memory")
#define GLD4C(dst, voff, sbase) asm volatile("global_load_dwordx4 %0, %1, %2 sc1" : "=v"(dst) : "v"(voff), "s"(sbase) : "memory")
#define GST2C(voff, val, sbase) asm volatile("global_store_dwordx2 %0, %1, %2 sc1" :: "v"(voff), "v"(val), "s"(sbase) : "memory")
#define GST4C(voff, val, sbase) asm volatile("global_store_dwordx4 %0, %1, %2 sc1\n\ts_nop 1" :: "v"(voff), "v"(val), "s"(sbase) : "memory")
#define WAIT_VM(n) asm volatile("s_waitcnt vmcnt(" #n ")" ::: "memory")
#define WAIT_BAR(n) asm volatile("s_waitcnt vmcnt(" #n ") lgkmcnt(0)\n\ts_barrier" ::: "memory")
#define LDS_BAR() asm volatile("s_waitcnt lgkmcnt(0)\n\ts_barrier" ::: "memory")
DI float xor32(float v) {
    auto r = __builtin_amdgcn_permlane32_swap(__float_as_uint(v), __float_as_uint(v), false, false);
    return (threadIdx.x & 32) ? __uint_as_float(r[0]) : __uint_as_float(r[1]);
}
DI float wave_sum(float v) {
#pragma unroll
    for (int o = 1; o < 64; o <<= 1) v += __shfl_xor(v, o);
    return v;
}
DI float wave_max(float v) {
#pragma unroll
    for (int o = 1; o < 64; o <<= 1) v = fmaxf(v, __shfl_xor(v, o));
    return v;
}

template <int CTRL> DI float dpp(float x) { return __builtin_bit_cast(float, __builtin_amdgcn_mov_dpp(__builtin_bit_cast(int, x), CTRL, 0xf, 0xf, true)); }
constexpr int XOR1 = 0xB1, XOR2 = 0x4E, ROR4 = 0x124, ROR8 = 0x128;
DI float sw32sum(float a, float b) {
    auto r = __builtin_amdgcn_permlane32_swap(__float_as_uint(a), __float_as_uint(b), false, false);
    return __uint_as_float(r[0]) + __uint_as_float(r[1]);
}
DI float sw16sum(float a, float b) {
    auto r = __builtin_amdgcn_permlane16_swap(__float_as_uint(a), __float_as_uint(b), false, false);
    return __uint_as_float(r[0]) + __uint_as_float(r[1]);
}
DI float row_sum16(float v) { v += dpp<XOR1>(v); v += dpp<XOR2>(v); v += dpp<ROR4>(v); v += dpp<ROR8>(v); return v; }
DI float row_max16(float v) { v = fmaxf(v, dpp<XOR1>(v)); v = fmaxf(v, dpp<XOR2>(v)); v = fmaxf(v, dpp<ROR4>(v)); v = fmaxf(v, dpp<ROR8>(v)); return v; }
DI float wsum(float v) { v = row_sum16(v); v = sw16sum(v, v); return sw32sum(v, v); }
DI float wmax(float m) {
    m = row_max16(m);
    { auto s_ = __builtin_amdgcn_permlane16_swap(__float_as_uint(m), __float_as_uint(m), false, false); m = fmaxf(__uint_as_float(s_[0]), __uint_as_float(s_[1])); }
    { auto s_ = __builtin_amdgcn_permlane32_swap(__float_as_uint(m), __float_as_uint(m), false, false); m = fmaxf(__uint_as_float(s_[0]), __uint_as_float(s_[1])); }
    return m;
}
DI float sfma(float a, float b, float c) { float r; asm("v_fma_f32 %0, %1, %2, %3" : "=v"(r) : "v"(a), "v"(b), "v"(c)); return r; }
#define LAS __attribute__((address_space(3)))
constexpr int M_SA = 0, M_G1 = 1024, M_GA = 2048, M_SB = 3072, M_G2 = 4096, M_GB = 5120, M_GW = 6144, M_SW = 7168;

#define MQ 4
DI unsigned wpf_off(int id, int xcd, int lane) {
    const int r2 = (id / 3) * 2 + (lane >> 5), k = (r2 >> 5) * 64 + (xcd & 1) * 32 + (r2 & 31);
    return (unsigned)(k * 6144 + (id % 3) * 2048 + (xcd >> 1) * 512 + (lane & 31) * 16);
}
#define MBAR 3
#define MSL0 0
#define MSL 0
#define MSC1 "sc1"
__global__ __launch_bounds__(512) void k_mod(const u16* __restrict__ c, const u16* __restrict__ wmod, u16* __restrict__ scb,
                                             const u16* __restrict__ x, float* __restrict__ modp, float* __restrict__ mean1,
                                             char* __restrict__ xn, char* __restrict__ gm, const u16* __restrict__ W DIAG_ARG) {
    DIAG_BEGIN
    __shared__ __attribute__((aligned(16))) float red[8][32];
    __shared__ __attribute__((aligned(16))) unsigned xt[8][528];
    const int t = threadIdx.x, lane = t & 63, b = blockIdx.x;
    const int w = __builtin_amdgcn_readfirstlane(t >> 6);
    const int xcd = b & 7, i = b >> 3;
    const int cg = (xcd * 4 + (i >> 3)) * 2 + (i & 1), ks = (i >> 1) & 3;
    const int rg = ks * 256 + (t >> 2), row = (b >> 1) * 16 + (w >> 1) * 4 + (b & 1) * 2 + (w & 1);
    u32x4 wv[2], v[2], pf[3]; unsigned cv[2];
    GLD4(v[0], (unsigned)(lane * 16), x + (size_t)row * D_); GLD4(v[1], (unsigned)(lane * 16), x + (size_t)row * D_ + 512);
    if (MBAR & 1) asm volatile("s_barrier" ::: "memory");
    if (MSL0) __builtin_amdgcn_s_sleep(MSL0);
    GLDH(cv[0], (unsigned)(rg * 2), c); GLDH(cv[1], (unsigned)(rg * 2), c + 128);
    GLD4(wv[0], (unsigned)((rg * 6144 + cg * 32 + (t & 3) * 8) * 2), wmod); GLD4(wv[1], (unsigned)((rg * 6144 + cg * 32 + (t & 3) * 8) * 2), wmod + 128 * 6144);
    if (MBAR & 2) asm volatile("s_barrier" ::: "memory");
    if (MSL) __builtin_amdgcn_s_sleep(MSL);
#pragma unroll
    for (int n = 0; n < 3; ++n) {
        const int id = n * 256 + i * 8 + w;
        GLD4(pf[n], id < 192 * MQ ? wpf_off(id, xcd, lane) : 0u, W);
    }
    asm volatile("s_waitcnt vmcnt(7)" : "+v"(v[0]), "+v"(v[1]) :: "memory");
    DIAG_T(0);
    {
        float f[16], s = 0.f;
#pragma unroll
        for (int j = 0; j < 8; ++j) { f[2 * j] = bflo(v[j >> 2][j & 3]); f[2 * j + 1] = bfhi(v[j >> 2][j & 3]); s += f[2 * j] + f[2 * j + 1]; }
        const float mean = wsum(s) * (1.f / D_);
        float q = 0.f;
#pragma unroll
        for (int j = 0; j < 16; ++j) { f[j] -= mean; q += f[j] * f[j]; }
        const float rstd = rsqrtf(wsum(q) * (1.f / D_) + LN_EPS);
#pragma unroll
        for (int j = 0; j < 2; ++j) {
            u32x4 o;
#pragma unroll
            for (int e = 0; e < 4; ++e) o[e] = pk2(f[8 * j + 2 * e] * rstd, f[8 * j + 2 * e + 1] * rstd);
            *(u32x4*)&xt[w][j * 256 + lane * 4] = o;
        }
        if (lane == 0) xt[w][512] = __float_as_uint(mean);
    }
    LDS_BAR();
#pragma unroll
    for (int j = 0; j < 2; ++j) {
        const unsigned* p = &xt[(t >> 4) & 1][j * 256 + (t >> 5) * 16 + (t & 15)];
        const u32x4 o = {p[0], p[2 * 528], p[4 * 528], p[6 * 528]};
        asm volatile("global_store_dwordx4 %0, %1, %2 " MSC1 "\n\ts_nop 1" :: "v"((unsigned)((t >> 5) * 8192 + (t & 31) * 16)), "v"(o), "s"(xn + (size_t)(b >> 4) * 262144 + (b & 15) * 512 + j * 131072) : "memory");
    }
    DIAG_T(1);
    asm volatile("s_waitcnt vmcnt(7)" : "+v"(cv[0]), "+v"(cv[1]) :: "memory");
    float sc[2];
#pragma unroll
    for (int j = 0; j < 2; ++j) { const float cf = bflo(cv[j]); sc[j] = rbf(cf / (1.f + __expf(-cf))); }
    asm volatile("s_waitcnt vmcnt(5)" : "+v"(wv[0]), "+v"(wv[1]) :: "memory");
    {
        float acc[8];
#pragma unroll
        for (int j = 0; j < 4; ++j) { acc[2 * j] = sc[0] * bflo(wv[0][j]) + sc[1] * bflo(wv[1][j]); acc[2 * j + 1] = sc[0] * bfhi(wv[0][j]) + sc[1] * bfhi(wv[1][j]); }
#pragma unroll
        for (int j = 0; j < 4; ++j) acc[j] = sw32sum(acc[j], acc[4 + j]);
        f32x2 z;
#pragma unroll
        for (int j = 0; j < 2; ++j) {
            float q = sw16sum(acc[j], acc[2 + j]);
            q += dpp<ROR4>(q); q += dpp<ROR8>(q);
            z[j] = q;
        }
        if ((lane & 12) == 0) *(f32x2*)&red[w][(lane & 3) * 8 + (lane >> 5) * 4 + ((lane >> 4) & 1) * 2] = z;
    }
    LDS_BAR();
    if (t < 32) {
        modp[ks * 2048 + cg * 32 + t] = ((red[0][t] + red[1][t]) + (red[2][t] + red[3][t])) + ((red[4][t] + red[5][t]) + (red[6][t] + red[7][t]));
        ((u32x4*)gm)[b * 32 + t] = u32x4{};
    }
    if (lane == 0) mean1[row] = __uint_as_float(xt[w][512]);
    if (cg == 0 && (t & 3) == 0) {
#pragma unroll
        for (int j = 0; j < 2; ++j) scb[128 * j + (rg & ~31) + ((rg >> 2) & 3) * 8 + ((rg >> 4) & 1) * 4 + (rg & 3)] = (u16)(__float_as_uint(sc[j]) >> 16);
    }
    DIAG_T(2);
    asm volatile("s_waitcnt vmcnt(0)" : "+v"(pf[0]), "+v"(pf[1]), "+v"(pf[2]) :: "memory");
    DIAG_END(0);
}

#define QSC1 "sc1"
#define QABL 0
#define QSLP 3
#define QUN 1
#define QPK 0
#define QPRIO 0
typedef volatile LAS unsigned* lds_cnt;
constexpr int QA_SLOT = 8704, QB_SLOT = 12288, QB_OFF = 5 * QA_SLOT, QG_OFF = QB_OFF + 9 * QB_SLOT, QK_CNT = QG_OFF + 8192 + 1024, QK_LDS = QK_CNT + 64;
__global__ __launch_bounds__(768, 3) void k_qkv(const char* __restrict__ xn, const float* __restrict__ mod,
                                                const u16* __restrict__ W, u16* __restrict__ Qf, u16* __restrict__ KVf,
                                                float* __restrict__ kn2 DIAG_ARG) {
    DIAG_BEGIN
    __shared__ __attribute__((aligned(16))) char lds[QK_LDS];
    const int b = blockIdx.x, tid = threadIdx.x, lane = tid & 63;
    const int w = __builtin_amdgcn_readfirstlane(tid >> 6);
    const int xcd = b & 7, idx = b >> 3;
    const int rp = (xcd & 1) * 8 + (idx & 7), cp = (xcd >> 1) * 4 + (idx >> 3);
    const unsigned lds0 = (unsigned)(uintptr_t)lds;
    const lds_cnt cnt = (lds_cnt)((lds_ptr)lds + QK_CNT);
#define Q_SPIN(f, p, need) do { while (__any((int)(f) < (need))) { __builtin_amdgcn_s_sleep(QSLP); f = *(p); } asm volatile("" ::: "memory"); } while (0)
    if (w >= 4) {
        const int pw = w - 4, par = pw >> 2;
        unsigned boff = (unsigned)(((pw * 8 + (lane >> 3)) * 3072 + cp * 64 + ((lane & 7) ^ ((lane >> 2) & 4)) * 8) * 2);
        const char* wv = (const char*)W + 3072;
        unsigned dst = lds0 + QB_OFF + par * QB_SLOT + (pw & 3) * 3072;
        const unsigned dend = lds0 + QB_OFF + 9 * QB_SLOT;
#define QB_M0() asm volatile("s_mov_b32 m0, %0\n\ts_nop 0" :: "s"(dst) : "memory")
#define QB_Q() asm volatile("global_load_lds_dwordx4 %0, %1" :: "v"(boff), "s"(W) : "memory")
#define QB_K() asm volatile("global_load_lds_dwordx4 %0, %1 offset:2048" :: "v"(boff), "s"(W) : "memory")
#define QB_V() asm volatile("global_load_lds_dwordx4 %0, %1 offset:1024" :: "v"(boff), "s"(wv) : "memory")
#define QB_NEXT() do { boff += bst; dst += 2 * QB_SLOT; if (dst >= dend) dst -= 9 * QB_SLOT; } while (0)
        unsigned xoff = (unsigned)(rp * 262144 + pw * 2048 + lane * 16), xst = (QABL & 4) ? 0 : 16384, bst = (QABL & 1) ? 0 : 64 * 3072 * 2;
        u32x4 xs[4][2];
#define QX_0(q) asm volatile("global_load_dwordx4 %0, %1, %2" : "=v"(xs[q][0]) : "v"(xoff), "s"(xn) : "memory")
#define QX_1(q) do { asm volatile("global_load_dwordx4 %0, %1, %2 offset:1024" : "=v"(xs[q][1]) : "v"(xoff), "s"(xn) : "memory"); xoff += xst; } while (0)
        QX_0(0); QX_1(0); QB_M0(); QB_Q(); QB_K(); QB_V(); QB_NEXT(); QX_0(1); QX_1(1); QX_0(2); QX_1(2); QB_M0(); QB_Q(); QB_K(); QB_V(); QB_NEXT(); QX_0(3); QX_1(3);
        lds_cptr gp = (lds_cptr)lds + QG_OFF + par * 256 + (lane & 15) * 16;
        unsigned ad = lds0 + par * QA_SLOT + (pw & 3) * 2176;
        const lds_cnt fr = cnt + (lane & 3);
        if (QPRIO == 1 || (QPRIO == 2 && par)) __builtin_amdgcn_s_setprio(1);
        asm volatile("s_barrier" ::: "memory");
        f32x4 g2[2] = {*(const LAS f32x4*)gp, *(const LAS f32x4*)(gp + 512)};
        unsigned o[8], f = 0, fl = par;
        lds_cnt cw = cnt + w;
        asm volatile("" : "+v"(cw));
#define QA_M2(q, i) do { float t0, t1;                                                                            \
            if (QABL & 32) { o[i] = xs[q][(i) >> 2][(i) & 3]; o[(i) + 1] = xs[q][(i) >> 2][((i) & 3) + 1]; } else \
            asm volatile("v_lshlrev_b32 %0, 16, %4\n\tv_and_b32 %2, 0xffff0000, %4\n\tv_lshlrev_b32 %1, 16, %5\n\tv_and_b32 %3, 0xffff0000, %5\n\t"      \
                         "v_fma_f32 %0, %0, %6, %8\n\tv_fma_f32 %2, %2, %7, %9\n\tv_fma_f32 %1, %1, %6, %8\n\tv_fma_f32 %3, %3, %7, %9\n\t"              \
                         "v_cvt_pk_bf16_f32 %0, %0, %2\n\tv_cvt_pk_bf16_f32 %1, %1, %3"                           \
                         : "=&v"(o[i]), "=&v"(o[(i) + 1]), "=&v"(t0), "=&v"(t1)                                   \
                         : "v"(xs[q][(i) >> 2][(i) & 3]), "v"(xs[q][(i) >> 2][((i) & 3) + 1]), "v"(g2[(q) & 1][0]), "v"(g2[(q) & 1][1]), "v"(g2[(q) & 1][2]), "v"(g2[(q) & 1][3])); } while (0)
        asm volatile("s_waitcnt vmcnt(12)" : "+v"(xs[0][0]), "+v"(xs[0][1]) :: "memory");
#pragma unroll
        for (int i = 0; i < 8; i += 2) QA_M2(0, i);
        DIAG_A0
#pragma unroll 1
        for (int it = 0; it < 16; it += 4) {
            if (it == 12 || (QABL & 16)) { xoff = 0; xst = 0; }
#pragma unroll
            for (int q = 0; q < 4; ++q) {
                const int u = 2 * (it + q) + par, n = (q + 1) & 3;
                asm volatile(".Lqp%=:\n\tv_cmp_gt_i32 vcc, %16, %4\n\ts_cbranch_vccz .Lqq%=\n\ts_sleep %18\n\tds_read_b32 %4, %17\n\ts_waitcnt lgkmcnt(0)\n\ts_branch .Lqp%=\n.Lqq%=:\n\t"
                             "s_mov_b32 m0, %5\n\tv_add_u32 %0, 2, %0\n\t"
                             "ds_write_addtid_b32 %8\n\tds_write_addtid_b32 %9 offset:272\n\tds_write_addtid_b32 %10 offset:544\n\tds_write_addtid_b32 %11 offset:816\n\t"
                             "ds_write_addtid_b32 %12 offset:1088\n\tds_write_addtid_b32 %13 offset:1360\n\tds_write_addtid_b32 %14 offset:1632\n\tds_write_addtid_b32 %15 offset:1904\n\t"
                             "s_mov_b32 m0, %6\n\ts_waitcnt vmcnt(7)\n\tds_write_b32 %7, %0"
                             : "+v"(fl), "+v"(xs[n][0]), "+v"(xs[n][1]), "+v"(g2[n & 1]), "+v"(f)
                             : "s"(ad), "s"(dst), "v"(cw), "v"(o[0]), "v"(o[1]), "v"(o[2]), "v"(o[3]), "v"(o[4]), "v"(o[5]), "v"(o[6]), "v"(o[7]),
                               "s"(u - 4), "v"(fr), "n"(QSLP) : "vcc", "memory");
                if (QPRIO == 4) { if (par ^ (q & 1)) __builtin_amdgcn_s_setprio(1); else __builtin_amdgcn_s_setprio(0); }
                DIAG_A(5);
                SBAR(); QB_Q();
                SBAR(); QA_M2(n, 0); SBAR(); QB_K();
                SBAR(); QA_M2(n, 2); SBAR(); QB_V();
                SBAR(); QA_M2(n, 4); SBAR(); QX_0(q);
                g2[q & 1] = *(const LAS f32x4*)(gp + (q + 2) * 512);
                f = *fr;
                SBAR(); QA_M2(n, 6); SBAR(); QX_1(q);
                if (q == 1 && it == 12) { boff = 0; bst = 0; }
                QB_NEXT();
                ad += 2 * QA_SLOT; if (ad >= lds0 + 5 * QA_SLOT) ad -= 5 * QA_SLOT;
                DIAG_A(6);
            }
            gp += 2048;
        }
        cnt[w] = 64;
        WAIT_VM(0);
#pragma unroll
        for (int q = 0; q < 4; ++q) asm volatile("" :: "v"(xs[q][0]), "v"(xs[q][1]));
    } else {
        const int rh = w & 1, hd = w >> 1, G = lane >> 4, hh = lane >> 5;
        lds_cptr a_ = (lds_cptr)lds + (rh * 16 + ((lane & 31) >> 2)) * 272 + (lane & 3) * 64 + hh * 16;
        lds_cptr b_ = (lds_cptr)lds + QB_OFF + hh * 3072 + ((lane & 15) >> 2) * 128 + ((hd ^ (lane >> 3)) & 1) * 64 + (G & 1) * 32 + (lane & 3) * 8;
        f32x16 acc[2][3];
#pragma unroll
        for (int i = 0; i < 2; ++i)
#pragma unroll
            for (int j = 0; j < 3; ++j) acc[i][j] = f32x16{};
        bf16x8 a0[2], b0[3], a1[2], b1[3];
#define QC_RB(bf, j, s) do { if (!(QABL & 2)) bf[j] = cat4(trread(b_ + (2 * (j) % 3) * 1024 + (s) * 6144), trread(b_ + (2 * (j) % 3) * 1024 + (s) * 6144 + 512)); } while (0)
#define QC_STEP(af, bf, an, bn, s, E2, E3, E5) do {                                                               \
            acc[0][0] = MFMA32(bf[0], af[0], acc[0][0]); SBAR();                                                  \
            if (!(QABL & 2)) { an[0] = ldsfrag(a_ + (s) * 32); an[1] = ldsfrag(a_ + (s) * 32 + 2176); } SBAR();   \
            acc[0][1] = MFMA32(bf[1], af[0], acc[0][1]); SBAR();                                                  \
            QC_RB(bn, 0, s); E2; SBAR();                                                                          \
            acc[0][2] = MFMA32(af[0], bf[2], acc[0][2]); SBAR();                                                  \
            QC_RB(bn, 1, s); E3; SBAR();                                                                          \
            acc[1][0] = MFMA32(bf[0], af[1], acc[1][0]); SBAR();                                                  \
            QC_RB(bn, 2, s); SBAR();                                                                              \
            acc[1][1] = MFMA32(bf[1], af[1], acc[1][1]); SBAR();                                                  \
            asm volatile("" ::: "memory"); E5; asm volatile("" ::: "memory"); SBAR();                             \
            acc[1][2] = MFMA32(af[1], bf[2], acc[1][2]); SBAR();                                                  \
        } while (0)
        {
            f32x4 p[8];
#pragma unroll
            for (int j = 0; j < 8; ++j) GLD4(p[j], (unsigned)(tid * 16), mod + j * 1024);
            if (tid < 16) cnt[tid] = tid >> 3;
            asm volatile("" : "+v"(acc[0][0]), "+v"(acc[0][1]), "+v"(acc[0][2]), "+v"(acc[1][0]), "+v"(acc[1][1]), "+v"(acc[1][2]));
            asm volatile("s_waitcnt vmcnt(0)" : "+v"(p[0]), "+v"(p[1]), "+v"(p[2]), "+v"(p[3]), "+v"(p[4]), "+v"(p[5]), "+v"(p[6]), "+v"(p[7]) :: "memory");
            DIAG_T(2);
            f32x4 sa = (p[0] + p[2]) + (p[4] + p[6]), g = (p[1] + p[3]) + (p[5] + p[7]);
#pragma unroll
            for (int e = 0; e < 4; ++e) { sa[e] = rbf(sa[e]); g[e] = rbf(1.f + rbf(g[e])); }
            *(LAS f32x4*)((lds_ptr)lds + QG_OFF + tid * 32) = f32x4{g[0], g[1], sa[0], sa[1]};
            *(LAS f32x4*)((lds_ptr)lds + QG_OFF + tid * 32 + 16) = f32x4{g[2], g[3], sa[2], sa[3]};
        }
        LDS_BAR();
        DIAG_T(3);
        const lds_cnt rd = cnt + 4 + (lane & 7);
        if (QPRIO >= 2) __builtin_amdgcn_s_setprio(3);
        unsigned f = *rd;
        Q_SPIN(f, rd, 1);
        DIAG_T(0);
        int as = 1, bs = 1, da = QA_SLOT, db = QB_SLOT;
        unsigned un = 1;
        unsigned qcn = 0;
        a0[0] = ldsfrag(a_); a0[1] = ldsfrag(a_ + 2176);
#pragma unroll
        for (int j = 0; j < 3; ++j) b0[j] = cat4(trread(b_ + (2 * j % 3) * 1024), trread(b_ + (2 * j % 3) * 1024 + 512));
        if (QABL & 2) { a1[0] = a0[0]; a1[1] = a0[1]; b1[0] = b0[0]; b1[1] = b0[1]; b1[2] = b0[2]; }
        f = *rd;
        SBAR(); __builtin_amdgcn_s_waitcnt(0xC07F); SBAR();
        DIAG_A0
#pragma unroll 2
        for (int u = 0; u < 32; ++u) {
            QC_STEP(a0, b0, a1, b1, 1, a_ += da; asm volatile("" : "+v"(a_)), asm volatile("" : "+v"(un)), cnt[w] = un; b_ += db; asm volatile("" : "+v"(b_)));
            SBAR();
            asm volatile(".Lqc%=:\n\tv_cmp_gt_i32 vcc, %2, %0\n\ts_cbranch_vccz .Lqd%=\n\tds_read_b32 %0, %1\n\ts_waitcnt lgkmcnt(0)\n\ts_branch .Lqc%=\n.Lqd%=:"
                         : "+v"(f) : "v"(rd), "s"(u + 2) : "vcc", "memory");
            SBAR();
            QC_STEP(a1, b1, a0, b0, 0, da = QA_SLOT; if (++as == 5) { as = 0; da = -4 * QA_SLOT; } asm volatile("" : "+s"(da), "+s"(as)),
                    db = QB_SLOT; if (++bs == 9) { bs = 0; db = -8 * QB_SLOT; } asm volatile("" : "+s"(db), "+s"(bs)), f = *rd; ++un);
        }
        DIAG_T(1);
        const int head = cp * 2 + hd;
        int ln = __builtin_amdgcn_mbcnt_hi(~0u, __builtin_amdgcn_mbcnt_lo(~0u, 0u));
        asm volatile("" : "+v"(ln));
#pragma unroll
        for (int i = 0; i < 2; ++i) {
            const int tile = rp * 4 + rh * 2 + i;
            u32x4* qo = (u32x4*)((char*)Qf + ((size_t)(head * 64 + tile) * 2) * 1024) + ln;
            u32x4* kvo = (u32x4*)((char*)KVf + ((size_t)(head * 64 + tile) * 4) * 1024) + ln;
            float n2a = 0.f, n2b = 0.f;
#pragma unroll
            for (int s = 0; s < 2; ++s) {
                u32x4 q, k, v;
#pragma unroll
                for (int j = 0; j < 4; ++j) {
                    const float k0 = acc[i][1][8 * s + 2 * j], k1 = acc[i][1][8 * s + 2 * j + 1];
                    q[j] = pk2(acc[i][0][8 * s + 2 * j] * QSCALE, acc[i][0][8 * s + 2 * j + 1] * QSCALE);
                    k[j] = pk2(k0, k1);
                    v[j] = pk2(acc[i][2][8 * s + 2 * j], acc[i][2][8 * s + 2 * j + 1]);
                    n2a += k0 * k0; n2b += k1 * k1;
                }
                {
                    const char* qb = (const char*)Qf + ((size_t)(head * 64 + tile) * 2 + s) * 1024;
                    const char* kb = (const char*)KVf + ((size_t)(head * 64 + tile) * 4 + s) * 1024;
                    asm volatile("global_store_dwordx4 %0, %1, %2 " QSC1 "\n\ts_nop 1" :: "v"(ln * 16), "v"(q), "s"(qb) : "memory");
                    asm volatile("global_store_dwordx4 %0, %1, %2 " QSC1 "\n\ts_nop 1" :: "v"(ln * 16), "v"(k), "s"(kb) : "memory");
                    asm volatile("global_store_dwordx4 %0, %1, %2 offset:2048 " QSC1 "\n\ts_nop 1" :: "v"(ln * 16), "v"(v), "s"(kb) : "memory");
                }
            }
            float n2 = n2a + n2b;
            n2 = wmax(sw32sum(n2, n2));
            if (ln == 0) kn2[head * 64 + tile] = n2;
        }
    }
    DIAG_END(2);
}

constexpr int AT_SLOT = 32768, AT_STG = 4 * AT_SLOT, AT_RED = AT_STG + 4 * 4224, AT_DUM = AT_RED + 1024, AT_CNT = AT_DUM + 1024, AT_LDS = AT_CNT + 64;
#define HABL 0
#define HSL0 16
#define HP 8
#define ABAL 0
#define HCUT 9
#define HW0 3
#define HP4 4
#define LP 3
#define HPRIO 0
#define LPRIO 3
#define GST4W GST4C
DI unsigned cnt_wait(lds_cnt p, unsigned target) {
    unsigned v;
    do { v = *p; } while (__any((v & 0xffffu) < target));
    asm volatile("" ::: "memory");
    return v;
}
__global__ __launch_bounds__(832) void k_attn(const u16* __restrict__ Qf, const u16* __restrict__ KVf, const float* __restrict__ kn2,
                                                 u16* __restrict__ Of, const u16* __restrict__ wg, const u16* __restrict__ wu,
                                                 const u16* __restrict__ wd, char* __restrict__ wt, const u16* __restrict__ wmod,
                                                 const u16* __restrict__ scb, const u16* __restrict__ wr, float* __restrict__ mod,
                                                 const u16* __restrict__ Wo, char* __restrict__ gran, char* __restrict__ gm DIAG_ARG) {
    DIAG_BEGIN
    __shared__ __attribute__((aligned(16))) char lds[AT_LDS];
    const int b = blockIdx.x, tid = threadIdx.x, lane = tid & 63;
    const int w = __builtin_amdgcn_readfirstlane(tid >> 6);
    const unsigned lds0 = (unsigned)(uintptr_t)lds;
    const lds_cnt cnt = (lds_cnt)((lds_ptr)lds + AT_CNT);
    const int head = (b & 7) * 4 + ((b >> 3) & 3);
    const char* kvbase = (const char*)KVf + (size_t)head * 262144;
    const int qt = (b >> 5) * 8 + (w & 7);
    const unsigned voff = (unsigned)(w * 4096 + lane * 16);
#define AT_LOAD(c)                                                                  \
    do {                                                                            \
        const char* g_ = kvbase + (c) * AT_SLOT;                                    \
        const unsigned d_ = lds0 + ((c) & 3) * AT_SLOT + w * 4096;                  \
        glds16(g_, voff, d_); glds16(g_ + 1024, voff, d_ + 1024);                   \
        glds16(g_ + 2048, voff, d_ + 2048); glds16(g_ + 3072, voff, d_ + 3072);     \
    } while (0)
    u32x4 qa, qb; float kmx;
    if (w < 8) {
        const char* qp = (const char*)Qf + (size_t)(head * 64 + qt) * 2048;
        GLD4(qa, (unsigned)(lane * 16), qp); GLD4(qb, (unsigned)(lane * 16), qp + 1024); GLD1(kmx, (unsigned)(lane * 4), kn2 + head * 64);
        AT_LOAD(0);
    } else if (tid < 528) cnt[lane] = 0;
    LDS_BAR();
    if (w == 12) {
        __builtin_amdgcn_s_setprio(LPRIO);
#pragma unroll 1
        for (int c = 1; c < 8; ++c) {
            if (c > 3) while (__any(cnt[lane & 7] < (unsigned)(c - 1))) __builtin_amdgcn_s_sleep(2);
#pragma unroll 1
            for (int i = 0; i < 32; ++i) glds16(kvbase + c * AT_SLOT + i * 1024, lane * 16, lds0 + (c & 3) * AT_SLOT + i * 1024);
            WAIT_VM(32);
            cnt[12] = c;
        }
        WAIT_VM(0);
        cnt[12] = 8;
        const char* po = (const char*)Wo + (size_t)(b >> 3) * 65536 + (b & 1) * 1024;
#pragma unroll 1
        for (int i = 0; i < 32; ++i) { __builtin_amdgcn_s_sleep(LP); if (!(HABL & 2)) glds16(po + i * 2048, lane * 16, lds0 + AT_DUM); }
        WAIT_VM(0);
        return;
    }
    if (w >= 8) {
        const int hw = w - 8;
        if (HABL & 1) return;
        __builtin_amdgcn_s_setprio(HPRIO);
        if (HABL & 64) {
#pragma unroll 1
            for (int i = 0; i < 70; ++i) __builtin_amdgcn_s_sleep(HP);
            return;
        }
        if (HABL & 128) {
#pragma unroll 1
            for (int i = 0; i < 5; ++i) __builtin_amdgcn_s_sleep(112);
            return;
        }
        const lds_ptr sp = (lds_ptr)lds + AT_STG + hw * 4224;
        const int fb = b < 32 ? b : b - 32;
        const unsigned l16 = lane * 16;
        const unsigned goff = (unsigned)(((lane >> 3) * F_ + (lane & 7) * 8) * 2), moff = (unsigned)(((lane >> 3) * 6144 + (lane & 7) * 8) * 2);
        const unsigned toff = (unsigned)(((lane >> 1) & 3) * 65536 + (lane >> 3) * 32 + (lane & 1) * 16);
        const char* pm = (const char*)wmod + ((size_t)((b & 3) * 256 + hw * 64) * 6144 + 2048 + (b >> 2) * 64) * 2;
        const size_t go = ((size_t)((fb & 3) * 256 + hw * 64) * F_ + (fb >> 2) * 64) * 2;
        const char* pg = (const char*)wg + go;
        const char* pu = (const char*)wu + go;
        char* o = wt + (size_t)(fb >> 2) * 262144 + ((fb & 3) * 2 + (hw >> 1)) * 8192 + (hw & 1) * 2048;
        u32x4 af[2], ra[8], rb[8]; unsigned wrb; u32x2 gv;
#define PACE() __builtin_amdgcn_s_sleep(HP)
#define H_LD1(r, j, h) GLD4(r[j], (HABL & 16) ? 0u : goff, ((j) & 1 ? pu : pg) + (size_t)((h) * 4 + ((j) >> 1)) * 8 * F_ * 2)
#define H_ST1(r, j, h, N) do { asm volatile("s_waitcnt vmcnt(" #N ")" : "+v"(r[j]) :: "memory"); if (HABL & 8) glds16((const char*)scb, 0u, lds0 + AT_STG + hw * 4224); else GST4W(toff, r[j], o + ((j) & 1) * 4096 + ((h) * 4 + ((j) >> 1)) * 256); } while (0)
#define H_WAIT(r, N) asm volatile("s_waitcnt vmcnt(" #N ")" : "+v"(r[0]), "+v"(r[1]), "+v"(r[2]), "+v"(r[3]), "+v"(r[4]), "+v"(r[5]), "+v"(r[6]), "+v"(r[7]) :: "memory")
        __builtin_amdgcn_s_sleep(HSL0);
        {
            const u32x4 z = u32x4{};
            const unsigned zo = (unsigned)((b * 256 + tid - 512) * 16);
            GST4W(zo, z, gran); PACE(); GST4W(zo, z, gran + (1 << 20)); PACE();
            if (zo + (2u << 20) < (unsigned)G_END) GST4W(zo, z, gran + (2 << 20));
        }
        if (HW0) while (cnt[12] < HW0) __builtin_amdgcn_s_sleep(2);
#pragma unroll
        for (int q = 0; q < 2; ++q) GLD4(af[q], (unsigned)((lane >> 4) * 16), scb + (b & 3) * 256 + hw * 64 + q * 32);
        GLDH(wrb, (unsigned)(((b * 16 + (lane & 15)) & 1023) * 2), wr);
#pragma unroll
        for (int q = 0; q < 8; ++q) { GLD4(ra[q], (HABL & 32) ? 0u : moff, pm + (size_t)q * 8 * 6144 * 2); PACE(); }
#pragma unroll
        for (int j = 0; j < 8; ++j) { H_LD1(rb, j, 0); PACE(); }
        DIAG_T(0);
        if (HCUT == 0) { WAIT_VM(0); return; }
        H_WAIT(ra, 8);
        DIAG_T(1);
        if (HCUT == 1) { WAIT_VM(0); return; }
        asm volatile("" : "+v"(wrb), "+v"(af[0]), "+v"(af[1]));
        {
            f32x4 m[4] = {f32x4{}, f32x4{}, f32x4{}, f32x4{}};
            const lds_ptr wp = sp + ((lane >> 1) & 3) * 1056 + (lane >> 3) * 32 + (lane & 1) * 16;
#pragma unroll
            for (int h = 0; h < 2; ++h) {
#pragma unroll
                for (int q = 0; q < 4; ++q) *(LAS u32x4*)(wp + q * 256) = ra[4 * h + q];
#pragma unroll
                for (int q = 0; q < 4; ++q)
                    m[q] = MFMA16(__builtin_bit_cast(bf16x8, af[h]), cat4(trread(sp + q * 1056 + lane * 8), trread(sp + q * 1056 + 512 + lane * 8)), m[q]);
                asm volatile("" ::: "memory");
            }
            const float lo = (lane & 16) ? m[1][0] : m[0][0], hi = (lane & 16) ? m[3][0] : m[2][0];
            *(LAS float*)((lds_ptr)lds + AT_RED + hw * 256 + lane * 4) = (lane & 32) ? hi : lo;
            cnt[w] = 1;
        }
        DIAG_T(2);
        if (HCUT == 2) { WAIT_VM(0); return; }
#define H_P3(j, N) do { H_LD1(ra, j, 1); PACE(); H_ST1(rb, j, 0, N); PACE(); } while (0)
        H_P3(0, 8); H_P3(1, 9);
        if (hw == 0) {
            cnt_wait(cnt + 8 + (lane & 3), 1);
            const LAS float* p = (const LAS float*)((lds_cptr)lds + AT_RED) + lane;
            const u32x2 v = {__float_as_uint((p[0] + p[64]) + (p[128] + p[192])), TAG};
            GST2C((unsigned)(lane * 8), v, gm + b * 512);
        }
        H_P3(2, 10); H_P3(3, 11); H_P3(4, 12); H_P3(5, 13); H_P3(6, 14); H_P3(7, 15);
        DIAG_T(3);
        if (HCUT == 3) { WAIT_VM(0); return; }
        const char* pd = (const char*)wd + (size_t)fb * 32768 + hw * 8192;
#define H_P4(j) do { glds16((HABL & 4) ? (const char*)scb : pd + (j) * 1024, (HABL & 4) ? 0u : l16, lds0 + AT_STG + hw * 4224); __builtin_amdgcn_s_sleep(HP4); H_ST1(ra, j, 1, 16); __builtin_amdgcn_s_sleep(HP4); } while (0)
        H_P4(0); H_P4(1); H_P4(2); H_P4(3); H_P4(4); H_P4(5); H_P4(6); H_P4(7);
        DIAG_T(4);
        if (HCUT == 4) { WAIT_VM(0); return; }
        const unsigned gmo = (unsigned)((lane >> 4) * 512 + ((b & 3) * 16 + (lane & 15)) * 8);
        GLD2C(gv, gmo, gm + (b >> 2) * 2048);
        DIAG_T(5);
        if (HCUT == 5) { WAIT_VM(0); return; }
        asm volatile("s_waitcnt vmcnt(0)" : "+v"(gv) :: "memory");
        if (hw == 0) {
#pragma unroll 1
            for (int spin = 0; spin < SPIN_MAX && !__all(gv[1] == TAG); ++spin) {
                GLD2C(gv, gmo, gm + (b >> 2) * 2048);
                asm volatile("s_waitcnt vmcnt(0)" : "+v"(gv) :: "memory");
            }
            float s = __uint_as_float(gv[0]);
            s = sw16sum(s, s); s = rbf(sw32sum(s, s));
            if (lane < 16) {
                const int n = 2048 + b * 16 + lane, sec = n >> 10;
                if (sec == 4) s = rbf(1.f + s);
                mod[n] = s;
                if (sec == 4) mod[M_GW + (n & 1023)] = s * bflo(wrb);
                if (sec == 3) mod[M_SW + (n & 1023)] = s * bflo(wrb);
            }
        }
        return;
    }
    if (ABAL) { if (w < 4) __builtin_amdgcn_s_setprio(2); else __builtin_amdgcn_s_setprio(1); }
    asm volatile("s_waitcnt vmcnt(4)" : "+v"(qa), "+v"(qb), "+v"(kmx) :: "memory");
    const bf16x8 q0 = __builtin_bit_cast(bf16x8, qa), q1 = __builtin_bit_cast(bf16x8, qb);
    kmx = wmax(kmx);
    float qn = 0.f;
#pragma unroll
    for (int j = 0; j < 4; ++j) { const float a = bflo(qa[j]), c = bfhi(qa[j]), d = bflo(qb[j]), e = bfhi(qb[j]); qn += (a * a + c * c) + (d * d + e * e); }
    qn = sw32sum(qn, qn);
    float mref = sqrtf(qn * kmx) * 1.01f;
    bf16x8 ones;
    { const short o = (((lane >> 4) & 1) == (((lane & 15) >> 2) & 1)) ? (short)0x3f80 : (short)0;
#pragma unroll
      for (int j = 0; j < 8; ++j) ones[j] = o; }
    const lds_cptr fp = (lds_cptr)lds + lane * 16;
    const lds_cnt call = cnt + (lane & 7);
    unsigned target = 0;
    for (int attempt = 0; attempt < 2; ++attempt) {
        f32x16 negm;
#pragma unroll
        for (int i = 0; i < 16; ++i) negm[i] = -mref;
        f32x16 o = f32x16{}; f32x4 ls = f32x4{};
        f32x16 sA, sB;
#define AT_S(dst, p_) do { const bf16x8 k0_ = ldsfrag(p_), k1_ = ldsfrag((p_) + 1024); dst = MFMA32(k0_, q0, negm); dst = MFMA32(k1_, q1, dst); } while (0)
#define AT_PV(src, p_) do {                                                                             \
            const bf16x8 v0_ = ldsfrag((p_) + 2048), v1_ = ldsfrag((p_) + 3072);                        \
            u32x4 p0_, p1_;                                                                             \
            _Pragma("unroll") for (int j = 0; j < 4; ++j) {                                             \
                p0_[j] = pk2(__builtin_amdgcn_exp2f(src[2 * j]), __builtin_amdgcn_exp2f(src[2 * j + 1]));         \
                p1_[j] = pk2(__builtin_amdgcn_exp2f(src[8 + 2 * j]), __builtin_amdgcn_exp2f(src[9 + 2 * j])); }   \
            const bf16x8 pa_ = __builtin_bit_cast(bf16x8, p0_), pb_ = __builtin_bit_cast(bf16x8, p1_);  \
            o = MFMA32(v0_, pa_, o); ls = MFMA16(ones, pa_, ls);                                        \
            o = MFMA32(v1_, pb_, o); ls = MFMA16(ones, pb_, ls);                                        \
        } while (0)
        if (attempt) { WAIT_VM(8); } else { WAIT_VM(0); }
        cnt[w] = ++target;
        cnt_wait(call, target);
        DIAG_T(0);
        AT_S(sA, fp);
        for (int c = 0; c < 8; ++c) {
            if (attempt && c < 6) { WAIT_VM(4); } else { WAIT_VM(0); }
            cnt[w] = ++target;
            const lds_cptr p = fp + (c & 3) * AT_SLOT;
            const lds_cptr pn = fp + ((c + 1) & 3) * AT_SLOT;
            const bool own = attempt;
            const lds_cnt cw = own ? call : cnt + 12;
            const unsigned need = own ? target : (c < 7 ? c + 2 : 0);
            unsigned cv;
#pragma unroll
            for (int t = 0; t < 8; t += 2) {
                if (t == 2) cv = *cw;
                if (t == 4) {
                    if (__any((cv & 0xffffu) < need)) cnt_wait(cw, need);
                    asm volatile("" ::: "memory");
                    if (attempt && c + 3 < 8) AT_LOAD(c + 3);
                    if (ABAL && c == ABAL && w < 4) __builtin_amdgcn_s_setprio(0);
                }
                AT_S(sB, p + (t + 1) * 4096);
                AT_PV(sA, p + t * 4096);
                if (t + 2 < 8) AT_S(sA, p + (t + 2) * 4096); else AT_S(sA, pn);
                AT_PV(sB, p + (t + 1) * 4096);
            }
        }
        DIAG_T(1);
        const bool bad = !(ls[0] >= 8.6736174e-19f);
        ++target;
        cnt[w] = target + (__any(bad) ? 0x10000u : 0u);
        const float rl = 1.f / ls[0];
        u32x4* op = (u32x4*)((char*)Of + ((size_t)(qt * 32 + head) * 2) * 1024) + lane;
#pragma unroll
        for (int s = 0; s < 2; ++s) {
            u32x4 v;
#pragma unroll
            for (int j = 0; j < 4; ++j) v[j] = pk2(o[8 * s + 2 * j] * rl, o[8 * s + 2 * j + 1] * rl);
            op[s * 64] = v;
        }
        if (attempt == 1 || !__any(cnt_wait(call, target) != target)) break;
        float mx = -INFINITY;
        const bf16x8* kp = (const bf16x8*)kvbase + lane;
        for (int t = 0; t < 64; ++t) {
            const bf16x8 k0 = kp[t * 256], k1 = kp[t * 256 + 64];
            f32x16 s = MFMA32(k0, q0, f32x16{}); s = MFMA32(k1, q1, s);
#pragma unroll
            for (int i = 0; i < 16; ++i) mx = fmaxf(mx, s[i]);
        }
        mx = fmaxf(mx, xor32(mx));
        mref = mx;
        WAIT_VM(0);
        AT_LOAD(0); AT_LOAD(1); AT_LOAD(2);
    }
    DIAG_END(3);
}

constexpr int OS = 24576, OV = 6 * OS, OST = OV + 3072, OBN = OST + 3072, OPT = OBN + 2048, OGS = OPT + 1024, O_LDS = OGS + 128;
#define OMAP 0
#define OROT 1
#define FF0 4
#define OSL3 16
#define OSLP 24
#define OSL2 0
#define OSPEC 1
#define OABL 0
constexpr int XOR7 = 0x141;
DI float oct_sum(float v) { v += dpp<XOR1>(v); v += dpp<XOR2>(v); v += dpp<XOR7>(v); return v; }
DI float dot2(unsigned a, unsigned b, float c) { return __builtin_amdgcn_fdot2_f32_bf16(__builtin_bit_cast(bf16x2_t, a), __builtin_bit_cast(bf16x2_t, b), c, false); }
__global__ __launch_bounds__(768, 3) void k_out(const u16* __restrict__ Of, const u16* __restrict__ Wo, const u16* __restrict__ x,
                                                const float* __restrict__ mean1, const float* __restrict__ mod,
                                                const char* __restrict__ wt, const u16* __restrict__ wd, char* __restrict__ gran,
                                                u16* __restrict__ out DIAG_ARG) {
    DIAG_BEGIN
    __shared__ __attribute__((aligned(16))) char lds[O_LDS];
    const int b = blockIdx.x, tid = threadIdx.x, lane = tid & 63;
    const int w = __builtin_amdgcn_readfirstlane(tid >> 6);
    const int xcd = b & 7, idx = b >> 3;
    const int rp = (xcd >> 1) * 8 + (idx & 7), cp = (xcd & 1) * 4 + (idx >> 3);
    const int mt = w & 1, nt = w >> 1, blk = lane >> 4, hh = lane >> 5;
    const unsigned lds0 = (unsigned)(uintptr_t)lds;
    const lds_ptr L = (lds_ptr)lds;
    if (w >= 8) {
        const int lw = w - 8;
        const char* pa = (const char*)Of + (size_t)(rp * 2 + (lw >> 1)) * 65536 + (lw & 1) * 2048;
        const char* pb = (const char*)Wo + ((size_t)lw * 16 * 1024 + cp * 128) * 2;
        const unsigned aoff = lane * 16;
        const unsigned boff = (unsigned)(((lane >> 3) * 1024 + ((lane & 7) ^ ((lane >> 2) & 4)) * 8) * 2);
        unsigned dst = lds0;
        int si = OROT * ((idx & 7) * 2 + (idx >> 3) * 4) & 15;
#define O_STAGE() do { const char* a_ = pa + si * 4096; const char* b_ = pb + (size_t)si * 131072; const unsigned d_ = dst + 8192 + lw * 4096;    \
                       glds16(a_, aoff, dst + lw * 2048); glds16(a_ + 1024, aoff, dst + lw * 2048 + 1024);                                        \
                       glds16(b_, boff, d_); glds16(b_ + 128, boff, d_ + 1024); glds16(b_ + 16384, boff, d_ + 2048); glds16(b_ + 16384 + 128, boff, d_ + 3072);  \
                       si = (si + 1) & 15; dst += OS; if (dst >= lds0 + OV) dst = lds0; } while (0)
        if (lw < 3) glds16(mod + M_GA + lw * 2048 + cp * 128, (unsigned)(hh * 4096 + (lane & 31) * 16), lds0 + OV + lw * 1024);
#pragma unroll 1
        for (int s = 0; s < 3; ++s) O_STAGE();
        WAIT_BAR(12);
#pragma unroll 1
        for (int s = 0; s < 2; ++s) O_STAGE();
        WAIT_BAR(18);
        O_STAGE();
#pragma unroll 1
        for (int s = 1; s < 11; ++s) { O_STAGE(); if (s < 10) WAIT_BAR(24); }
        WAIT_BAR(24); WAIT_BAR(18); WAIT_BAR(12); WAIT_BAR(6); WAIT_BAR(0);
        asm volatile("s_barrier" ::: "memory");
        DIAG_T(0);
        {
            u32x2 v[6];
            const char* gp = gran + (size_t)rp * 12288 + lw * 3072;
#pragma unroll 1
            for (int spin = 0; spin < SPIN_MAX; ++spin) {
#pragma unroll
                for (int i = 0; i < 6; ++i) GLD2C(v[i], (unsigned)(lane * 8), gp + i * 512);
                asm volatile("s_waitcnt vmcnt(0)" : "+v"(v[0]), "+v"(v[1]), "+v"(v[2]), "+v"(v[3]), "+v"(v[4]), "+v"(v[5]) :: "memory");
                if (__all((v[0][1] & v[1][1] & v[2][1] & v[3][1] & v[4][1] & v[5][1]) == TAG)) break;
            }
            DIAG_T(1);
            LAS u32x2* p = (LAS u32x2*)(L + 2 * OS + lane * 112 + lw * 8);
            p[0] = u32x2{v[0][0], v[3][0]}; p[4] = u32x2{v[1][0], v[4][0]}; p[8] = u32x2{v[2][0], v[5][0]};
        }
        LDS_BAR();
        DIAG_T(2);
        if (lw == 0) {
            const char* g2 = gran + G_H2;
            u32x2 v; u32x4 hv[4];
            int cur = 63; bool have = false;
            hv[0] = u32x4{}; hv[1] = u32x4{}; hv[2] = u32x4{}; hv[3] = u32x4{};
            __builtin_amdgcn_s_sleep(OSLP);
#pragma unroll 1
            for (int spin = 0; spin < SPIN_MAX; ++spin) {
                GLD2C(v, (unsigned)((lane & 31) * 128), g2 + 32 * 4096 + (b & (NREP - 1)) * 4096);
                if (!have && cur < 32) {
#pragma unroll
                    for (int i = 0; i < 4; ++i) GLD4C(hv[i], (unsigned)(lane * 16), g2 + cur * 4096 + i * 1024);
                }
                asm volatile("s_waitcnt vmcnt(0)" : "+v"(v), "+v"(hv[0]), "+v"(hv[1]), "+v"(hv[2]), "+v"(hv[3]) :: "memory");
                const bool ok = (v[1] & 0xffff0000u) == TAG;
                const float r = ok ? __uint_as_float(v[0]) : -INFINITY;
                const float wbest = wmax(r);
                const int win = (int)__builtin_ctzll(__ballot(ok && r == wbest) | (1ull << 63));
                have = win == cur && __all((hv[0][1] & hv[0][3] & hv[1][1] & hv[1][3] & hv[2][1] & hv[2][3] & hv[3][1] & hv[3][3]) == TAG);
                cur = __builtin_popcountll(__ballot(ok)) >= 2 * OSPEC ? win : 63;
                if (have && __all(ok)) break;
                if (OSL2) __builtin_amdgcn_s_sleep(OSL2);
            }
            if (lane == cur) *(LAS u32x2*)(L + OGS + 64) = u32x2{v[0], (unsigned)(cur * 64) + (v[1] & 63)};
            LAS u32x2* p = (LAS u32x2*)(L + OBN + (lane >> 3) * 64 + (lane & 3) * 16 + ((lane >> 2) & 1) * 8);
#pragma unroll
            for (int i = 0; i < 4; ++i) p[i * 64] = u32x2{hv[i][0], hv[i][2]};
        }
        DIAG_T(6);
        LDS_BAR();
        return;
    }
    const int row = rp * 64 + mt * 32 + (lane & 31);
    const unsigned xoff = (unsigned)((row * D_ + cp * 128 + nt * 32 + 8 * hh) * 2);
    u32x4 xi[2]; float ref; f32x2 gws, sws, gbv;
    const int ycol = w * 128 + (lane & 1) * 63 + lane;
    GLD4(xi[0], xoff, x); GLD4(xi[1], xoff, x + 16);
    GLD1(ref, (unsigned)(row * 4), mean1);
    GLD2(gws, (unsigned)(tid * 8), mod + M_GW); GLD2(sws, (unsigned)(tid * 8), mod + M_SW); GLD2(gbv, (unsigned)(ycol * 4), mod + M_GB);
    u32x4 fr[12];
    const int fc = b < 32 ? b : b - 32;
    const char* pw = (OABL & 1) ? wt : wt + (size_t)(fc * 8 + w) * 8192;
    const char* pd = (OABL & 1) ? wt : (const char*)wd + (size_t)fc * 32768 + w * 4096 - 8192;
    const lds_cptr ap = (lds_cptr)lds + mt * 4096 + lane * 16;
    const lds_cptr bp = (lds_cptr)lds + 8192 + (nt >> 1) * 1024 + hh * 512 + ((lane & 15) >> 2) * 128 + ((nt ^ (lane >> 3)) & 1) * 64 + (blk & 1) * 32 + (lane & 3) * 8;
    f32x16 acc = f32x16{};
    asm volatile("s_waitcnt vmcnt(0)" : "+v"(xi[0]), "+v"(xi[1]), "+v"(ref), "+v"(gws), "+v"(sws), "+v"(gbv) :: "memory");
    DIAG_T(0);
    {
        const float a = wsum(gws[0] + gws[1]), c = wsum(sws[0] + sws[1]);
        if (lane == 0) *(LAS f32x2*)(L + OGS + w * 8) = f32x2{a, c};
    }
    float d[16];
#pragma unroll
    for (int j = 0; j < 2; ++j)
#pragma unroll
        for (int e = 0; e < 2; ++e) {
            const auto r = __builtin_amdgcn_permlane32_swap(xi[j][e], xi[j][2 + e], false, false);
            d[8 * j + 2 * e] = bflo(r[0]) - ref; d[8 * j + 2 * e + 1] = bfhi(r[0]) - ref;
            d[8 * j + 4 + 2 * e] = bflo(r[1]) - ref; d[8 * j + 5 + 2 * e] = bfhi(r[1]) - ref;
        }
    bf16x8 fa[2][4], fb[2][4];
    f32x4 va[4], vb[4];
    const lds_cptr vp = (lds_cptr)lds + OV + (nt * 32 + 4 * hh) * 4;
#pragma unroll
    for (int s = 0; s < 17; ++s) {
        if (s < 16) {
            LDS_BAR();
            int so = (s % 6) * OS;
            asm volatile("" : "+s"(so));
#pragma unroll
            for (int ks = 0; ks < 4; ++ks) { fa[s & 1][ks] = ldsfrag(ap + so + ks * 1024); fb[s & 1][ks] = cat4(trread(bp + so + ks * 4096), trread(bp + so + ks * 4096 + 2048)); }
            if (s >= FF0 && s < FF0 + 12) GLD4(fr[s - FF0], (unsigned)((OABL & 2) ? 0 : lane * 16), (s - FF0 < 8 ? pw : pd) + (s - FF0) * 1024);
            SBAR();
        }
        if (s == 16) {
#pragma unroll
            for (int g = 0; g < 4; ++g) { va[g] = *(const LAS f32x4*)(vp + g * 32); vb[g] = *(const LAS f32x4*)(vp + 2048 + g * 32); }
            SBAR();
        }
        if (s > 0) {
#pragma unroll
            for (int ks = 0; ks < 4; ++ks) acc = MFMA32(fb[(s - 1) & 1][ks], fa[(s - 1) & 1][ks], acc);
            SBAR();
        }
    }
    DIAG_T(2);
    {
        float s1a = 0.f, s1b = 0.f, s2a = 0.f, s2b = 0.f, sda = 0.f, sdb = 0.f;
#pragma unroll
        for (int g = 0; g < 4; ++g)
#pragma unroll
            for (int e = 0; e < 4; e += 2) {
                const float v0 = __builtin_fmaf(va[g][e], acc[4 * g + e], d[4 * g + e]), v1 = __builtin_fmaf(va[g][e + 1], acc[4 * g + e + 1], d[4 * g + e + 1]);
                d[4 * g + e] = v0; d[4 * g + e + 1] = v1;
                s1a += v0; s1b += v1; s2a = __builtin_fmaf(v0, v0, s2a); s2b = __builtin_fmaf(v1, v1, s2b);
                sda = __builtin_fmaf(v0, vb[g][e], sda); sdb = __builtin_fmaf(v1, vb[g][e + 1], sdb);
            }
        const float q12 = sw32sum(s1a + s1b, s2a + s2b), sd = sda + sdb, q3 = sw32sum(sd, sd);
        const int o = OST + nt * 256 + (mt * 32 + (lane & 31)) * 4;
        *(LAS float*)(L + o + hh * 1024) = q12;
        *(LAS float*)(L + o + 2048) = q3;
    }
    asm volatile("s_waitcnt vmcnt(0)" : "+v"(fr[0]), "+v"(fr[1]), "+v"(fr[2]), "+v"(fr[3]), "+v"(fr[4]), "+v"(fr[5]), "+v"(fr[6]), "+v"(fr[7]), "+v"(fr[8]), "+v"(fr[9]), "+v"(fr[10]), "+v"(fr[11]) :: "memory");
    LDS_BAR();
    if (w < 3) {
        const LAS float* p = (const LAS float*)(L + OST + w * 1024) + lane;
        const float s = (p[0] + p[64]) + (p[128] + p[192]);
        const u32x2 v = {__float_as_uint(s), TAG};
        GST2C((unsigned)(cp * 1536 + w * 512 + lane * 8), v, gran + (size_t)rp * 12288);
    }
    DIAG_T(3);
    {
        LAS u32x4* p = (LAS u32x4*)(L + (w < 6 ? 4 * OS + w * 8192 : (w - 6) * 8192)) + lane;
#pragma unroll
        for (int j = 0; j < 8; ++j) p[j * 64] = fr[j];
        LAS u32x4* q = (LAS u32x4*)(L + 16384 + w * 4096) + lane;
#pragma unroll
        for (int j = 0; j < 4; ++j) q[j * 64] = fr[8 + j];
    }
    float gtot, ctot;
    {
        const f32x2 t = *(const LAS f32x2*)(L + OGS + (lane & 7) * 8);
        gtot = oct_sum(t[0]); ctot = oct_sum(t[1]);
    }
    f32x4 vc[4];
#pragma unroll
    for (int g = 0; g < 4; ++g) { va[g] = *(const LAS f32x4*)(vp + 512 + g * 32); vb[g] = *(const LAS f32x4*)(vp + 1024 + g * 32); vc[g] = *(const LAS f32x4*)(vp + 1536 + g * 32); }
    bf16x8 fw[12];
    {
        const lds_cptr p = (lds_cptr)lds + (w < 6 ? 4 * OS + w * 8192 : (w - 6) * 8192) + lane * 8;
#pragma unroll
        for (int j = 0; j < 8; ++j) fw[j] = cat4(trread(p + j * 1024), trread(p + j * 1024 + 512));
    }
    DIAG_T(4);
    LDS_BAR();
    DIAG_T(5);
    int brow; f32x2 fs;
    char* g2 = gran + G_H2;
    {
        const LAS f32x4* p = (const LAS f32x4*)(L + 2 * OS + lane * 112);
        const f32x4 a = p[0] + p[1], q = p[2] + p[3], c = p[4] + p[5];
        const float md = ((a[0] + a[1]) + (a[2] + a[3])) * (1.f / D_), s2 = ((q[0] + q[1]) + (q[2] + q[3])) * (1.f / D_), sd = (c[0] + c[1]) + (c[2] + c[3]);
        const float rstd = __builtin_amdgcn_rsqf(fmaxf(s2 - md * md, 0.f) + LN_EPS);
        float r = (sd - md * gtot) * rstd;
        r = r == r ? r : -INFINITY;
        const float best = wmax(r);
        brow = (int)__builtin_ctzll(__ballot(r == best) | (1ull << 63));
        if (w == 0 && cp == 0 && lane < NREP) { const u32x2 v = {__float_as_uint(best), TAG | (unsigned)brow}; GST2C((unsigned)(lane * 4096 + rp * 128), v, g2 + 32 * 4096); }
        const auto r0 = __builtin_amdgcn_permlane32_swap(__float_as_uint(rstd), __float_as_uint(rstd), false, false);
        const auto r1 = __builtin_amdgcn_permlane32_swap(__float_as_uint(-md * rstd), __float_as_uint(-md * rstd), false, false);
        fs = f32x2{__uint_as_float(mt ? r0[1] : r0[0]), __uint_as_float(mt ? r1[1] : r1[0])};
    }
    float h[16];
#pragma unroll
    for (int i = 0; i < 16; ++i) h[i] = __builtin_fmaf(__builtin_fmaf(d[i], fs[0], fs[1]), vb[i >> 2][i & 3], va[i >> 2][i & 3]);
    const bool mine = mt * 32 + (lane & 31) == brow;
    if ((brow >> 5) == mt) {
#pragma unroll
        for (int g = 0; g < 4; ++g) {
            const u32x4 v = {pk2(h[4 * g], h[4 * g + 1]), TAG, pk2(h[4 * g + 2], h[4 * g + 3]), TAG};
            if (mine) GST4C((unsigned)(rp * 4096 + (cp * 128 + nt * 32 + 8 * g + 4 * hh) * 4), v, g2);
        }
    }
    DIAG_T(6);
#pragma unroll
    for (int i = 0; i < 16; ++i) h[i] = __builtin_fmaf(vc[i >> 2][i & 3], h[i], d[i]) + ref;
    u32x4 o[2];
#pragma unroll
    for (int j = 0; j < 2; ++j) {
#pragma unroll
        for (int e = 0; e < 2; ++e) {
            const auto r = __builtin_amdgcn_permlane32_swap(pk2(h[8 * j + 2 * e], h[8 * j + 2 * e + 1]), pk2(h[8 * j + 4 + 2 * e], h[8 * j + 5 + 2 * e]), false, false);
            o[j][e] = r[0]; o[j][2 + e] = r[1];
        }
        if (!mine) *(u32x4*)((char*)(out + 16 * j) + xoff) = o[j];
    }
    {
        const lds_cptr q = (lds_cptr)lds + 16384 + ((lane & 15) >> 2) * 2048 + w * 256 + blk * 32 + (lane & 3) * 8;
#pragma unroll
        for (int j = 0; j < 4; ++j) fw[8 + j] = cat4(trread(q + (j & 1) * 16384 + (j >> 1) * 128), trread(q + (j & 1) * 16384 + (j >> 1) * 128 + 8192));
    }
    LDS_BAR();
    DIAG_T(7);
    const u32x2 wi = *(const LAS u32x2*)(L + OGS + 64);
    char* gy = gran + G_Y;
    if ((brow >> 5) == mt) {
        if ((int)(wi[1] >> 6) == rp) {
#pragma unroll
            for (int g = 0; g < 8; ++g) {
                const u32x4 v = {__float_as_uint(h[2 * g]), TAG, __float_as_uint(h[2 * g + 1]), TAG};
                if (mine) GST4C((unsigned)(224 * 8192 + (cp * 128 + nt * 32 + 8 * (g >> 1) + 4 * hh + 2 * (g & 1)) * 8), v, gy);
            }
        } else if (mine) {
            *(u32x4*)((char*)out + xoff) = o[0]; *(u32x4*)((char*)(out + 16) + xoff) = o[1];
        }
    }
    if (b >= 32) {
        f32x4 ag = f32x4{}, au = f32x4{};
#pragma unroll
        for (int q = 0; q < 4; ++q) {
            const bf16x8 a = ldsfrag((lds_cptr)lds + OBN + w * 256 + q * 64 + blk * 16);
            ag = MFMA16(a, fw[q], ag); au = MFMA16(a, fw[4 + q], au);
        }
        if (lane < 32) *(LAS float*)(L + OPT + (lane & 31) * 32 + w * 4) = lane < 16 ? ag[0] : au[0];
        const float sig = __builtin_amdgcn_rcpf(1.f + __expf(-(__uint_as_float(wi[0]) + ctot)));
        LDS_BAR();
        const LAS f32x4* pp = (const LAS f32x4*)(L + OPT + (lane & 15) * 32);
        const f32x4 a0 = pp[0] + pp[1], u0 = pp[32] + pp[33];
        const float sg = (a0[0] + a0[1]) + (a0[2] + a0[3]), su = (u0[0] + u0[1]) + (u0[2] + u0[3]);
        const float act = sg * su * __builtin_amdgcn_rcpf(1.f + __expf(-sg));
        const unsigned pr = pk2(act, dpp<XOR1>(act));
        float y0 = 0.f, y1 = 0.f, y2 = 0.f, y3 = 0.f;
#pragma unroll
        for (int i = 0; i < 8; i += 2) {
            const unsigned s0 = __builtin_amdgcn_readlane(pr, 2 * i), s1 = __builtin_amdgcn_readlane(pr, 2 * i + 2);
            const u32x4 w0 = __builtin_bit_cast(u32x4, fw[8 + (i >> 2)]), w1 = __builtin_bit_cast(u32x4, fw[10 + (i >> 2)]);
            y0 = dot2(s0, w0[i & 3], y0); y1 = dot2(s0, w1[i & 3], y1); y2 = dot2(s1, w0[(i & 3) + 1], y2); y3 = dot2(s1, w1[(i & 3) + 1], y3);
        }
        y0 += y2; y1 += y3;
        const float t = dpp<XOR1>((lane & 1) ? y0 : y1);
        const u32x4 v = {__float_as_uint(((lane & 1) ? t : y0) * (gbv[0] * sig)), TAG, __float_as_uint(((lane & 1) ? y1 : t) * (gbv[1] * sig)), TAG};
        GST4C((unsigned)((b - 32) * 8192 + ycol * 8), v, gy);
    } else __builtin_amdgcn_s_sleep(OSL3 + 10);
    DIAG_T(1);
    {
        const int cg = (b & 7) * 32 + (b >> 3), r = w * 32 + (lane >> 1);
        u32x4 v;
        __builtin_amdgcn_s_sleep(OSL3);
#pragma unroll 1
        for (int spin = 0; spin < SPIN_MAX; ++spin) {
            GLD4C(v, (unsigned)((r < 224 ? r : 224) * 8192 + (lane & 1) * 16), gy + cg * 32);
            asm volatile("s_waitcnt vmcnt(0)" : "+v"(v) :: "memory");
            if (__all((v[1] & v[3]) == TAG)) break;
        }
        float a = r > 224 ? 0.f : __uint_as_float(v[0]), c = r > 224 ? 0.f : __uint_as_float(v[2]);
        a = sw32sum(a, c);
        a = sw16sum(a, a); a += dpp<XOR2>(a); a += dpp<ROR4>(a); a += dpp<ROR8>(a);
        if ((lane & 30) == 0) *(LAS float*)(L + OST + w * 16 + (lane & 1) * 8 + hh * 4) = a;
        LDS_BAR();
        if (tid < 8) {
            f32x4 q = *(const LAS f32x4*)(L + OST + lane * 16);
#pragma unroll
            for (int e = 0; e < 4; ++e) q[e] = oct_sum(q[e]);
            if (tid == 0) *(u32x2*)(out + (size_t)(wi[1] & (S_ - 1)) * D_ + cg * 4) = u32x2{pk2(q[0], q[1]), pk2(q[2], q[3])};
        }
    }
    DIAG_END(4);
}

extern "C" void kernel_launch(void* const* d_in, const int* in_sizes, int n_in, void* d_out, int out_size, void* d_ws, size_t ws_size,
                              hipStream_t stream) {
    const u16* x = (const u16*)d_in[0];
    const u16* wqkv = (const u16*)d_in[1];
    const u16* wo = (const u16*)d_in[2];
    const u16* wr = (const u16*)d_in[4];
    const u16* wg = (const u16*)d_in[5];
    const u16* wu = (const u16*)d_in[6];
    const u16* wd = (const u16*)d_in[7];
    const u16* c = (const u16*)d_in[8];
    const u16* wmod = (const u16*)d_in[9];
    char* ws = (char*)d_ws;
    float* mod = (float*)(ws + WS_MOD);
    char* gran = ws + WS_GRAN;
    float* modp = (float*)(ws + WS_MODP);
    float* kn2 = (float*)(ws + WS_KN2);
    float* mean1 = (float*)(ws + WS_MEAN);
    u16* Qf = (u16*)(ws + WS_QF);
    u16* KVf = (u16*)(ws + WS_KVF);
    u16* Of = (u16*)(ws + WS_OF);
    u16* out = (u16*)d_out;
    u16* scb = (u16*)(ws + WS_SC);
    char* wt = ws + WS_WT;
    k_mod<<<256, 512, 0, stream>>>(c, wmod, scb, x, modp, mean1, ws + WS_XN, ws + WS_GM, wqkv DIAG_PASS);
    k_qkv<<<256, 768, 0, stream>>>(ws + WS_XN, modp, wqkv, Qf, KVf, kn2 DIAG_PASS);
    k_attn<<<256, 832, 0, stream>>>(Qf, KVf, kn2, Of, wg, wu, wd, wt, wmod, scb, wr, mod, wo, gran, ws + WS_GM DIAG_PASS);
    k_out<<<256, 768, 0, stream>>>(Of, wo, x, mean1, mod, wt, wd, gran, out DIAG_PASS);
}
```

```cpp
#include <hip/hip_runtime.h>
#include <hip/hip_bf16.h>
#include <stdint.h>

typedef __attribute__((ext_vector_type(8))) short bf16x8;
typedef __attribute__((ext_vector_type(4))) short s16x4;
typedef __attribute__((ext_vector_type(16))) float f32x16;
typedef __attribute__((ext_vector_type(4))) float f32x4;
typedef __attribute__((ext_vector_type(2))) float f32x2;
typedef __attribute__((ext_vector_type(4))) unsigned u32x4;
typedef __attribute__((ext_vector_type(2))) unsigned u32x2;
typedef __bf16 bf16x2_t __attribute__((ext_vector_type(2)));
typedef unsigned short u16;
typedef __attribute__((address_space(3))) const char* lds_cptr;
typedef __attribute__((address_space(3))) char* lds_ptr;

#define DI __device__ __forceinline__
#define MFMA32(a, b, c) __builtin_amdgcn_mfma_f32_32x32x16_bf16((a), (b), (c), 0, 0, 0)
#define MFMA16(a, b, c) __builtin_amdgcn_mfma_f32_16x16x32_bf16((a), (b), (c), 0, 0, 0)
#define SBAR() __builtin_amdgcn_sched_barrier(0)

constexpr int S_ = 2048, D_ = 1024, H_ = 32, F_ = 3584;
constexpr float LN_EPS = 1e-5f;
constexpr float QSCALE = 0.17677669529663687f * 1.4426950408889634f;

constexpr size_t WS_MOD = 0;
constexpr size_t WS_SC = 65536;
constexpr size_t WS_MEAN = 114688;
constexpr size_t WS_GRAN = 131072;
#define NREP 8
constexpr int G_H2 = 32 * 12288, G_BEST = G_H2 + 32 * 4096, G_Y = G_BEST + NREP * 4096, G_END = G_Y + 225 * 8192;
constexpr size_t WS_MODP = 5u << 20;
constexpr size_t WS_GM = 4u << 20;
constexpr unsigned TAG = 0x5EED0000u;
constexpr int SPIN_MAX = 1 << 18;
constexpr size_t WS_QF = 6u << 20;
constexpr size_t WS_KVF = WS_QF + (4u << 20);
constexpr size_t WS_OF = WS_KVF + (8u << 20);
#define WT_PITCH 65792
constexpr size_t WS_WT = WS_OF + (4u << 20);
constexpr size_t WS_XN = WS_WT + (16u << 20);

#define DIAG_ARG
#define DIAG_BEGIN
#define DIAG_T(i)
#define DIAG_END(kid)
#define DIAG_PASS
#define DIAG_A(i)
#define DIAG_A0
DI float bflo(unsigned u) { return __uint_as_float(u << 16); }
DI float bfhi(unsigned u) { return __uint_as_float(u & 0xffff0000u); }
DI unsigned pk2(float lo, float hi) {
    f32x2 v = {lo, hi};
    return __builtin_bit_cast(unsigned, __builtin_convertvector(v, bf16x2_t));
}
DI float rbf(float x) { return bflo(pk2(x, x) & 0xffffu); }
DI float bf2f(u16 v) { return __uint_as_float((unsigned)v << 16); }
DI s16x4 trread(lds_cptr p) {
    typedef short v4i16_t __attribute__((ext_vector_type(4)));
    return __builtin_bit_cast(s16x4, __builtin_amdgcn_ds_read_tr16_b64_v4i16((__attribute__((address_space(3))) v4i16_t*)p));
}
DI bf16x8 ldsfrag(lds_cptr p) { return *(const __attribute__((address_space(3))) bf16x8*)p; }
DI bf16x8 cat4(s16x4 a, s16x4 b) { return __builtin_shufflevector(a, b, 0, 1, 2, 3, 4, 5, 6, 7); }
DI void glds16(const void* sbase, unsigned voff, unsigned lds_base) {
    unsigned sv;
    asm volatile("s_mov_b32 %0, m0\n\ts_mov_b32 m0, %3\n\ts_nop 0\n\tglobal_load_lds_dwordx4 %1, %2\n\ts_mov_b32 m0, %0"
                 : "=&s"(sv) : "v"(voff), "s"(sbase), "s"(lds_base) : "memory");
}
#define GLD4(dst, voff, sbase) asm volatile("global_load_dwordx4 %0, %1, %2" : "=v"(dst) : "v"(voff), "s"(sbase) : "memory")
#define GLD2(dst, voff, sbase) asm volatile("global_load_dwordx2 %0, %1, %2" : "=v"(dst) : "v"(voff), "s"(sbase) : "memory")
#define GLD1(dst, voff, sbase) asm volatile("global_load_dword %0, %1, %2" : "=v"(dst) : "v"(voff), "s"(sbase) : "memory")
#define GLDH(dst, voff, sbase) asm volatile("global_load_ushort %0, %1, %2" : "=v"(dst) : "v"(voff), "s"(sbase) : "memory")
#define GLD2C(dst, voff, sbase) asm volatile("global_load_dwordx2 %0, %1, %2 sc1" : "=v"(dst) : "v"(voff), "s"(sbase) : "memory")
#define GLD4C(dst, voff, sbase) asm volatile("global_load_dwordx4 %0, %1, %2 sc1" : "=v"(dst) : "v"(voff), "s"(sbase) : "memory")
#define GST2C(voff, val, sbase) asm volatile("global_store_dwordx2 %0, %1, %2 sc1" :: "v"(voff), "v"(val), "s"(sbase) : "memory")
#define GST4C(voff, val, sbase) asm volatile("global_store_dwordx4 %0, %1, %2 sc1\n\ts_nop 1" :: "v"(voff), "v"(val), "s"(sbase) : "memory")
#define WAIT_VM(n) asm volatile("s_waitcnt vmcnt(" #n ")" ::: "memory")
#define WAIT_BAR(n) asm volatile("s_waitcnt vmcnt(" #n ") lgkmcnt(0)\n\ts_barrier" ::: "memory")
#define LDS_BAR() asm volatile("s_waitcnt lgkmcnt(0)\n\ts_barrier" ::: "memory")
DI float xor32(float v) {
    auto r = __builtin_amdgcn_permlane32_swap(__float_as_uint(v), __float_as_uint(v), false, false);
    return (threadIdx.x & 32) ? __uint_as_float(r[0]) : __uint_as_float(r[1]);
}
DI float wave_sum(float v) {
#pragma unroll
    for (int o = 1; o < 64; o <<= 1) v += __shfl_xor(v, o);
    return v;
}
DI float wave_max(float v) {
#pragma unroll
    for (int o = 1; o < 64; o <<= 1) v = fmaxf(v, __shfl_xor(v, o));
    return v;
}

template <int CTRL> DI float dpp(float x) { return __builtin_bit_cast(float, __builtin_amdgcn_mov_dpp(__builtin_bit_cast(int, x), CTRL, 0xf, 0xf, true)); }
constexpr int XOR1 = 0xB1, XOR2 = 0x4E, ROR4 = 0x124, ROR8 = 0x128;
DI float sw32sum(float a, float b) {
    auto r = __builtin_amdgcn_permlane32_swap(__float_as_uint(a), __float_as_uint(b), false, false);
    return __uint_as_float(r[0]) + __uint_as_float(r[1]);
}
DI float sw16sum(float a, float b) {
    auto r = __builtin_amdgcn_permlane16_swap(__float_as_uint(a), __float_as_uint(b), false, false);
    return __uint_as_float(r[0]) + __uint_as_float(r[1]);
}
DI float row_sum16(float v) { v += dpp<XOR1>(v); v += dpp<XOR2>(v); v += dpp<ROR4>(v); v += dpp<ROR8>(v); return v; }
DI float row_max16(float v) { v = fmaxf(v, dpp<XOR1>(v)); v = fmaxf(v, dpp<XOR2>(v)); v = fmaxf(v, dpp<ROR4>(v)); v = fmaxf(v, dpp<ROR8>(v)); return v; }
DI float wsum(float v) { v = row_sum16(v); v = sw16sum(v, v); return sw32sum(v, v); }
DI float wmax(float m) {
    m = row_max16(m);
    { auto s_ = __builtin_amdgcn_permlane16_swap(__float_as_uint(m), __float_as_uint(m), false, false); m = fmaxf(__uint_as_float(s_[0]), __uint_as_float(s_[1])); }
    { auto s_ = __builtin_amdgcn_permlane32_swap(__float_as_uint(m), __float_as_uint(m), false, false); m = fmaxf(__uint_as_float(s_[0]), __uint_as_float(s_[1])); }
    return m;
}
DI int lane_id() { int l = __builtin_amdgcn_mbcnt_hi(~0u, __builtin_amdgcn_mbcnt_lo(~0u, 0u)); asm volatile("" : "+v"(l)); return l; }
DI float sfma(float a, float b, float c) { float r; asm("v_fma_f32 %0, %1, %2, %3" : "=v"(r) : "v"(a), "v"(b), "v"(c)); return r; }
#define LAS __attribute__((address_space(3)))
constexpr int M_SA = 0, M_G1 = 1024, M_GA = 2048, M_SB = 3072, M_G2 = 4096, M_GB = 5120, M_GW = 6144, M_SW = 7168;

#define MQ 4
DI unsigned wpf_off(int id, int xcd, int lane) {
    const int r2 = (id / 3) * 2 + (lane >> 5), k = (r2 >> 5) * 64 + (xcd & 1) * 32 + (r2 & 31);
    return (unsigned)(k * 6144 + (id % 3) * 2048 + (xcd >> 1) * 512 + (lane & 31) * 16);
}
#define MBAR 3
#define MSL0 0
#define MSL 0
#define MSC1 "sc1"
__global__ __launch_bounds__(512) void k_mod(const u16* __restrict__ c, const u16* __restrict__ wmod, u16* __restrict__ scb,
                                             const u16* __restrict__ x, float* __restrict__ modp, float* __restrict__ mean1,
                                             char* __restrict__ xn, char* __restrict__ gm, const u16* __restrict__ W DIAG_ARG) {
    DIAG_BEGIN
    __shared__ __attribute__((aligned(16))) float red[8][32];
    __shared__ __attribute__((aligned(16))) unsigned xt[8][528];
    const int t = threadIdx.x, lane = t & 63, b = blockIdx.x;
    const int w = __builtin_amdgcn_readfirstlane(t >> 6);
    const int xcd = b & 7, i = b >> 3;
    const int cg = (xcd * 4 + (i >> 3)) * 2 + (i & 1), ks = (i >> 1) & 3;
    const int rg = ks * 256 + (t >> 2), row = (b >> 1) * 16 + (w >> 1) * 4 + (b & 1) * 2 + (w & 1);
    u32x4 wv[2], v[2], pf[3]; unsigned cv[2];
    GLD4(v[0], (unsigned)(lane * 16), x + (size_t)row * D_); GLD4(v[1], (unsigned)(lane * 16), x + (size_t)row * D_ + 512);
    if (MBAR & 1) asm volatile("s_barrier" ::: "memory");
    if (MSL0) __builtin_amdgcn_s_sleep(MSL0);
    GLDH(cv[0], (unsigned)(rg * 2), c); GLDH(cv[1], (unsigned)(rg * 2), c + 128);
    GLD4(wv[0], (unsigned)((rg * 6144 + cg * 32 + (t & 3) * 8) * 2), wmod); GLD4(wv[1], (unsigned)((rg * 6144 + cg * 32 + (t & 3) * 8) * 2), wmod + 128 * 6144);
    if (MBAR & 2) asm volatile("s_barrier" ::: "memory");
    if (MSL) __builtin_amdgcn_s_sleep(MSL);
#pragma unroll
    for (int n = 0; n < 3; ++n) {
        const int id = n * 256 + i * 8 + w;
        GLD4(pf[n], id < 192 * MQ ? wpf_off(id, xcd, lane) : 0u, W);
    }
    asm volatile("s_waitcnt vmcnt(7)" : "+v"(v[0]), "+v"(v[1]) :: "memory");
    DIAG_T(0);
    {
        float f[16], s = 0.f;
#pragma unroll
        for (int j = 0; j < 8; ++j) { f[2 * j] = bflo(v[j >> 2][j & 3]); f[2 * j + 1] = bfhi(v[j >> 2][j & 3]); s += f[2 * j] + f[2 * j + 1]; }
        const float mean = wsum(s) * (1.f / D_);
        float q = 0.f;
#pragma unroll
        for (int j = 0; j < 16; ++j) { f[j] -= mean; q += f[j] * f[j]; }
        const float rstd = rsqrtf(wsum(q) * (1.f / D_) + LN_EPS);
#pragma unroll
        for (int j = 0; j < 2; ++j) {
            u32x4 o;
#pragma unroll
            for (int e = 0; e < 4; ++e) o[e] = pk2(f[8 * j + 2 * e] * rstd, f[8 * j + 2 * e + 1] * rstd);
            *(u32x4*)&xt[w][j * 256 + lane * 4] = o;
        }
        if (lane == 0) xt[w][512] = __float_as_uint(mean);
    }
    LDS_BAR();
#pragma unroll
    for (int j = 0; j < 2; ++j) {
        const unsigned* p = &xt[(t >> 4) & 1][j * 256 + (t >> 5) * 16 + (t & 15)];
        const u32x4 o = {p[0], p[2 * 528], p[4 * 528], p[6 * 528]};
        asm volatile("global_store_dwordx4 %0, %1, %2 " MSC1 "\n\ts_nop 1" :: "v"((unsigned)((t >> 5) * 8192 + (t & 31) * 16)), "v"(o), "s"(xn + (size_t)(b >> 4) * 262144 + (b & 15) * 512 + j * 131072) : "memory");
    }
    DIAG_T(1);
    asm volatile("s_waitcnt vmcnt(7)" : "+v"(cv[0]), "+v"(cv[1]) :: "memory");
    float sc[2];
#pragma unroll
    for (int j = 0; j < 2; ++j) { const float cf = bflo(cv[j]); sc[j] = rbf(cf / (1.f + __expf(-cf))); }
    asm volatile("s_waitcnt vmcnt(5)" : "+v"(wv[0]), "+v"(wv[1]) :: "memory");
    {
        float acc[8];
#pragma unroll
        for (int j = 0; j < 4; ++j) { acc[2 * j] = sc[0] * bflo(wv[0][j]) + sc[1] * bflo(wv[1][j]); acc[2 * j + 1] = sc[0] * bfhi(wv[0][j]) + sc[1] * bfhi(wv[1][j]); }
#pragma unroll
        for (int j = 0; j < 4; ++j) acc[j] = sw32sum(acc[j], acc[4 + j]);
        f32x2 z;
#pragma unroll
        for (int j = 0; j < 2; ++j) {
            float q = sw16sum(acc[j], acc[2 + j]);
            q += dpp<ROR4>(q); q += dpp<ROR8>(q);
            z[j] = q;
        }
        if ((lane & 12) == 0) *(f32x2*)&red[w][(lane & 3) * 8 + (lane >> 5) * 4 + ((lane >> 4) & 1) * 2] = z;
    }
    LDS_BAR();
    if (t < 32) {
        modp[ks * 2048 + cg * 32 + t] = ((red[0][t] + red[1][t]) + (red[2][t] + red[3][t])) + ((red[4][t] + red[5][t]) + (red[6][t] + red[7][t]));
        ((u32x4*)gm)[b * 32 + t] = u32x4{};
    }
    if (lane == 0) mean1[row] = __uint_as_float(xt[w][512]);
    if (cg == 0 && (t & 3) == 0) {
#pragma unroll
        for (int j = 0; j < 2; ++j) scb[128 * j + (rg & ~31) + ((rg >> 2) & 3) * 8 + ((rg >> 4) & 1) * 4 + (rg & 3)] = (u16)(__float_as_uint(sc[j]) >> 16);
    }
    DIAG_T(2);
    asm volatile("s_waitcnt vmcnt(0)" : "+v"(pf[0]), "+v"(pf[1]), "+v"(pf[2]) :: "memory");
    DIAG_END(0);
}

#define QSC1 "sc1"
#define QW 0
#define QWF "nt"
#define QABL 0
#define QSLP 3
#define QUN 1
#define QPK 0
#define QPRIO 0
typedef volatile LAS unsigned* lds_cnt;
constexpr int QA_SLOT = 8704, QB_SLOT = 12288, QB_OFF = 5 * QA_SLOT, QG_OFF = QB_OFF + 9 * QB_SLOT, QK_CNT = QG_OFF + 8192 + 1024, QK_LDS = QK_CNT + 64;
__global__ __launch_bounds__(768, 3) void k_qkv(const char* __restrict__ xn, const float* __restrict__ mod,
                                                const u16* __restrict__ W, u16* __restrict__ Qf, u16* __restrict__ KVf,
                                                const u16* __restrict__ wg, const u16* __restrict__ wu, const u16* __restrict__ wd,
                                                const u16* __restrict__ wmod DIAG_ARG) {
    DIAG_BEGIN
    __shared__ __attribute__((aligned(16))) char lds[QK_LDS];
    const int b = blockIdx.x, tid = threadIdx.x, lane = tid & 63;
    const int w = __builtin_amdgcn_readfirstlane(tid >> 6);
    const int xcd = b & 7, idx = b >> 3;
    const int rp = (xcd & 1) * 8 + (idx & 7), cp = (xcd >> 1) * 4 + (idx >> 3);
    const unsigned lds0 = (unsigned)(uintptr_t)lds;
    const lds_cnt cnt = (lds_cnt)((lds_ptr)lds + QK_CNT);
#define Q_SPIN(f, p, need) do { while (__any((int)(f) < (need))) { __builtin_amdgcn_s_sleep(QSLP); f = *(p); } asm volatile("" ::: "memory"); } while (0)
    if (w >= 4) {
        const int pw = w - 4, par = pw >> 2;
        unsigned boff = (unsigned)(((pw * 8 + (lane >> 3)) * 3072 + cp * 64 + ((lane & 7) ^ ((lane >> 2) & 4)) * 8) * 2);
        const char* wv = (const char*)W + 3072;
        unsigned dst = lds0 + QB_OFF + par * QB_SLOT + (pw & 3) * 3072;
        const unsigned dend = lds0 + QB_OFF + 9 * QB_SLOT;
#define QB_M0() asm volatile("s_mov_b32 m0, %0\n\ts_nop 0" :: "s"(dst) : "memory")
#define QB_Q() asm volatile("global_load_lds_dwordx4 %0, %1" :: "v"(boff), "s"(W) : "memory")
#define QB_K() asm volatile("global_load_lds_dwordx4 %0, %1 offset:2048" :: "v"(boff), "s"(W) : "memory")
#define QB_V() asm volatile("global_load_lds_dwordx4 %0, %1 offset:1024" :: "v"(boff), "s"(wv) : "memory")
#define QB_NEXT() do { boff += bst; dst += 2 * QB_SLOT; if (dst >= dend) dst -= 9 * QB_SLOT; } while (0)
        unsigned xoff = (unsigned)(rp * 262144 + pw * 2048 + lane * 16), xst = (QABL & 4) ? 0 : 16384, bst = (QABL & 1) ? 0 : 64 * 3072 * 2;
        u32x4 xs[4][2];
#define QX_0(q) asm volatile("global_load_dwordx4 %0, %1, %2" : "=v"(xs[q][0]) : "v"(xoff), "s"(xn) : "memory")
#define QX_1(q) do { asm volatile("global_load_dwordx4 %0, %1, %2 offset:1024" : "=v"(xs[q][1]) : "v"(xoff), "s"(xn) : "memory"); xoff += xst; } while (0)
        QX_0(0); QX_1(0); QB_M0(); QB_Q(); QB_K(); QB_V(); QB_NEXT(); QX_0(1); QX_1(1); QX_0(2); QX_1(2); QB_M0(); QB_Q(); QB_K(); QB_V(); QB_NEXT(); QX_0(3); QX_1(3);
        lds_cptr gp = (lds_cptr)lds + QG_OFF + par * 256 + (lane & 15) * 16;
        unsigned ad = lds0 + par * QA_SLOT + (pw & 3) * 2176;
        const lds_cnt fr = cnt + (lane & 3);
        if (QPRIO == 1 || (QPRIO == 2 && par)) __builtin_amdgcn_s_setprio(1);
        asm volatile("s_barrier" ::: "memory");
        f32x4 g2[2] = {*(const LAS f32x4*)gp, *(const LAS f32x4*)(gp + 512)};
        unsigned o[8], f = 0, fl = par;
        lds_cnt cw = cnt + w;
        asm volatile("" : "+v"(cw));
#define QA_M2(q, i) do { float t0, t1;                                                                            \
            if (QABL & 32) { o[i] = xs[q][(i) >> 2][(i) & 3]; o[(i) + 1] = xs[q][(i) >> 2][((i) & 3) + 1]; } else \
            asm volatile("v_lshlrev_b32 %0, 16, %4\n\tv_and_b32 %2, 0xffff0000, %4\n\tv_lshlrev_b32 %1, 16, %5\n\tv_and_b32 %3, 0xffff0000, %5\n\t"      \
                         "v_fma_f32 %0, %0, %6, %8\n\tv_fma_f32 %2, %2, %7, %9\n\tv_fma_f32 %1, %1, %6, %8\n\tv_fma_f32 %3, %3, %7, %9\n\t"              \
                         "v_cvt_pk_bf16_f32 %0, %0, %2\n\tv_cvt_pk_bf16_f32 %1, %1, %3"                           \
                         : "=&v"(o[i]), "=&v"(o[(i) + 1]), "=&v"(t0), "=&v"(t1)                                   \
                         : "v"(xs[q][(i) >> 2][(i) & 3]), "v"(xs[q][(i) >> 2][((i) & 3) + 1]), "v"(g2[(q) & 1][0]), "v"(g2[(q) & 1][1]), "v"(g2[(q) & 1][2]), "v"(g2[(q) & 1][3])); } while (0)
        asm volatile("s_waitcnt vmcnt(12)" : "+v"(xs[0][0]), "+v"(xs[0][1]) :: "memory");
#pragma unroll
        for (int i = 0; i < 8; i += 2) QA_M2(0, i);
        DIAG_A0
#pragma unroll 1
        for (int it = 0; it < 16; it += 4) {
            if (it == 12 || (QABL & 16)) { xoff = 0; xst = 0; }
#pragma unroll
            for (int q = 0; q < 4; ++q) {
                const int u = 2 * (it + q) + par, n = (q + 1) & 3;
                asm volatile(".Lqp%=:\n\tv_cmp_gt_i32 vcc, %16, %4\n\ts_cbranch_vccz .Lqq%=\n\ts_sleep %18\n\tds_read_b32 %4, %17\n\ts_waitcnt lgkmcnt(0)\n\ts_branch .Lqp%=\n.Lqq%=:\n\t"
                             "s_mov_b32 m0, %5\n\tv_add_u32 %0, 2, %0\n\t"
                             "ds_write_addtid_b32 %8\n\tds_write_addtid_b32 %9 offset:272\n\tds_write_addtid_b32 %10 offset:544\n\tds_write_addtid_b32 %11 offset:816\n\t"
                             "ds_write_addtid_b32 %12 offset:1088\n\tds_write_addtid_b32 %13 offset:1360\n\tds_write_addtid_b32 %14 offset:1632\n\tds_write_addtid_b32 %15 offset:1904\n\t"
                             "s_mov_b32 m0, %6\n\ts_waitcnt vmcnt(7)\n\tds_write_b32 %7, %0"
                             : "+v"(fl), "+v"(xs[n][0]), "+v"(xs[n][1]), "+v"(g2[n & 1]), "+v"(f)
                             : "s"(ad), "s"(dst), "v"(cw), "v"(o[0]), "v"(o[1]), "v"(o[2]), "v"(o[3]), "v"(o[4]), "v"(o[5]), "v"(o[6]), "v"(o[7]),
                               "s"(u - 4), "v"(fr), "n"(QSLP) : "vcc", "memory");
                if (QPRIO == 4) { if (par ^ (q & 1)) __builtin_amdgcn_s_setprio(1); else __builtin_amdgcn_s_setprio(0); }
                DIAG_A(5);
                SBAR(); QB_Q();
                SBAR(); QA_M2(n, 0); SBAR(); QB_K();
                SBAR(); QA_M2(n, 2); SBAR(); QB_V();
                SBAR(); QA_M2(n, 4); SBAR(); QX_0(q);
                g2[q & 1] = *(const LAS f32x4*)(gp + (q + 2) * 512);
                f = *fr;
                SBAR(); QA_M2(n, 6); SBAR(); QX_1(q);
                if (q == 1 && it == 12) { boff = 0; bst = 0; }
                QB_NEXT();
                ad += 2 * QA_SLOT; if (ad >= lds0 + 5 * QA_SLOT) ad -= 5 * QA_SLOT;
                DIAG_A(6);
            }
            gp += 2048;
        }
        cnt[w] = 64;
        WAIT_VM(0);
#pragma unroll
        for (int q = 0; q < 4; ++q) asm volatile("" :: "v"(xs[q][0]), "v"(xs[q][1]));
    } else {
        const int rh = w & 1, hd = w >> 1, G = lane >> 4, hh = lane >> 5;
        lds_cptr a_ = (lds_cptr)lds + (rh * 16 + ((lane & 31) >> 2)) * 272 + (lane & 3) * 64 + hh * 16;
        lds_cptr b_ = (lds_cptr)lds + QB_OFF + hh * 3072 + ((lane & 15) >> 2) * 128 + ((hd ^ (lane >> 3)) & 1) * 64 + (G & 1) * 32 + (lane & 3) * 8;
        f32x16 acc[2][3];
#pragma unroll
        for (int i = 0; i < 2; ++i)
#pragma unroll
            for (int j = 0; j < 3; ++j) acc[i][j] = f32x16{};
        bf16x8 a0[2], b0[3], a1[2], b1[3];
#define QC_RB(bf, j, s) do { if (!(QABL & 2)) bf[j] = cat4(trread(b_ + (2 * (j) % 3) * 1024 + (s) * 6144), trread(b_ + (2 * (j) % 3) * 1024 + (s) * 6144 + 512)); } while (0)
#define QC_STEP(af, bf, an, bn, s, E2, E3, E5) do {                                                               \
            acc[0][0] = MFMA32(bf[0], af[0], acc[0][0]); SBAR();                                                  \
            if (!(QABL & 2)) { an[0] = ldsfrag(a_ + (s) * 32); an[1] = ldsfrag(a_ + (s) * 32 + 2176); } SBAR();   \
            acc[0][1] = MFMA32(bf[1], af[0], acc[0][1]); SBAR();                                                  \
            QC_RB(bn, 0, s); E2; SBAR();                                                                          \
            acc[0][2] = MFMA32(af[0], bf[2], acc[0][2]); SBAR();                                                  \
            QC_RB(bn, 1, s); E3; SBAR();                                                                          \
            acc[1][0] = MFMA32(bf[0], af[1], acc[1][0]); SBAR();                                                  \
            QC_RB(bn, 2, s); SBAR();                                                                              \
            acc[1][1] = MFMA32(bf[1], af[1], acc[1][1]); SBAR();                                                  \
            asm volatile("" ::: "memory"); E5; asm volatile("" ::: "memory"); SBAR();                             \
            acc[1][2] = MFMA32(af[1], bf[2], acc[1][2]); SBAR();                                                  \
        } while (0)
        {
            f32x4 p[8];
#pragma unroll
            for (int j = 0; j < 8; ++j) GLD4(p[j], (unsigned)(tid * 16), mod + j * 1024);
            if (tid < 16) cnt[tid] = tid >> 3;
            asm volatile("" : "+v"(acc[0][0]), "+v"(acc[0][1]), "+v"(acc[0][2]), "+v"(acc[1][0]), "+v"(acc[1][1]), "+v"(acc[1][2]));
            asm volatile("s_waitcnt vmcnt(0)" : "+v"(p[0]), "+v"(p[1]), "+v"(p[2]), "+v"(p[3]), "+v"(p[4]), "+v"(p[5]), "+v"(p[6]), "+v"(p[7]) :: "memory");
            DIAG_T(2);
            f32x4 sa = (p[0] + p[2]) + (p[4] + p[6]), g = (p[1] + p[3]) + (p[5] + p[7]);
#pragma unroll
            for (int e = 0; e < 4; ++e) { sa[e] = rbf(sa[e]); g[e] = rbf(1.f + rbf(g[e])); }
            *(LAS f32x4*)((lds_ptr)lds + QG_OFF + tid * 32) = f32x4{g[0], g[1], sa[0], sa[1]};
            *(LAS f32x4*)((lds_ptr)lds + QG_OFF + tid * 32 + 16) = f32x4{g[2], g[3], sa[2], sa[3]};
        }
        LDS_BAR();
        DIAG_T(3);
        const lds_cnt rd = cnt + 4 + (lane & 7);
        if (QPRIO >= 2) __builtin_amdgcn_s_setprio(3);
        unsigned f = *rd;
        Q_SPIN(f, rd, 1);
        DIAG_T(0);
        int as = 1, bs = 1, da = QA_SLOT, db = QB_SLOT;
        unsigned un = 1;
        unsigned qcn = 0;
        a0[0] = ldsfrag(a_); a0[1] = ldsfrag(a_ + 2176);
#pragma unroll
        for (int j = 0; j < 3; ++j) b0[j] = cat4(trread(b_ + (2 * j % 3) * 1024), trread(b_ + (2 * j % 3) * 1024 + 512));
        if (QABL & 2) { a1[0] = a0[0]; a1[1] = a0[1]; b1[0] = b0[0]; b1[1] = b0[1]; b1[2] = b0[2]; }
        f = *rd;
        const int wrs = w == 3 ? 12288 : 7168, wst = w == 3 ? 512 : 448;
        const char* wsrc = (w == 0 ? (const char*)wg : w == 1 ? (const char*)wu : w == 2 ? (const char*)wd : (const char*)wmod + 4096) + (size_t)b * 4 * wrs;
        unsigned woff = (unsigned)((lane >> 4) * wrs + (lane & 15) * (wst >> 4)), wjunk;
        SBAR(); __builtin_amdgcn_s_waitcnt(0xC07F); SBAR();
        DIAG_A0
#pragma unroll 2
        for (int u = 0; u < 32; ++u) {
            if (QW && (u & 1)) { asm volatile("global_load_dword %0, %1, %2 " QWF "\n\tv_add_u32 %1, %3, %1" : "=&v"(wjunk), "+v"(woff) : "s"(wsrc), "s"(wst) : "memory"); SBAR(); }
            QC_STEP(a0, b0, a1, b1, 1, a_ += da; asm volatile("" : "+v"(a_)), asm volatile("" : "+v"(un)), cnt[w] = un; b_ += db; asm volatile("" : "+v"(b_)));
            SBAR();
            asm volatile(".Lqc%=:\n\tv_cmp_gt_i32 vcc, %2, %0\n\ts_cbranch_vccz .Lqd%=\n\tds_read_b32 %0, %1\n\ts_waitcnt lgkmcnt(0)\n\ts_branch .Lqc%=\n.Lqd%=:"
                         : "+v"(f) : "v"(rd), "s"(u + 2) : "vcc", "memory");
            SBAR();
            QC_STEP(a1, b1, a0, b0, 0, da = QA_SLOT; if (++as == 5) { as = 0; da = -4 * QA_SLOT; } asm volatile("" : "+s"(da), "+s"(as)),
                    db = QB_SLOT; if (++bs == 9) { bs = 0; db = -8 * QB_SLOT; } asm volatile("" : "+s"(db), "+s"(bs)), f = *rd; ++un);
        }
        DIAG_T(1);
        if (QW) asm volatile("s_waitcnt vmcnt(0)" : "+v"(wjunk) :: "memory");
        const int head = cp * 2 + hd;
        int ln = __builtin_amdgcn_mbcnt_hi(~0u, __builtin_amdgcn_mbcnt_lo(~0u, 0u));
        asm volatile("" : "+v"(ln));
#pragma unroll
        for (int i = 0; i < 2; ++i) {
            const int tile = rp * 4 + rh * 2 + i;
            u32x4* qo = (u32x4*)((char*)Qf + ((size_t)(head * 64 + tile) * 2) * 1024) + ln;
            u32x4* kvo = (u32x4*)((char*)KVf + ((size_t)(head * 64 + tile) * 4) * 1024) + ln;
#pragma unroll
            for (int s = 0; s < 2; ++s) {
                u32x4 q, k, v;
#pragma unroll
                for (int j = 0; j < 4; ++j) {
                    const float k0 = acc[i][1][8 * s + 2 * j], k1 = acc[i][1][8 * s + 2 * j + 1];
                    q[j] = pk2(acc[i][0][8 * s + 2 * j] * QSCALE, acc[i][0][8 * s + 2 * j + 1] * QSCALE);
                    k[j] = pk2(k0, k1);
                    v[j] = pk2(acc[i][2][8 * s + 2 * j], acc[i][2][8 * s + 2 * j + 1]);
                }
                {
                    const char* qb = (const char*)Qf + ((size_t)(head * 64 + tile) * 2 + s) * 1024;
                    const char* kb = (const char*)KVf + ((size_t)(head * 64 + tile) * 4 + s) * 1024;
                    asm volatile("global_store_dwordx4 %0, %1, %2 " QSC1 "\n\ts_nop 1" :: "v"(ln * 16), "v"(q), "s"(qb) : "memory");
                    asm volatile("global_store_dwordx4 %0, %1, %2 " QSC1 "\n\ts_nop 1" :: "v"(ln * 16), "v"(k), "s"(kb) : "memory");
                    asm volatile("global_store_dwordx4 %0, %1, %2 offset:2048 " QSC1 "\n\ts_nop 1" :: "v"(ln * 16), "v"(v), "s"(kb) : "memory");
                }
            }
        }
    }
    DIAG_END(2);
}

constexpr int AT_SLOT = 32768, AT_STG = 4 * AT_SLOT, AT_RED = AT_STG + 4 * 4224, AT_DUM = AT_RED + 1024, AT_LX = AT_DUM + 1024, AT_CNT = AT_LX + 2048, AT_LDS = AT_CNT + 64;
typedef __attribute__((ext_vector_type(8))) unsigned u32x8;
typedef __attribute__((ext_vector_type(8))) __bf16 b8_t;
typedef __attribute__((ext_vector_type(16))) __bf16 b16_t;
#define SMF(a, b, c, ix) __builtin_amdgcn_smfmac_f32_16x16x64_bf16(__builtin_bit_cast(b8_t, a), __builtin_bit_cast(b16_t, b), (c), (ix), 0, 0)
DI bf16x8 lo8(u32x8 p) { const u32x4 r = {p[0], p[1], p[2], p[3]}; return __builtin_bit_cast(bf16x8, r); }
DI bf16x8 hi8(u32x8 p) { const u32x4 r = {p[4], p[5], p[6], p[7]}; return __builtin_bit_cast(bf16x8, r); }
#define PIN(x) asm volatile("" : "+v"(x))
#define MFMA32_M4(d, a, b) asm volatile("v_mfma_f32_32x32x16_bf16 %0, %1, %2, -4.0" : "=&v"(d) : "v"(a), "v"(b))
#define HABL 0
#define HSL0 16
#define HP 6
#define ABAL 6
#define HCUT 9
#define HW0 4
#define HP4 3
#define LP 2
#define HPRIO 2
#define ASL 2
#define LSL 0
#define AONE 1
#define LPUB 0
#define KW 0
#define LPRIO 3
#define GST4W GST4C
DI int ffn_unit(int b) { const int i = b < 32 ? b >> 3 : (b >> 3) - 4; return (b & 7) * 28 + i; }
DI unsigned cnt_wait(lds_cnt p, unsigned target) {
    unsigned v;
    do { v = *p; } while (__any((v & 0xffffu) < target));
    asm volatile("" ::: "memory");
    return v;
}
__global__ __launch_bounds__(832) void k_attn(const u16* __restrict__ Qf, const u16* __restrict__ KVf,
                                                 u16* __restrict__ Of, const u16* __restrict__ wg, const u16* __restrict__ wu,
                                                 const u16* __restrict__ wd, char* __restrict__ wt, const u16* __restrict__ wmod,
                                                 const u16* __restrict__ scb, const u16* __restrict__ wr, float* __restrict__ mod,
                                                 const u16* __restrict__ Wo, char* __restrict__ gran, char* __restrict__ gm DIAG_ARG) {
    DIAG_BEGIN
    __shared__ __attribute__((aligned(16))) char lds[AT_LDS];
    const int b = blockIdx.x, tid = threadIdx.x, lane = tid & 63;
    const int w = __builtin_amdgcn_readfirstlane(tid >> 6);
    const unsigned lds0 = (unsigned)(uintptr_t)lds;
    const lds_cnt cnt = (lds_cnt)((lds_ptr)lds + AT_CNT);
    const int head = (b & 7) * 4 + ((b >> 3) & 3);
    const char* kvbase = (const char*)KVf + (size_t)head * 262144;
    const int kh = (w >> 2) & 1, qt = (b >> 5) * 8 + (w & 3) * 2 + kh;
    const unsigned voff = (unsigned)(w * 4096 + lane * 16);
#define AT_LOAD(c)                                                                  \
    do {                                                                            \
        const char* g_ = kvbase + (c) * AT_SLOT;                                    \
        const unsigned d_ = lds0 + ((c) & 3) * AT_SLOT + w * 4096;                  \
        glds16(g_, voff, d_); glds16(g_ + 1024, voff, d_ + 1024);                   \
        glds16(g_ + 2048, voff, d_ + 2048); glds16(g_ + 3072, voff, d_ + 3072);     \
    } while (0)
    u32x4 qa, qb, qc, qd;
    if (w < 8) {
        const char* qp = (const char*)Qf + (size_t)(head * 64 + qt) * 2048;
        const char* qx = (const char*)Qf + (size_t)(head * 64 + (qt ^ 1)) * 2048;
        GLD4(qa, (unsigned)(lane * 16), qx); GLD4(qb, (unsigned)(lane * 16), qx + 1024);
        AT_LOAD(0);
        GLD4(qc, (unsigned)(lane * 16), qp); GLD4(qd, (unsigned)(lane * 16), qp + 1024);
    } else if (tid < 528) cnt[lane] = 0;
    LDS_BAR();
    if (w == 12) {
        __builtin_amdgcn_s_setprio(LPRIO);
        const int lane = lane_id();
#pragma unroll 1
        for (int c = 1; c < 8; ++c) {
            if (c > 3) {
                if (LPUB) { WAIT_VM(0); cnt[12] = c; }
                while (__any(cnt[lane & 7] < (unsigned)(c - 1))) __builtin_amdgcn_s_sleep(2);
            }
#pragma unroll
            for (int i = 0; i < 8; ++i)
                asm volatile("s_mov_b32 m0, %2\n\ts_nop 0\n\tglobal_load_lds_dwordx4 %0, %1\n\tglobal_load_lds_dwordx4 %0, %1 offset:1024\n\t"
                             "global_load_lds_dwordx4 %0, %1 offset:2048\n\tglobal_load_lds_dwordx4 %0, %1 offset:3072"
                             "\n\ts_sleep %3" :: "v"(lane * 16), "s"(kvbase + c * AT_SLOT + i * 4096), "s"(lds0 + (c & 3) * AT_SLOT + i * 4096), "n"(LSL) : "memory");
            WAIT_VM(32);
            cnt[12] = c;
            if (KW && c == KW) {
#pragma unroll 1
                for (int i = 0; i < 16; ++i) glds16(kvbase + 4 * AT_SLOT + ((b >> 5) * 4 + (i >> 2) * 32 + (i & 3)) * 1024, lane * 16, lds0 + AT_DUM);
            }
        }
        WAIT_VM(0);
        cnt[12] = 8;
        const char* po = (const char*)Wo + (size_t)(b >> 3) * 65536 + (b & 1) * 1024;
#pragma unroll 1
        for (int i = 0; i < 32; ++i) { __builtin_amdgcn_s_sleep(LP); if (!(HABL & 2)) glds16(po + i * 2048, lane * 16, lds0 + AT_DUM); }
        WAIT_VM(0);
        return;
    }
    if (w >= 8) {
        const int hw = w - 8, lane = lane_id();
        if (HABL & 1) return;
        __builtin_amdgcn_s_setprio(HPRIO);
        if (HABL & 64) {
#pragma unroll 1
            for (int i = 0; i < 70; ++i) __builtin_amdgcn_s_sleep(HP);
            return;
        }
        if (HABL & 128) {
#pragma unroll 1
            for (int i = 0; i < 5; ++i) __builtin_amdgcn_s_sleep(112);
            return;
        }
        const lds_ptr sp = (lds_ptr)lds + AT_STG + hw * 4224;
        const unsigned l16 = lane * 16, moff = (unsigned)(((lane >> 3) * 6144 + (lane & 7) * 8) * 2);
        const char* pm = (const char*)wmod + ((size_t)((b & 3) * 256 + hw * 64) * 6144 + 2048 + (b >> 2) * 64) * 2;
        u32x4 af[2], ra[8], rb[8]; unsigned wrb; u32x2 gv;
#define PACE() __builtin_amdgcn_s_sleep(HP)
#define H_WAIT(r, N) asm volatile("s_waitcnt vmcnt(" #N ")" : "+v"(r[0]), "+v"(r[1]), "+v"(r[2]), "+v"(r[3]), "+v"(r[4]), "+v"(r[5]), "+v"(r[6]), "+v"(r[7]) :: "memory")
        const int k0 = (b >> 3) * 32 + hw * 8, l56 = lane < 56 ? lane : lane - 8;
        const size_t go = ((size_t)k0 * F_ + (b & 7) * 448) * 2;
        char* o = wt + (size_t)(b & 7) * 28 * WT_PITCH + (k0 >> 7) * 8192 + (k0 & 127) * 32;
        const unsigned goff = l56 * 16, toff = (unsigned)((l56 >> 3) * WT_PITCH + (lane & 7) * 16);
        const lds_cptr tp = sp + ((lane >> 1) & 3) * 1056 + (l56 >> 3) * 32 + (lane & 1) * 16;
#define H_LD1(r, j, m) do { GLD4(r[j], (HABL & 16) ? 0u : goff, ((m) ? (const char*)wu : (const char*)wg) + go + (j) * F_ * 2); PACE(); } while (0)
#define H_TR(r, h, N) do { asm volatile("s_waitcnt vmcnt(" #N ")" : "+v"(r[4 * (h)]), "+v"(r[4 * (h) + 1]), "+v"(r[4 * (h) + 2]), "+v"(r[4 * (h) + 3]) :: "memory");     \
                           _Pragma("unroll") for (int q_ = 0; q_ < 4; ++q_) *(LAS u32x4*)(sp + q_ * 1056 + l16) = r[4 * (h) + q_];                                        \
                           asm volatile("" ::: "memory");                                                                                                                \
                           _Pragma("unroll") for (int q_ = 0; q_ < 4; ++q_) r[4 * (h) + q_] = *(const LAS u32x4*)(tp + q_ * 224);                                         \
                           asm volatile("" ::: "memory"); } while (0)
#define H_ST1(r, j, m) GST4W(toff, r[j], o + (size_t)((j) & 3) * 7 * WT_PITCH + (m) * 4096 + ((j) >> 2) * 128)
        __builtin_amdgcn_s_sleep(HSL0);
        {
            const u32x4 z = u32x4{};
            const unsigned zo = (unsigned)((b * 256 + hw * 64 + lane) * 16);
            GST4W(zo, z, gran); PACE(); GST4W(zo, z, gran + (1 << 20)); PACE();
            if (zo + (2u << 20) < (unsigned)G_END) GST4W(zo, z, gran + (2 << 20));
        }
        if (HW0) while (cnt[12] < HW0) __builtin_amdgcn_s_sleep(2);
#pragma unroll
        for (int q = 0; q < 2; ++q) GLD4(af[q], (unsigned)((lane >> 4) * 16), scb + (b & 3) * 256 + hw * 64 + q * 32);
        GLDH(wrb, (unsigned)(((b * 16 + (lane & 15)) & 1023) * 2), wr);
#pragma unroll
        for (int q = 0; q < 8; ++q) { GLD4(ra[q], (HABL & 32) ? 0u : moff, pm + (size_t)q * 8 * 6144 * 2); PACE(); }
#pragma unroll
        for (int j = 0; j < 8; ++j) H_LD1(rb, j, 0);
        DIAG_T(0);
        H_WAIT(ra, 8);
        DIAG_T(1);
        asm volatile("" : "+v"(wrb), "+v"(af[0]), "+v"(af[1]));
        {
            f32x4 m[4] = {f32x4{}, f32x4{}, f32x4{}, f32x4{}};
            const lds_ptr wp = sp + ((lane >> 1) & 3) * 1056 + (lane >> 3) * 32 + (lane & 1) * 16;
#pragma unroll
            for (int h = 0; h < 2; ++h) {
#pragma unroll
                for (int q = 0; q < 4; ++q) *(LAS u32x4*)(wp + q * 256) = ra[4 * h + q];
#pragma unroll
                for (int q = 0; q < 4; ++q)
                    m[q] = MFMA16(__builtin_bit_cast(bf16x8, af[h]), cat4(trread(sp + q * 1056 + lane * 8), trread(sp + q * 1056 + 512 + lane * 8)), m[q]);
                asm volatile("" ::: "memory");
            }
            const float lo = (lane & 16) ? m[1][0] : m[0][0], hi = (lane & 16) ? m[3][0] : m[2][0];
            *(LAS float*)((lds_ptr)lds + AT_RED + hw * 256 + lane * 4) = (lane & 32) ? hi : lo;
            cnt[w] = 1;
        }
        DIAG_T(2);
        H_TR(rb, 0, 4);
#define H_P3(j) do { H_LD1(ra, j, 1); H_ST1(rb, j, 0); PACE(); } while (0)
        H_P3(0); H_P3(1);
        if (hw == 0) {
            cnt_wait(cnt + 8 + (lane & 3), 1);
            const LAS float* p = (const LAS float*)((lds_cptr)lds + AT_RED) + lane;
            const u32x2 v = {__float_as_uint((p[0] + p[64]) + (p[128] + p[192])), TAG};
            GST2C((unsigned)(lane * 8), v, gm + b * 512);
        }
        H_P3(2); H_P3(3);
        H_TR(rb, 1, 8);
        H_P3(4); H_P3(5); H_P3(6); H_P3(7);
        DIAG_T(3);
        const char* pd = (const char*)wd + (size_t)ffn_unit(b) * 32768 + hw * 8192;
#define H_P4(j) do { glds16((HABL & 4) ? (const char*)scb : pd + (j) * 1024, (HABL & 4) ? 0u : l16, lds0 + AT_DUM); __builtin_amdgcn_s_sleep(HP4); H_ST1(ra, j, 1); __builtin_amdgcn_s_sleep(HP4); } while (0)
        H_TR(ra, 0, 9);
        H_P4(0); H_P4(1); H_P4(2); H_P4(3);
        H_TR(ra, 1, 9);
        H_P4(4); H_P4(5); H_P4(6); H_P4(7);
        DIAG_T(4);
        const unsigned gmo = (unsigned)((lane >> 4) * 512 + ((b & 3) * 16 + (lane & 15)) * 8);
        GLD2C(gv, gmo, gm + (b >> 2) * 2048);
        DIAG_T(5);
        asm volatile("s_waitcnt vmcnt(0)" : "+v"(gv) :: "memory");
        if (hw == 0) {
#pragma unroll 1
            for (int spin = 0; spin < SPIN_MAX && !__all(gv[1] == TAG); ++spin) {
                GLD2C(gv, gmo, gm + (b >> 2) * 2048);
                asm volatile("s_waitcnt vmcnt(0)" : "+v"(gv) :: "memory");
            }
            float s = __uint_as_float(gv[0]);
            s = sw16sum(s, s); s = rbf(sw32sum(s, s));
            if (lane < 16) {
                const int n = 2048 + b * 16 + lane, sec = n >> 10;
                if (sec == 4) s = rbf(1.f + s);
                mod[n] = s;
                if (sec == 4) mod[M_GW + (n & 1023)] = s * bflo(wrb);
                if (sec == 3) mod[M_SW + (n & 1023)] = s * bflo(wrb);
            }
        }
        return;
    }
    bf16x8 ones;
#pragma unroll
    for (int j = 0; j < 8; ++j) ones[j] = ((j >> 2) == ((lane >> 2) & 1) && !(AONE && (lane & 2))) ? (short)0x3f80 : (short)0;
    const int six = (lane & 1) ? 0xEEEE : 0x4444;
    lds_cptr p = (lds_cptr)lds + lane * 16 + kh * 16384;
    f32x16 oX = f32x16{}, oY = f32x16{}, sX, sY; f32x4 lX = f32x4{}, lY = f32x4{};
    bf16x8 k0, k1, v0, v1 = bf16x8{}; u32x8 pp = u32x8{};
    PIN(oX); PIN(oY); PIN(lX); PIN(lY); PIN(v1); PIN(pp);
    asm volatile("s_waitcnt vmcnt(0)" : "+v"(qa), "+v"(qb), "+v"(qc), "+v"(qd) :: "memory");
    const bf16x8 x0 = __builtin_bit_cast(bf16x8, qa), x1 = __builtin_bit_cast(bf16x8, qb);
    const bf16x8 y0 = __builtin_bit_cast(bf16x8, qc), y1 = __builtin_bit_cast(bf16x8, qd);
    cnt[w] = 1;
    cnt_wait(cnt + kh * 4 + (lane & 3), 1);
    DIAG_T(0);
    k0 = ldsfrag(p); k1 = ldsfrag(p + 1024); v0 = ldsfrag(p + 2048);
    MFMA32_M4(sX, k0, x0); sX = MFMA32(k1, x1, sX);
#define AE(c, i) c[i] = __builtin_amdgcn_exp2f(c[i])
#define AC(c, i) pp[i] = pk2(c[2 * (i)], c[2 * (i) + 1])
#define AT_STEP(cur, nxt, op_, lp_, oc_, qa_, qb_, L1, L2, L3, L4) do {                    \
        SBAR(); op_ = MFMA32(v1, hi8(pp), op_); SBAR();                                    \
        L1; AE(cur, 0); AE(cur, 1); AE(cur, 2); PIN(cur); SBAR();                          \
        lp_ = SMF(ones, pp, lp_, six); SBAR();                                             \
        AE(cur, 3); AE(cur, 4); PIN(cur); SBAR();                                          \
        MFMA32_M4(nxt, k0, qa_); SBAR();                                                   \
        L2; AE(cur, 5); AE(cur, 6); AE(cur, 7); AC(cur, 0); AC(cur, 1); PIN(pp); PIN(cur); SBAR();      \
        nxt = MFMA32(k1, qb_, nxt); SBAR();                                                \
        L3; AC(cur, 2); AC(cur, 3); PIN(pp); SBAR(); AE(cur, 8); AE(cur, 9); PIN(cur); SBAR();          \
        oc_ = MFMA32(v0, lo8(pp), oc_); SBAR();                                            \
        L4; AE(cur, 10); AE(cur, 11); AE(cur, 12); AE(cur, 13); AE(cur, 14); AE(cur, 15);  \
        AC(cur, 4); AC(cur, 5); AC(cur, 6); AC(cur, 7); PIN(pp);                           \
    } while (0)
    const unsigned cme = lds0 + AT_CNT + w * 4;
#pragma unroll 1
    for (int c = 0; c < 8; ++c) {
        lds_cptr pn;
        unsigned cv, t0_, t1_;
#pragma unroll
        for (int t = 0; t < 4; ++t) {
            if (t == 3) {
                SBAR();
                asm volatile("s_waitcnt lgkmcnt(1)\n.Lac%=:\n\tv_cmp_gt_u32 vcc, %2, %0\n\ts_cbranch_vccz .Lad%=\n\tv_mov_b32 %0, %1\n\tds_read_b32 %0, %0\n\ts_waitcnt lgkmcnt(0)\n\ts_branch .Lac%=\n.Lad%=:"
                             : "+v"(cv) : "s"(lds0 + AT_CNT + 48), "s"(c < 7 ? c + 2 : 0) : "vcc", "memory");
                asm volatile("v_add_u32 %0, %1, %2" : "=v"(pn) : "s"((c & 3) == 3 ? -3 * AT_SLOT : AT_SLOT), "v"(p));
            }
            const lds_cptr pc = p + t * 4096, px = t < 3 ? p + (t + 1) * 4096 : pn;
#define AT_PRIO(n) asm volatile("s_cmp_eq_u32 %0, 0\n\ts_cbranch_scc1 .Lap%=\n\ts_setprio " #n "\n.Lap%=:" :: "s"(kh) : "scc")
            if (ABAL && t == 0) AT_PRIO(1);
            if (ABAL && ABAL == 2 * t) AT_PRIO(0);
            AT_STEP(sX, sY, oY, lY, oX, y0, y1, v1 = ldsfrag(pc + 3072), k0 = ldsfrag(px), k1 = ldsfrag(px + 1024),
                    if (t == 0) asm volatile("v_mov_b32 %0, %2\n\tv_mov_b32 %1, %3\n\tds_write_b32 %0, %1" : "=&v"(t0_), "=&v"(t1_) : "s"(cme), "s"(c + 2) : "memory"));
            if (ABAL == 2 * t + 1) AT_PRIO(0);
            AT_STEP(sY, sX, oX, lX, oY, x0, x1, , , if (t == 2) asm volatile("v_mov_b32 %0, %1\n\tds_read_b32 %0, %0" : "=&v"(cv) : "s"(lds0 + AT_CNT + 48) : "memory"), v0 = ldsfrag(px + 2048));
        }
        p = pn;
    }
    SBAR();
    oY = MFMA32(v1, hi8(pp), oY); lY = SMF(ones, pp, lY, six);
    DIAG_T(1);
    {
        const int ln = lane_id();
        cnt_wait(cnt + (ln & 7), 7);
        LAS f32x4* mine = (LAS f32x4*)((lds_ptr)lds + w * 4096) + ln;
#pragma unroll
        for (int g = 0; g < 4; ++g) mine[g * 64] = f32x4{oX[4 * g], oX[4 * g + 1], oX[4 * g + 2], oX[4 * g + 3]};
        ((LAS float*)((lds_ptr)lds + AT_LX))[w * 64 + ln] = lX[0] + lX[1];
        asm volatile("" ::: "memory");
        cnt[w] = 10;
        const LAS f32x4* other = (const LAS f32x4*)((lds_cptr)lds + (w ^ 4) * 4096) + ln;
        u32x4* op = (u32x4*)((char*)Of + ((size_t)(qt * 32 + head) * 2) * 1024) + ln;
        bool bad;
#pragma unroll 1
        for (int pass = 0;; ++pass) {
            if (pass) while (__any(cnt[w ^ 4] < 10u)) __builtin_amdgcn_s_sleep(ASL);
            const bool ready = !__any(cnt[w ^ 4] < 10u);
            asm volatile("" ::: "memory");
            const float l = (lY[0] + lY[1]) + ((const LAS float*)((lds_cptr)lds + AT_LX))[(w ^ 4) * 64 + ln];
            f32x2 o[8];
#pragma unroll
            for (int g = 0; g < 4; ++g) {
                const f32x4 v = other[g * 64];
                o[2 * g] = f32x2{oY[4 * g], oY[4 * g + 1]} + f32x2{v[0], v[1]}; o[2 * g + 1] = f32x2{oY[4 * g + 2], oY[4 * g + 3]} + f32x2{v[2], v[3]};
            }
            const float rl = __builtin_amdgcn_rcpf(l);
            u32x4 v[2];
#pragma unroll
            for (int j = 0; j < 8; ++j) { const f32x2 t = o[j] * rl; v[j >> 2][j & 3] = pk2(t[0], t[1]); }
            const f32x2 chk = ((o[0] + o[1]) + (o[2] + o[3])) + ((o[4] + o[5]) + (o[6] + o[7]));
            bad = __any(!((l >= 8.6736174e-19f) & (l <= 1.2676506e30f) & (fabsf(chk[0] + chk[1]) <= 3.0e38f)));
            asm volatile("" : "+v"(v[0]), "+v"(v[1]));
            if (ready) { op[0] = v[0]; op[64] = v[1]; break; }
        }
        if (__builtin_expect(bad, 0)) {
            const bf16x8* kp = (const bf16x8*)kvbase + ln;
            float mx = -INFINITY;
#pragma unroll 1
            for (int t = 0; t < 64; ++t) {
                f32x16 s = MFMA32(kp[t * 256], y0, f32x16{}); s = MFMA32(kp[t * 256 + 64], y1, s);
#pragma unroll
                for (int i = 0; i < 16; ++i) mx = fmaxf(mx, s[i]);
            }
            { auto s_ = __builtin_amdgcn_permlane32_swap(__float_as_uint(mx), __float_as_uint(mx), false, false); mx = fmaxf(__uint_as_float(s_[0]), __uint_as_float(s_[1])); }
            oY = f32x16{}; lY = f32x4{};
#pragma unroll 1
            for (int t = 0; t < 64; ++t) {
                f32x16 s = MFMA32(kp[t * 256], y0, f32x16{}); s = MFMA32(kp[t * 256 + 64], y1, s);
#pragma unroll
                for (int i = 0; i < 8; ++i) pp[i] = pk2(__builtin_amdgcn_exp2f(s[2 * i] - mx), __builtin_amdgcn_exp2f(s[2 * i + 1] - mx));
                oY = MFMA32(kp[t * 256 + 128], lo8(pp), oY); oY = MFMA32(kp[t * 256 + 192], hi8(pp), oY);
                lY = SMF(ones, pp, lY, six);
            }
            const float rl = 1.f / (lY[0] + lY[1]);
#pragma unroll
            for (int s = 0; s < 2; ++s) {
                u32x4 v;
#pragma unroll
                for (int j = 0; j < 4; ++j) v[j] = pk2(oY[8 * s + 2 * j] * rl, oY[8 * s + 2 * j + 1] * rl);
                op[s * 64] = v;
            }
        }
    }
    DIAG_END(3);
}
constexpr int OS = 24576, OV = 6 * OS, OST = OV + 3072, OBN = OST + 3072, OPT = OBN + 2048, OGS = OPT + 1024, O_LDS = OGS + 128;
#define OMAP 0
#define OROT 1
#define FF0 4
#define FF2 0
#define OSL3 16
#define OSLP 24
#define OSL2 0
#define OSPEC 1
#define OABL 0
constexpr int XOR7 = 0x141;
DI float oct_sum(float v) { v += dpp<XOR1>(v); v += dpp<XOR2>(v); v += dpp<XOR7>(v); return v; }
DI float dot2(unsigned a, unsigned b, float c) { return __builtin_amdgcn_fdot2_f32_bf16(__builtin_bit_cast(bf16x2_t, a), __builtin_bit_cast(bf16x2_t, b), c, false); }
__global__ __launch_bounds__(768, 3) void k_out(const u16* __restrict__ Of, const u16* __restrict__ Wo, const u16* __restrict__ x,
                                                const float* __restrict__ mean1, const float* __restrict__ mod,
                                                const char* __restrict__ wt, const u16* __restrict__ wd, char* __restrict__ gran,
                                                u16* __restrict__ out DIAG_ARG) {
    DIAG_BEGIN
    __shared__ __attribute__((aligned(16))) char lds[O_LDS];
    const int b = blockIdx.x, tid = threadIdx.x, lane = tid & 63;
    const int w = __builtin_amdgcn_readfirstlane(tid >> 6);
    const int xcd = b & 7, idx = b >> 3;
    const int rp = (xcd >> 1) * 8 + (idx & 7), cp = (xcd & 1) * 4 + (idx >> 3);
    const int mt = w & 1, nt = w >> 1, blk = lane >> 4, hh = lane >> 5;
    const unsigned lds0 = (unsigned)(uintptr_t)lds;
    const lds_ptr L = (lds_ptr)lds;
    if (w >= 8) {
        const int lw = w - 8;
        const char* pa = (const char*)Of + (size_t)(rp * 2 + (lw >> 1)) * 65536 + (lw & 1) * 2048;
        const char* pb = (const char*)Wo + ((size_t)lw * 16 * 1024 + cp * 128) * 2;
        const unsigned aoff = lane * 16;
        const unsigned boff = (unsigned)(((lane >> 3) * 1024 + ((lane & 7) ^ ((lane >> 2) & 4)) * 8) * 2);
        unsigned dst = lds0;
        int si = OROT * ((idx & 7) * 2 + (idx >> 3) * 4) & 15;
#define O_STAGE() do { const char* a_ = pa + si * 4096; const char* b_ = pb + (size_t)si * 131072; const unsigned d_ = dst + 8192 + lw * 4096;    \
                       glds16(a_, aoff, dst + lw * 2048); glds16(a_ + 1024, aoff, dst + lw * 2048 + 1024);                                        \
                       glds16(b_, boff, d_); glds16(b_ + 128, boff, d_ + 1024); glds16(b_ + 16384, boff, d_ + 2048); glds16(b_ + 16384 + 128, boff, d_ + 3072);  \
                       si = (si + 1) & 15; dst += OS; if (dst >= lds0 + OV) dst = lds0; } while (0)
        if (lw < 3) glds16(mod + M_GA + lw * 2048 + cp * 128, (unsigned)(hh * 4096 + (lane & 31) * 16), lds0 + OV + lw * 1024);
#pragma unroll 1
        for (int s = 0; s < 3; ++s) O_STAGE();
        WAIT_BAR(12);
#pragma unroll 1
        for (int s = 0; s < 2; ++s) O_STAGE();
        WAIT_BAR(18);
        O_STAGE();
#pragma unroll 1
        for (int s = 1; s < 11; ++s) { O_STAGE(); if (s < 10) WAIT_BAR(24); }
        WAIT_BAR(24); WAIT_BAR(18); WAIT_BAR(12); WAIT_BAR(6); WAIT_BAR(0);
        asm volatile("s_barrier" ::: "memory");
        DIAG_T(0);
        {
            u32x2 v[6];
            const char* gp = gran + (size_t)rp * 12288 + lw * 3072;
#pragma unroll 1
            for (int spin = 0; spin < SPIN_MAX; ++spin) {
#pragma unroll
                for (int i = 0; i < 6; ++i) GLD2C(v[i], (unsigned)(lane * 8), gp + i * 512);
                asm volatile("s_waitcnt vmcnt(0)" : "+v"(v[0]), "+v"(v[1]), "+v"(v[2]), "+v"(v[3]), "+v"(v[4]), "+v"(v[5]) :: "memory");
                if (__all((v[0][1] & v[1][1] & v[2][1] & v[3][1] & v[4][1] & v[5][1]) == TAG)) break;
            }
            DIAG_T(1);
            LAS u32x2* p = (LAS u32x2*)(L + 2 * OS + lane * 112 + lw * 8);
            p[0] = u32x2{v[0][0], v[3][0]}; p[4] = u32x2{v[1][0], v[4][0]}; p[8] = u32x2{v[2][0], v[5][0]};
        }
        LDS_BAR();
        DIAG_T(2);
        if (lw == 0) {
            const char* g2 = gran + G_H2;
            u32x2 v; u32x4 hv[4];
            int cur = 63; bool have = false;
            hv[0] = u32x4{}; hv[1] = u32x4{}; hv[2] = u32x4{}; hv[3] = u32x4{};
            __builtin_amdgcn_s_sleep(OSLP);
#pragma unroll 1
            for (int spin = 0; spin < SPIN_MAX; ++spin) {
                GLD2C(v, (unsigned)((lane & 31) * 128), g2 + 32 * 4096 + (b & (NREP - 1)) * 4096);
                if (!have && cur < 32) {
#pragma unroll
                    for (int i = 0; i < 4; ++i) GLD4C(hv[i], (unsigned)(lane * 16), g2 + cur * 4096 + i * 1024);
                }
                asm volatile("s_waitcnt vmcnt(0)" : "+v"(v), "+v"(hv[0]), "+v"(hv[1]), "+v"(hv[2]), "+v"(hv[3]) :: "memory");
                const bool ok = (v[1] & 0xffff0000u) == TAG;
                const float r = ok ? __uint_as_float(v[0]) : -INFINITY;
                const float wbest = wmax(r);
                const int win = (int)__builtin_ctzll(__ballot(ok && r == wbest) | (1ull << 63));
                have = win == cur && __all((hv[0][1] & hv[0][3] & hv[1][1] & hv[1][3] & hv[2][1] & hv[2][3] & hv[3][1] & hv[3][3]) == TAG);
                cur = __builtin_popcountll(__ballot(ok)) >= 2 * OSPEC ? win : 63;
                if (have && __all(ok)) break;
                if (OSL2) __builtin_amdgcn_s_sleep(OSL2);
            }
            if (lane == cur) *(LAS u32x2*)(L + OGS + 64) = u32x2{v[0], (unsigned)(cur * 64) + (v[1] & 63)};
            LAS u32x2* p = (LAS u32x2*)(L + OBN + (lane >> 3) * 64 + (lane & 3) * 16 + ((lane >> 2) & 1) * 8);
#pragma unroll
            for (int i = 0; i < 4; ++i) p[i * 64] = u32x2{hv[i][0], hv[i][2]};
        }
        DIAG_T(6);
        LDS_BAR();
        return;
    }
    const int row = rp * 64 + mt * 32 + (lane & 31);
    const unsigned xoff = (unsigned)((row * D_ + cp * 128 + nt * 32 + 8 * hh) * 2);
    u32x4 xi[2]; float ref; f32x2 gws, sws, gbv;
    const int ycol = w * 128 + (lane & 1) * 63 + lane;
    GLD4(xi[0], xoff, x); GLD4(xi[1], xoff, x + 16);
    GLD1(ref, (unsigned)(row * 4), mean1);
    GLD2(gws, (unsigned)(tid * 8), mod + M_GW); GLD2(sws, (unsigned)(tid * 8), mod + M_SW); GLD2(gbv, (unsigned)(ycol * 4), mod + M_GB);
    u32x4 fr[12];
    const int fc = ffn_unit(b);
    const char* pw = wt + (size_t)fc * WT_PITCH + w * 8192;
    const char* pd = (const char*)wd + (size_t)fc * 32768 + w * 4096 - 8192;
#define O_FL(j) GLD4(fr[j], (unsigned)(lane * 16), ((j) < 8 ? pw : pd) + (j) * 1024)
    const lds_cptr ap = (lds_cptr)lds + mt * 4096 + lane * 16;
    const lds_cptr bp = (lds_cptr)lds + 8192 + (nt >> 1) * 1024 + hh * 512 + ((lane & 15) >> 2) * 128 + ((nt ^ (lane >> 3)) & 1) * 64 + (blk & 1) * 32 + (lane & 3) * 8;
    f32x16 acc = f32x16{};
    asm volatile("s_waitcnt vmcnt(0)" : "+v"(xi[0]), "+v"(xi[1]), "+v"(ref), "+v"(gws), "+v"(sws), "+v"(gbv) :: "memory");
    DIAG_T(0);
    {
        const float a = wsum(gws[0] + gws[1]), c = wsum(sws[0] + sws[1]);
        if (lane == 0) *(LAS f32x2*)(L + OGS + w * 8) = f32x2{a, c};
    }
    float d[16];
#pragma unroll
    for (int j = 0; j < 2; ++j)
#pragma unroll
        for (int e = 0; e < 2; ++e) {
            const auto r = __builtin_amdgcn_permlane32_swap(xi[j][e], xi[j][2 + e], false, false);
            d[8 * j + 2 * e] = bflo(r[0]) - ref; d[8 * j + 2 * e + 1] = bfhi(r[0]) - ref;
            d[8 * j + 4 + 2 * e] = bflo(r[1]) - ref; d[8 * j + 5 + 2 * e] = bfhi(r[1]) - ref;
        }
    bf16x8 fa[2][4], fb[2][4];
    f32x4 va[4], vb[4];
    const lds_cptr vp = (lds_cptr)lds + OV + (nt * 32 + 4 * hh) * 4;
#pragma unroll
    for (int s = 0; s < 17; ++s) {
        if (s < 16) {
            LDS_BAR();
            int so = (s % 6) * OS;
            asm volatile("" : "+s"(so));
#pragma unroll
            for (int ks = 0; ks < 4; ++ks) { fa[s & 1][ks] = ldsfrag(ap + so + ks * 1024); fb[s & 1][ks] = cat4(trread(bp + so + ks * 4096), trread(bp + so + ks * 4096 + 2048)); }
            if (FF2 == 0) { if (s >= FF0 && s < FF0 + 12) O_FL(s - FF0); }
            else if (s >= FF2 && s < FF2 + 6) { O_FL(2 * (s - FF2)); O_FL(2 * (s - FF2) + 1); }
            SBAR();
        }
        if (s == 16) {
#pragma unroll
            for (int g = 0; g < 4; ++g) { va[g] = *(const LAS f32x4*)(vp + g * 32); vb[g] = *(const LAS f32x4*)(vp + 2048 + g * 32); }
            SBAR();
        }
        if (s > 0) {
#pragma unroll
            for (int ks = 0; ks < 4; ++ks) acc = MFMA32(fb[(s - 1) & 1][ks], fa[(s - 1) & 1][ks], acc);
            SBAR();
        }
    }
    DIAG_T(2);
    {
        float s1a = 0.f, s1b = 0.f, s2a = 0.f, s2b = 0.f, sda = 0.f, sdb = 0.f;
#pragma unroll
        for (int g = 0; g < 4; ++g)
#pragma unroll
            for (int e = 0; e < 4; e += 2) {
                const float v0 = __builtin_fmaf(va[g][e], acc[4 * g + e], d[4 * g + e]), v1 = __builtin_fmaf(va[g][e + 1], acc[4 * g + e + 1], d[4 * g + e + 1]);
                d[4 * g + e] = v0; d[4 * g + e + 1] = v1;
                s1a += v0; s1b += v1; s2a = __builtin_fmaf(v0, v0, s2a); s2b = __builtin_fmaf(v1, v1, s2b);
                sda = __builtin_fmaf(v0, vb[g][e], sda); sdb = __builtin_fmaf(v1, vb[g][e + 1], sdb);
            }
        const float q12 = sw32sum(s1a + s1b, s2a + s2b), sd = sda + sdb, q3 = sw32sum(sd, sd);
        const int o = OST + nt * 256 + (mt * 32 + (lane & 31)) * 4;
        *(LAS float*)(L + o + hh * 1024) = q12;
        *(LAS float*)(L + o + 2048) = q3;
    }
    asm volatile("s_waitcnt vmcnt(0)" : "+v"(fr[0]), "+v"(fr[1]), "+v"(fr[2]), "+v"(fr[3]), "+v"(fr[4]), "+v"(fr[5]), "+v"(fr[6]), "+v"(fr[7]), "+v"(fr[8]), "+v"(fr[9]), "+v"(fr[10]), "+v"(fr[11]) :: "memory");
    LDS_BAR();
    if (w < 3) {
        const LAS float* p = (const LAS float*)(L + OST + w * 1024) + lane;
        const float s = (p[0] + p[64]) + (p[128] + p[192]);
        const u32x2 v = {__float_as_uint(s), TAG};
        GST2C((unsigned)(cp * 1536 + w * 512 + lane * 8), v, gran + (size_t)rp * 12288);
    }
    DIAG_T(3);
    {
        LAS u32x4* p = (LAS u32x4*)(L + (w < 6 ? 4 * OS + w * 8192 : (w - 6) * 8192)) + lane;
#pragma unroll
        for (int j = 0; j < 8; ++j) p[j * 64] = fr[j];
        LAS u32x4* q = (LAS u32x4*)(L + 16384 + w * 4096) + lane;
#pragma unroll
        for (int j = 0; j < 4; ++j) q[j * 64] = fr[8 + j];
    }
    float gtot, ctot;
    {
        const f32x2 t = *(const LAS f32x2*)(L + OGS + (lane & 7) * 8);
        gtot = oct_sum(t[0]); ctot = oct_sum(t[1]);
    }
    f32x4 vc[4];
#pragma unroll
    for (int g = 0; g < 4; ++g) { va[g] = *(const LAS f32x4*)(vp + 512 + g * 32); vb[g] = *(const LAS f32x4*)(vp + 1024 + g * 32); vc[g] = *(const LAS f32x4*)(vp + 1536 + g * 32); }
    bf16x8 fw[12];
    {
        const lds_cptr p = (lds_cptr)lds + (w < 6 ? 4 * OS + w * 8192 : (w - 6) * 8192) + lane * 8;
#pragma unroll
        for (int j = 0; j < 8; ++j) fw[j] = cat4(trread(p + j * 1024), trread(p + j * 1024 + 512));
    }
    DIAG_T(4);
    LDS_BAR();
    DIAG_T(5);
    int brow; f32x2 fs;
    char* g2 = gran + G_H2;
    {
        const LAS f32x4* p = (const LAS f32x4*)(L + 2 * OS + lane * 112);
        const f32x4 a = p[0] + p[1], q = p[2] + p[3], c = p[4] + p[5];
        const float md = ((a[0] + a[1]) + (a[2] + a[3])) * (1.f / D_), s2 = ((q[0] + q[1]) + (q[2] + q[3])) * (1.f / D_), sd = (c[0] + c[1]) + (c[2] + c[3]);
        const float rstd = __builtin_amdgcn_rsqf(fmaxf(s2 - md * md, 0.f) + LN_EPS);
        float r = (sd - md * gtot) * rstd;
        r = r == r ? r : -INFINITY;
        const float best = wmax(r);
        brow = (int)__builtin_ctzll(__ballot(r == best) | (1ull << 63));
        if (w == 0 && cp == 0 && lane < NREP) { const u32x2 v = {__float_as_uint(best), TAG | (unsigned)brow}; GST2C((unsigned)(lane * 4096 + rp * 128), v, g2 + 32 * 4096); }
        const auto r0 = __builtin_amdgcn_permlane32_swap(__float_as_uint(rstd), __float_as_uint(rstd), false, false);
        const auto r1 = __builtin_amdgcn_permlane32_swap(__float_as_uint(-md * rstd), __float_as_uint(-md * rstd), false, false);
        fs = f32x2{__uint_as_float(mt ? r0[1] : r0[0]), __uint_as_float(mt ? r1[1] : r1[0])};
    }
    float h[16];
#pragma unroll
    for (int i = 0; i < 16; ++i) h[i] = __builtin_fmaf(__builtin_fmaf(d[i], fs[0], fs[1]), vb[i >> 2][i & 3], va[i >> 2][i & 3]);
    const bool mine = mt * 32 + (lane & 31) == brow;
    if ((brow >> 5) == mt) {
#pragma unroll
        for (int g = 0; g < 4; ++g) {
            const u32x4 v = {pk2(h[4 * g], h[4 * g + 1]), TAG, pk2(h[4 * g + 2], h[4 * g + 3]), TAG};
            if (mine) GST4C((unsigned)(rp * 4096 + (cp * 128 + nt * 32 + 8 * g + 4 * hh) * 4), v, g2);
        }
    }
    DIAG_T(6);
#pragma unroll
    for (int i = 0; i < 16; ++i) h[i] = __builtin_fmaf(vc[i >> 2][i & 3], h[i], d[i]) + ref;
    u32x4 o[2];
#pragma unroll
    for (int j = 0; j < 2; ++j) {
#pragma unroll
        for (int e = 0; e < 2; ++e) {
            const auto r = __builtin_amdgcn_permlane32_swap(pk2(h[8 * j + 2 * e], h[8 * j + 2 * e + 1]), pk2(h[8 * j + 4 + 2 * e], h[8 * j + 5 + 2 * e]), false, false);
            o[j][e] = r[0]; o[j][2 + e] = r[1];
        }
        if (!mine) *(u32x4*)((char*)(out + 16 * j) + xoff) = o[j];
    }
    {
        const lds_cptr q = (lds_cptr)lds + 16384 + ((lane & 15) >> 2) * 2048 + w * 256 + blk * 32 + (lane & 3) * 8;
#pragma unroll
        for (int j = 0; j < 4; ++j) fw[8 + j] = cat4(trread(q + (j & 1) * 16384 + (j >> 1) * 128), trread(q + (j & 1) * 16384 + (j >> 1) * 128 + 8192));
    }
    LDS_BAR();
    DIAG_T(7);
    const u32x2 wi = *(const LAS u32x2*)(L + OGS + 64);
    char* gy = gran + G_Y;
    if ((brow >> 5) == mt) {
        if ((int)(wi[1] >> 6) == rp) {
#pragma unroll
            for (int g = 0; g < 8; ++g) {
                const u32x4 v = {__float_as_uint(h[2 * g]), TAG, __float_as_uint(h[2 * g + 1]), TAG};
                if (mine) GST4C((unsigned)(224 * 8192 + (cp * 128 + nt * 32 + 8 * (g >> 1) + 4 * hh + 2 * (g & 1)) * 8), v, gy);
            }
        } else if (mine) {
            *(u32x4*)((char*)out + xoff) = o[0]; *(u32x4*)((char*)(out + 16) + xoff) = o[1];
        }
    }
    if (b >= 32) {
        f32x4 ag = f32x4{}, au = f32x4{};
#pragma unroll
        for (int q = 0; q < 4; ++q) {
            const bf16x8 a = ldsfrag((lds_cptr)lds + OBN + w * 256 + q * 64 + blk * 16);
            ag = MFMA16(a, fw[q], ag); au = MFMA16(a, fw[4 + q], au);
        }
        if (lane < 32) *(LAS float*)(L + OPT + (lane & 31) * 32 + w * 4) = lane < 16 ? ag[0] : au[0];
        const float sig = __builtin_amdgcn_rcpf(1.f + __expf(-(__uint_as_float(wi[0]) + ctot)));
        LDS_BAR();
        const LAS f32x4* pp = (const LAS f32x4*)(L + OPT + (lane & 15) * 32);
        const f32x4 a0 = pp[0] + pp[1], u0 = pp[32] + pp[33];
        const float sg = (a0[0] + a0[1]) + (a0[2] + a0[3]), su = (u0[0] + u0[1]) + (u0[2] + u0[3]);
        const float act = sg * su * __builtin_amdgcn_rcpf(1.f + __expf(-sg));
        const unsigned pr = pk2(act, dpp<XOR1>(act));
        float y0 = 0.f, y1 = 0.f, y2 = 0.f, y3 = 0.f;
#pragma unroll
        for (int i = 0; i < 8; i += 2) {
            const unsigned s0 = __builtin_amdgcn_readlane(pr, 2 * i), s1 = __builtin_amdgcn_readlane(pr, 2 * i + 2);
            const u32x4 w0 = __builtin_bit_cast(u32x4, fw[8 + (i >> 2)]), w1 = __builtin_bit_cast(u32x4, fw[10 + (i >> 2)]);
            y0 = dot2(s0, w0[i & 3], y0); y1 = dot2(s0, w1[i & 3], y1); y2 = dot2(s1, w0[(i & 3) + 1], y2); y3 = dot2(s1, w1[(i & 3) + 1], y3);
        }
        y0 += y2; y1 += y3;
        const float t = dpp<XOR1>((lane & 1) ? y0 : y1);
        const u32x4 v = {__float_as_uint(((lane & 1) ? t : y0) * (gbv[0] * sig)), TAG, __float_as_uint(((lane & 1) ? y1 : t) * (gbv[1] * sig)), TAG};
        GST4C((unsigned)((b - 32) * 8192 + ycol * 8), v, gy);
    } else __builtin_amdgcn_s_sleep(OSL3 + 10);
    DIAG_T(1);
    {
        const int cg = (b & 7) * 32 + (b >> 3), r = w * 32 + (lane >> 1);
        u32x4 v;
        __builtin_amdgcn_s_sleep(OSL3);
#pragma unroll 1
        for (int spin = 0; spin < SPIN_MAX; ++spin) {
            GLD4C(v, (unsigned)((r < 224 ? r : 224) * 8192 + (lane & 1) * 16), gy + cg * 32);
            asm volatile("s_waitcnt vmcnt(0)" : "+v"(v) :: "memory");
            if (__all((v[1] & v[3]) == TAG)) break;
        }
        float a = r > 224 ? 0.f : __uint_as_float(v[0]), c = r > 224 ? 0.f : __uint_as_float(v[2]);
        a = sw32sum(a, c);
        a = sw16sum(a, a); a += dpp<XOR2>(a); a += dpp<ROR4>(a); a += dpp<ROR8>(a);
        if ((lane & 30) == 0) *(LAS float*)(L + OST + w * 16 + (lane & 1) * 8 + hh * 4) = a;
        LDS_BAR();
        if (tid < 8) {
            f32x4 q = *(const LAS f32x4*)(L + OST + lane * 16);
#pragma unroll
            for (int e = 0; e < 4; ++e) q[e] = oct_sum(q[e]);
            if (tid == 0) *(u32x2*)(out + (size_t)(wi[1] & (S_ - 1)) * D_ + cg * 4) = u32x2{pk2(q[0], q[1]), pk2(q[2], q[3])};
        }
    }
    DIAG_END(4);
}

extern "C" void kernel_launch(void* const* d_in, const int* in_sizes, int n_in, void* d_out, int out_size, void* d_ws, size_t ws_size,
                              hipStream_t stream) {
    const u16* x = (const u16*)d_in[0];
    const u16* wqkv = (const u16*)d_in[1];
    const u16* wo = (const u16*)d_in[2];
    const u16* wr = (const u16*)d_in[4];
    const u16* wg = (const u16*)d_in[5];
    const u16* wu = (const u16*)d_in[6];
    const u16* wd = (const u16*)d_in[7];
    const u16* c = (const u16*)d_in[8];
    const u16* wmod = (const u16*)d_in[9];
    char* ws = (char*)d_ws;
    float* mod = (float*)(ws + WS_MOD);
    char* gran = ws + WS_GRAN;
    float* modp = (float*)(ws + WS_MODP);
    float* mean1 = (float*)(ws + WS_MEAN);
    u16* Qf = (u16*)(ws + WS_QF);
    u16* KVf = (u16*)(ws + WS_KVF);
    u16* Of = (u16*)(ws + WS_OF);
    u16* out = (u16*)d_out;
    u16* scb = (u16*)(ws + WS_SC);
    char* wt = ws + WS_WT;
    k_mod<<<256, 512, 0, stream>>>(c, wmod, scb, x, modp, mean1, ws + WS_XN, ws + WS_GM, wqkv DIAG_PASS);
    k_qkv<<<256, 768, 0, stream>>>(ws + WS_XN, modp, wqkv, Qf, KVf, wg, wu, wd, wmod DIAG_PASS);
    k_attn<<<256, 832, 0, stream>>>(Qf, KVf, Of, wg, wu, wd, wt, wmod, scb, wr, mod, wo, gran, ws + WS_GM DIAG_PASS);
    k_out<<<256, 768, 0, stream>>>(Of, wo, x, mean1, mod, wt, wd, gran, out DIAG_PASS);
}
```

```cpp
#include <hip/hip_runtime.h>
#include <hip/hip_bf16.h>
#include <stdint.h>

typedef __attribute__((ext_vector_type(8))) short bf16x8;
typedef __attribute__((ext_vector_type(4))) short s16x4;
typedef __attribute__((ext_vector_type(16))) float f32x16;
typedef __attribute__((ext_vector_type(4))) float f32x4;
typedef __attribute__((ext_vector_type(2))) float f32x2;
typedef __attribute__((ext_vector_type(4))) unsigned u32x4;
typedef __attribute__((ext_vector_type(2))) unsigned u32x2;
typedef __bf16 bf16x2_t __attribute__((ext_vector_type(2)));
typedef unsigned short u16;
typedef __attribute__((address_space(3))) const char* lds_cptr;
typedef __attribute__((address_space(3))) char* lds_ptr;

#define DI __device__ __forceinline__
#define MFMA32(a, b, c) __builtin_amdgcn_mfma_f32_32x32x16_bf16((a), (b), (c), 0, 0, 0)
#define MFMA16(a, b, c) __builtin_amdgcn_mfma_f32_16x16x32_bf16((a), (b), (c), 0, 0, 0)
#define SBAR() __builtin_amdgcn_sched_barrier(0)

constexpr int S_ = 2048, D_ = 1024, H_ = 32, F_ = 3584;
constexpr float LN_EPS = 1e-5f;
constexpr float QSCALE = 0.17677669529663687f * 1.4426950408889634f;

constexpr size_t WS_SC = 65536;
constexpr size_t WS_MEAN = 114688;
constexpr size_t WS_GRAN = 131072;
#define NREP 8
constexpr int G_H2 = 32 * 12288, G_BEST = G_H2 + 32 * 4096, G_Y = G_BEST + NREP * 4096, G_END = G_Y + 256 * 8192;
constexpr size_t WS_MODP = 5u << 20;
constexpr size_t WS_GM = 4u << 20;
constexpr int G_MF = 131072, G_MS = G_MF + 32768, G_FL = G_MS + 32768;
constexpr unsigned TAG = 0x5EED0000u;
constexpr int SPIN_MAX = 1 << 18;
constexpr size_t WS_QF = 6u << 20;
constexpr size_t WS_KVF = WS_QF + (4u << 20);
constexpr size_t WS_OF = WS_KVF + (8u << 20);
constexpr size_t WS_XN = WS_OF + (4u << 20);

#define DIAG_ARG
#define DIAG_BEGIN
#define DIAG_T(i)
#define DIAG_END(kid)
#define DIAG_PASS
#define DIAG_R(i)
#define DIAG_A(i)
#define DIAG_A0
DI float bflo(unsigned u) { return __uint_as_float(u << 16); }
DI float bfhi(unsigned u) { return __uint_as_float(u & 0xffff0000u); }
DI unsigned pk2(float lo, float hi) {
    f32x2 v = {lo, hi};
    return __builtin_bit_cast(unsigned, __builtin_convertvector(v, bf16x2_t));
}
DI float rbf(float x) { return __uint_as_float(pk2(0.f, x)); }
#define ATS 1
#define MIM1 1
#define MIMH 0
#define QMIX 1
DI float bf2f(u16 v) { return __uint_as_float((unsigned)v << 16); }
DI s16x4 trread(lds_cptr p) {
    typedef short v4i16_t __attribute__((ext_vector_type(4)));
    return __builtin_bit_cast(s16x4, __builtin_amdgcn_ds_read_tr16_b64_v4i16((__attribute__((address_space(3))) v4i16_t*)p));
}
DI bf16x8 ldsfrag(lds_cptr p) { return *(const __attribute__((address_space(3))) bf16x8*)p; }
DI bf16x8 cat4(s16x4 a, s16x4 b) { return __builtin_shufflevector(a, b, 0, 1, 2, 3, 4, 5, 6, 7); }
DI void glds16(const void* sbase, unsigned voff, unsigned lds_base) {
    unsigned sv;
    asm volatile("s_mov_b32 %0, m0\n\ts_mov_b32 m0, %3\n\ts_nop 0\n\tglobal_load_lds_dwordx4 %1, %2\n\ts_mov_b32 m0, %0"
                 : "=&s"(sv) : "v"(voff), "s"(sbase), "s"(lds_base) : "memory");
}
#define GLD4(dst, voff, sbase) asm volatile("global_load_dwordx4 %0, %1, %2" : "=v"(dst) : "v"(voff), "s"(sbase) : "memory")
#define GLD2(dst, voff, sbase) asm volatile("global_load_dwordx2 %0, %1, %2" : "=v"(dst) : "v"(voff), "s"(sbase) : "memory")
#define GLD1(dst, voff, sbase) asm volatile("global_load_dword %0, %1, %2" : "=v"(dst) : "v"(voff), "s"(sbase) : "memory")
#define GLDH(dst, voff, sbase) asm volatile("global_load_ushort %0, %1, %2" : "=v"(dst) : "v"(voff), "s"(sbase) : "memory")
#define GLD2C(dst, voff, sbase) asm volatile("global_load_dwordx2 %0, %1, %2 sc1" : "=v"(dst) : "v"(voff), "s"(sbase) : "memory")
#define GLD4C(dst, voff, sbase) asm volatile("global_load_dwordx4 %0, %1, %2 sc1" : "=v"(dst) : "v"(voff), "s"(sbase) : "memory")
#define GST2C(voff, val, sbase) asm volatile("global_store_dwordx2 %0, %1, %2 sc1" :: "v"(voff), "v"(val), "s"(sbase) : "memory")
#define GST4C(voff, val, sbase) asm volatile("global_store_dwordx4 %0, %1, %2 sc1\n\ts_nop 1" :: "v"(voff), "v"(val), "s"(sbase) : "memory")
#define WAIT_VM(n) asm volatile("s_waitcnt vmcnt(" #n ")" ::: "memory")
#define WAIT_BAR(n) asm volatile("s_waitcnt vmcnt(" #n ") lgkmcnt(0)\n\ts_barrier" ::: "memory")
#define LDS_BAR() asm volatile("s_waitcnt lgkmcnt(0)\n\ts_barrier" ::: "memory")
DI float xor32(float v) {
    auto r = __builtin_amdgcn_permlane32_swap(__float_as_uint(v), __float_as_uint(v), false, false);
    return (threadIdx.x & 32) ? __uint_as_float(r[0]) : __uint_as_float(r[1]);
}
DI float wave_sum(float v) {
#pragma unroll
    for (int o = 1; o < 64; o <<= 1) v += __shfl_xor(v, o);
    return v;
}
DI float wave_max(float v) {
#pragma unroll
    for (int o = 1; o < 64; o <<= 1) v = fmaxf(v, __shfl_xor(v, o));
    return v;
}

template <int CTRL> DI float dpp(float x) { return __builtin_bit_cast(float, __builtin_amdgcn_mov_dpp(__builtin_bit_cast(int, x), CTRL, 0xf, 0xf, true)); }
constexpr int XOR1 = 0xB1, XOR2 = 0x4E, ROR4 = 0x124, ROR8 = 0x128;
DI float sw32sum(float a, float b) {
    auto r = __builtin_amdgcn_permlane32_swap(__float_as_uint(a), __float_as_uint(b), false, false);
    return __uint_as_float(r[0]) + __uint_as_float(r[1]);
}
DI float sw16sum(float a, float b) {
    auto r = __builtin_amdgcn_permlane16_swap(__float_as_uint(a), __float_as_uint(b), false, false);
    return __uint_as_float(r[0]) + __uint_as_float(r[1]);
}
DI float row_sum16(float v) { v += dpp<XOR1>(v); v += dpp<XOR2>(v); v += dpp<ROR4>(v); v += dpp<ROR8>(v); return v; }
DI float row_max16(float v) { v = fmaxf(v, dpp<XOR1>(v)); v = fmaxf(v, dpp<XOR2>(v)); v = fmaxf(v, dpp<ROR4>(v)); v = fmaxf(v, dpp<ROR8>(v)); return v; }
DI float wsum(float v) { v = row_sum16(v); v = sw16sum(v, v); return sw32sum(v, v); }
DI float wmax(float m) {
    m = row_max16(m);
    { auto s_ = __builtin_amdgcn_permlane16_swap(__float_as_uint(m), __float_as_uint(m), false, false); m = fmaxf(__uint_as_float(s_[0]), __uint_as_float(s_[1])); }
    { auto s_ = __builtin_amdgcn_permlane32_swap(__float_as_uint(m), __float_as_uint(m), false, false); m = fmaxf(__uint_as_float(s_[0]), __uint_as_float(s_[1])); }
    return m;
}
DI int lane_id() { int l = __builtin_amdgcn_mbcnt_hi(~0u, __builtin_amdgcn_mbcnt_lo(~0u, 0u)); asm volatile("" : "+v"(l)); return l; }
DI float sfma(float a, float b, float c) { float r; asm("v_fma_f32 %0, %1, %2, %3" : "=v"(r) : "v"(a), "v"(b), "v"(c)); return r; }
#define LAS __attribute__((address_space(3)))
#define CPF 7
#define CODE_ADDR(sym) ({ unsigned lo_, hi_; asm volatile("s_getpc_b64 vcc\n\ts_add_u32 %0, vcc_lo, " #sym "@rel32@lo+4\n\ts_addc_u32 %1, vcc_hi, " #sym "@rel32@hi+12" : "=s"(lo_), "=s"(hi_) :: "vcc", "scc"); (const char*)(((unsigned long long)hi_ << 32) | lo_); })
#define SZ_AO 16384
#define SZ_QKV 6784
constexpr int SZ_MOD = 2560;

#define MQ 4
DI unsigned wpf_off(int id, int xcd, int lane) {
    const int r2 = (id / 3) * 2 + (lane >> 5), k = (r2 >> 5) * 64 + (xcd & 1) * 32 + (r2 & 31);
    return (unsigned)(k * 6144 + (id % 3) * 2048 + (xcd >> 1) * 512 + (lane & 31) * 16);
}
#define MODF 0
#define MPX 0
#define MBAR 3
#define MSL0 0
#define MSL 0
#define MSC1 "sc1"
#define MSP "sc1"
#define KXPOL ""
#define KMPOL "nt"
#define KWPOL ""
#define GLD4X(dst, voff, sbase, pol) asm volatile("global_load_dwordx4 %0, %1, %2 " pol : "=v"(dst) : "v"(voff), "s"(sbase) : "memory")
extern "C" __global__ __launch_bounds__(512) void k_mod(char* __restrict__ ws, const u16* __restrict__ x, const u16* __restrict__ c, const u16* __restrict__ wmod, const u16* __restrict__ W DIAG_ARG) {
    asm volatile("" :: "s"(ws), "s"(x), "s"(c), "s"(wmod), "s"(W));
    DIAG_BEGIN
    u16* __restrict__ scb = (u16*)(ws + WS_SC); float* __restrict__ modp = (float*)(ws + WS_MODP); float* __restrict__ mean1 = (float*)(ws + WS_MEAN);
    char* __restrict__ xn = ws + WS_XN; char* __restrict__ gm = ws + WS_GM;
    __shared__ __attribute__((aligned(16))) float red[8][32];
    __shared__ __attribute__((aligned(16))) unsigned xt[8][528];
    const int t = threadIdx.x, lane = t & 63, b = blockIdx.x;
    const int w = __builtin_amdgcn_readfirstlane(t >> 6);
    const int xcd = b & 7, i = b >> 3;
    const int cg = (xcd * 4 + (i >> 3)) * 2 + (i & 1), ks = (i >> 1) & 3;
    const int rg = ks * 256 + (t >> 2), row = (b >> 1) * 16 + (w >> 1) * 4 + (b & 1) * 2 + (w & 1);
    u32x4 wv[2], v[2], pf[3]; unsigned cv[2], cj0 = 0, cj1 = 0;
    if (CPF & 8) GLD1(cj0, (unsigned)(t * 128 < SZ_MOD ? t * 128 : 0), CODE_ADDR(k_mod));
    GLD4X(v[0], (unsigned)(lane * 16), x + (size_t)row * D_, KXPOL); GLD4X(v[1], (unsigned)(lane * 16), x + (size_t)row * D_ + 512, KXPOL);
    if (MBAR & 1) asm volatile("s_barrier" ::: "memory");
    if (MSL0) __builtin_amdgcn_s_sleep(MSL0);
    GLDH(cv[0], (unsigned)(rg * 2), c); GLDH(cv[1], (unsigned)(rg * 2), c + 128);
    GLD4X(wv[0], (unsigned)((rg * 6144 + cg * 32 + (t & 3) * 8) * 2), wmod, KMPOL); GLD4X(wv[1], (unsigned)((rg * 6144 + cg * 32 + (t & 3) * 8) * 2), wmod + 128 * 6144, KMPOL);
    if (MBAR & 2) asm volatile("s_barrier" ::: "memory");
    if (MSL) __builtin_amdgcn_s_sleep(MSL);
#pragma unroll
    for (int n = 0; n < 3; ++n) {
        const int id = n * 256 + i * 8 + w;
        GLD4X(pf[n], id < 192 * MQ ? wpf_off(id, xcd, lane) : 0u, W, KWPOL);
    }
    asm volatile("s_waitcnt vmcnt(7)" : "+v"(v[0]), "+v"(v[1]) :: "memory");
    DIAG_T(0);
    {
        float f[16], s = 0.f;
#pragma unroll
        for (int j = 0; j < 8; ++j) { f[2 * j] = bflo(v[j >> 2][j & 3]); f[2 * j + 1] = bfhi(v[j >> 2][j & 3]); s += f[2 * j] + f[2 * j + 1]; }
        const float mean = wsum(s) * (1.f / D_), mr = rbf(mean), dm = mean - mr;
        float q = 0.f;
#pragma unroll
        for (int j = 0; j < 16; ++j) { f[j] -= mr; q += f[j] * f[j]; }
#pragma unroll
        for (int j = 0; j < 16; ++j) f[j] = __uint_as_float(pk2(0.f, f[j]));
        const float rstd = __builtin_amdgcn_rcpf(rbf(__builtin_amdgcn_sqrtf(rbf(rbf(fmaxf(wsum(q) * (1.f / D_) - dm * dm, 0.f)) + LN_EPS))));
#pragma unroll
        for (int j = 0; j < 2; ++j) {
            u32x4 o;
#pragma unroll
            for (int e = 0; e < 4; ++e)
                o[e] = __builtin_bit_cast(unsigned, __builtin_amdgcn_cvt_pkrtz(rbf(f[8 * j + 2 * e] * rstd), rbf(f[8 * j + 2 * e + 1] * rstd)));
            *(u32x4*)&xt[w][j * 256 + lane * 4] = o;
        }
        if (lane == 0) xt[w][512] = __float_as_uint(mean);
    }
    LDS_BAR();
#pragma unroll
    for (int j = 0; j < 2; ++j) {
        const unsigned* p = &xt[(t >> 4) & 1][j * 256 + (t >> 5) * 16 + (t & 15)];
        const u32x4 o = {p[0], p[2 * 528], p[4 * 528], p[6 * 528]};
        asm volatile("global_store_dwordx4 %0, %1, %2 " MSC1 "\n\ts_nop 1" :: "v"((unsigned)((t >> 5) * 8192 + (t & 31) * 16)), "v"(o), "s"(xn + (size_t)(b >> 4) * 262144 + (b & 15) * 512 + j * 131072) : "memory");
    }
    DIAG_T(1);
    asm volatile("s_waitcnt vmcnt(7)" : "+v"(cv[0]), "+v"(cv[1]) :: "memory");
    float sc[2];
#pragma unroll
    for (int j = 0; j < 2; ++j) { const float cf = bflo(cv[j]); sc[j] = rbf(cf / (1.f + __expf(-cf))); }
    asm volatile("s_waitcnt vmcnt(5)" : "+v"(wv[0]), "+v"(wv[1]) :: "memory");
    {
        float acc[8];
#pragma unroll
        for (int j = 0; j < 4; ++j) { acc[2 * j] = sc[0] * bflo(wv[0][j]) + sc[1] * bflo(wv[1][j]); acc[2 * j + 1] = sc[0] * bfhi(wv[0][j]) + sc[1] * bfhi(wv[1][j]); }
#pragma unroll
        for (int j = 0; j < 4; ++j) acc[j] = sw32sum(acc[j], acc[4 + j]);
        f32x2 z;
#pragma unroll
        for (int j = 0; j < 2; ++j) {
            float q = sw16sum(acc[j], acc[2 + j]);
            q += dpp<ROR4>(q); q += dpp<ROR8>(q);
            z[j] = q;
        }
        if ((lane & 12) == 0) *(f32x2*)&red[w][(lane & 3) * 8 + (lane >> 5) * 4 + ((lane >> 4) & 1) * 2] = z;
    }
    LDS_BAR();
    if (t < 32) {
        asm volatile("global_store_dword %0, %1, %2 " MSP :: "v"(t * 4), "v"(((red[0][t] + red[1][t]) + (red[2][t] + red[3][t])) + ((red[4][t] + red[5][t]) + (red[6][t] + red[7][t]))), "s"(modp + ks * 2048 + cg * 32) : "memory");
    }
    if (w == 1) { const u32x4 z = u32x4{}; GST4C((unsigned)(b * 1024 + lane * 16), z, gm); }
    if (lane == 0) asm volatile("global_store_dword %0, %1, %2 " MSP :: "v"(0u), "v"(xt[w][512]), "s"(mean1 + row) : "memory");
    if (cg == 0 && (t & 3) == 0) {
#pragma unroll
        for (int j = 0; j < 2; ++j) asm volatile("global_store_short_d16_hi %0, %1, %2 " MSP :: "v"((128 * j + (rg & ~31) + ((rg >> 2) & 3) * 8 + ((rg >> 4) & 1) * 4 + (rg & 3)) * 2), "v"(sc[j]), "s"(scb) : "memory");
    }
    DIAG_T(2);
    if (CPF & 1) GLD1(cj1, (unsigned)(t * 128 < SZ_QKV ? t * 128 : 0), CODE_ADDR(k_qkv));
    asm volatile("s_waitcnt vmcnt(0)" : "+v"(pf[0]), "+v"(pf[1]), "+v"(pf[2]), "+v"(cj0), "+v"(cj1) :: "memory");
    DIAG_END(0);
}

#define QSLP 3
#define QGS 1
#define QPI 0
#define QTL 1
#define QNS 0
#define QTS 0
#define QRD 0
#define QD2 0
#define QLP 12
#define QLS ""
#define QPF 0
#define QPU 2
#define QPN 4
#define QPD 0
#define QPOL ""
#define QX2 0
typedef volatile LAS unsigned* lds_cnt;
constexpr int QA_SLOT = 8704, QB_SLOT = 12288, QB_OFF = 5 * QA_SLOT, QG_OFF = QB_OFF + 9 * QB_SLOT, QK_CNT = QG_OFF + 8192 + 1024, QK_LDS = QK_CNT + 64;
extern "C" __global__ __launch_bounds__(768, 3) void k_qkv(char* ws, const u16* W DIAG_ARG) {
    asm volatile("" :: "s"(ws), "s"(W));
    DIAG_BEGIN
    const char* __restrict__ xn = ws + WS_XN; const float* __restrict__ mod = (const float*)(ws + WS_MODP);
    u16* __restrict__ Qf = (u16*)(ws + WS_QF); u16* __restrict__ KVf = (u16*)(ws + WS_KVF);
    __shared__ __attribute__((aligned(16))) char lds[QK_LDS];
    const int b = blockIdx.x, tid = threadIdx.x, lane = tid & 63;
    const int w = __builtin_amdgcn_readfirstlane(tid >> 6);
    const int xcd = b & 7, idx = b >> 3;
    const int rp = (xcd & 1) * 8 + (idx & 7), cp = (xcd >> 1) * 4 + (idx >> 3);
    const unsigned lds0 = (unsigned)(uintptr_t)lds;
    const lds_cnt cnt = (lds_cnt)((lds_ptr)lds + QK_CNT);
#define Q_SPIN(f, p, need, sl) do { while (__any((int)(f) < (need))) { __builtin_amdgcn_s_sleep(sl); f = *(p); } asm volatile("" ::: "memory"); } while (0)
    if (w >= 4) {
        const int pw = w - 4, par = pw >> 2;
        unsigned boff = (unsigned)(((pw * 8 + (lane >> 3)) * 3072 + cp * 64 + ((lane & 7) ^ ((lane >> 2) & 4)) * 8) * 2);
        const char* wv = (const char*)W + 3072;
        unsigned dst = lds0 + QB_OFF + par * QB_SLOT + (pw & 3) * 3072;
        const unsigned dend = lds0 + QB_OFF + 9 * QB_SLOT;
#define QB_M0() asm volatile("s_mov_b32 m0, %0\n\ts_nop 0" :: "s"(dst) : "memory")
#define QB_Q() asm volatile("global_load_lds_dwordx4 %0, %1" :: "v"(boff), "s"(W) : "memory")
#define QB_K() asm volatile("global_load_lds_dwordx4 %0, %1 offset:2048" :: "v"(boff), "s"(W) : "memory")
#define QB_V() asm volatile("global_load_lds_dwordx4 %0, %1 offset:1024" :: "v"(boff), "s"(wv) : "memory")
#define QB_NEXT() do { boff += bst; dst += 2 * QB_SLOT; if (dst >= dend) dst -= 9 * QB_SLOT; } while (0)
        unsigned xoff = (unsigned)(rp * 262144 + pw * 2048 + lane * 16), xst = 16384, bst = 64 * 3072 * 2;
        u32x4 xs[4][2];
#define QX_0(q) asm volatile("global_load_dwordx4 %0, %1, %2" : "=v"(xs[q][0]) : "v"(xoff), "s"(xn) : "memory")
#define QX_1(q) do { asm volatile("global_load_dwordx4 %0, %1, %2 offset:1024" : "=v"(xs[q][1]) : "v"(xoff), "s"(xn) : "memory"); xoff += xst; } while (0)
        QX_0(0); QX_1(0); QB_M0(); QB_Q(); QB_K(); QB_V(); QB_NEXT();
        lds_cptr gp = (lds_cptr)lds + QG_OFF + par * 256 + (lane & 15) * 16;
        unsigned ad = lds0 + par * QA_SLOT + (pw & 3) * 2176;
        const lds_cnt fr = cnt + (lane & 3);
        DIAG_T(2);
        asm volatile("s_barrier" ::: "memory");
        QX_0(1); QX_1(1); QB_M0(); QB_Q(); QB_K(); QB_V(); QB_NEXT();
        if (QX2) { QX_0(2); QX_1(2); }
        DIAG_T(3);
        unsigned o[8], f, fl = par;
        lds_cnt cw = cnt + w;
        asm volatile("" : "+v"(cw));
        asm volatile(".Lqg%=:\n\tds_read_b32 %0, %1\n\ts_waitcnt lgkmcnt(0)\n\tv_cmp_gt_i32 vcc, 0, %0\n\ts_cbranch_vccz .Lqh%=\n\ts_sleep %2\n\ts_branch .Lqg%=\n.Lqh%=:"
                     : "=&v"(f) : "v"(fr), "n"(QGS) : "vcc", "memory");
        f32x4 g2[2] = {*(const LAS f32x4*)gp, *(const LAS f32x4*)(gp + 512)};
#define QA_M2(q, i) do { float t0, t1;                                                                            \
            asm volatile("v_fma_mix_f32 %0, %4, %6, %8 op_sel_hi:[1,0,0]\n\tv_fma_mix_f32 %2, %4, %7, %9 op_sel:[1,0,0] op_sel_hi:[1,0,0]\n\t"           \
                         "v_fma_mix_f32 %1, %5, %6, %8 op_sel_hi:[1,0,0]\n\tv_fma_mix_f32 %3, %5, %7, %9 op_sel:[1,0,0] op_sel_hi:[1,0,0]\n\t"           \
                         "v_cvt_pk_bf16_f32 %0, %0, %2\n\tv_cvt_pk_bf16_f32 %1, %1, %3"                           \
                         : "=&v"(o[i]), "=&v"(o[(i) + 1]), "=&v"(t0), "=&v"(t1)                                   \
                         : "v"(xs[q][(i) >> 2][(i) & 3]), "v"(xs[q][(i) >> 2][((i) & 3) + 1]), "v"(g2[(q) & 1][0]), "v"(g2[(q) & 1][1]), "v"(g2[(q) & 1][2]), "v"(g2[(q) & 1][3])); } while (0)
#define QA_WR(u, g, cn) asm volatile(".Lqp%=:\n\tv_cmp_gt_i32 vcc, %14, %2\n\ts_cbranch_vccz .Lqq%=\n\ts_sleep %16\n\tds_read_b32 %2, %15\n\ts_waitcnt lgkmcnt(0)\n\ts_branch .Lqp%=\n.Lqq%=:\n\t"   \
                             "s_mov_b32 m0, %3\n\tv_add_u32 %0, 2, %0\n\t"                                        \
                             "ds_write_addtid_b32 %6\n\tds_write_addtid_b32 %7 offset:272\n\tds_write_addtid_b32 %8 offset:544\n\tds_write_addtid_b32 %9 offset:816\n\t"                 \
                             "ds_write_addtid_b32 %10 offset:1088\n\tds_write_addtid_b32 %11 offset:1360\n\tds_write_addtid_b32 %12 offset:1632\n\tds_write_addtid_b32 %13 offset:1904\n\t"   \
                             "s_mov_b32 m0, %4\n\ts_waitcnt vmcnt(%17)\n\tds_write_b32 %5, %0"                    \
                             : "+v"(fl), "+v"(g), "+v"(f)                                                         \
                             : "s"(ad), "s"(dst), "v"(cw), "v"(o[0]), "v"(o[1]), "v"(o[2]), "v"(o[3]), "v"(o[4]), "v"(o[5]), "v"(o[6]), "v"(o[7]),       \
                               "s"((u) - 4), "v"(fr), "n"(QSLP), "n"(cn) : "vcc", "memory")
        asm volatile("s_waitcnt vmcnt(%2)" : "+v"(xs[0][0]), "+v"(xs[0][1]) : "n"(8 + 2 * QX2) : "memory");
        DIAG_T(0);
        SBAR(); QA_M2(0, 0); QA_M2(0, 2); QA_M2(0, 4); QA_M2(0, 6); SBAR();
        DIAG_T(1);
        QA_WR(par, g2[1], 5 + 2 * QX2);
        if (!QX2) { QX_0(2); QX_1(2); }
#pragma unroll 1
        for (int it = 0; it < 16; it += 4) {
#pragma unroll
            for (int q = 0; q < 4; ++q) {
                const int n = (q + 1) & 3, m = (q + 3) & 3;
                if (q == 3 && it == 12) break;
                if (q == 1 && it == 12) { xoff = 0; xst = 0; }
                if (q == 2 && it == 12) { boff = 0; bst = 0; }
                ad += 2 * QA_SLOT; if (ad >= lds0 + 5 * QA_SLOT) ad -= 5 * QA_SLOT;
                SBAR(); QB_Q(); SBAR();
                asm volatile("s_waitcnt vmcnt(6)" : "+v"(xs[n][0]), "+v"(xs[n][1]) :: "memory");
                SBAR(); QA_M2(n, 0); SBAR(); QB_K();
                SBAR(); QA_M2(n, 2); SBAR(); QB_V();
                SBAR(); QA_M2(n, 4); SBAR(); QX_0(m);
                g2[q & 1] = *(const LAS f32x4*)(gp + (q + 2) * 512);
                f = *fr;
                SBAR(); QA_M2(n, 6); SBAR(); QX_1(m);
                QB_NEXT();
                QA_WR(2 * (it + q + 1) + par, g2[q & 1], 7);
            }
            gp += 2048;
        }
        cnt[w] = 64;
        WAIT_VM(0);
#pragma unroll
        for (int q = 0; q < 4; ++q) asm volatile("" :: "v"(xs[q][0]), "v"(xs[q][1]));
        {
            const u32x4 z = u32x4{};
            const unsigned zo = (unsigned)((b * 512 + pw * 64 + lane_id()) * 16);
            GST4C(zo, z, ws + WS_GRAN);
            if (zo + (2u << 20) < (unsigned)G_END) GST4C(zo, z, ws + WS_GRAN + (2 << 20));
        }
    } else {
        const int rh = w & 1, hd = w >> 1, G = lane >> 4, hh = lane >> 5;
        lds_cptr a_ = (lds_cptr)lds + (rh * 16 + ((lane & 31) >> 2)) * 272 + (lane & 3) * 64 + hh * 16;
        lds_cptr b_ = (lds_cptr)lds + QB_OFF + hh * 3072 + ((lane & 15) >> 2) * 128 + ((hd ^ (lane >> 3)) & 1) * 64 + (G & 1) * 32 + (lane & 3) * 8;
        f32x16 acc[2][3];
#pragma unroll
        for (int i = 0; i < 2; ++i)
#pragma unroll
            for (int j = 0; j < 3; ++j) acc[i][j] = f32x16{};
        bf16x8 a0[2], b0[3], a1[2], b1[3];
#define QC_RB(bf, j, s) bf[j] = cat4(trread(b_ + (2 * (j) % 3) * 1024 + (s) * 6144), trread(b_ + (2 * (j) % 3) * 1024 + (s) * 6144 + 512))
#define QC_STEP(af, bf, an, bn, s, E2, E3, E5) do {                                                               \
            acc[0][0] = MFMA32(bf[0], af[0], acc[0][0]); SBAR();                                                  \
            an[0] = ldsfrag(a_ + (s) * 32); an[1] = ldsfrag(a_ + (s) * 32 + 2176); SBAR();                        \
            acc[0][1] = MFMA32(bf[1], af[0], acc[0][1]); SBAR();                                                  \
            QC_RB(bn, 0, s); E2; SBAR();                                                                          \
            acc[0][2] = MFMA32(af[0], bf[2], acc[0][2]); SBAR();                                                  \
            QC_RB(bn, 1, s); E3; SBAR();                                                                          \
            acc[1][0] = MFMA32(bf[0], af[1], acc[1][0]); SBAR();                                                  \
            QC_RB(bn, 2, s); SBAR();                                                                              \
            acc[1][1] = MFMA32(bf[1], af[1], acc[1][1]); SBAR();                                                  \
            asm volatile("" ::: "memory"); E5; asm volatile("" ::: "memory"); SBAR();                             \
            acc[1][2] = MFMA32(af[1], bf[2], acc[1][2]); SBAR();                                                  \
        } while (0)
        unsigned wjunk = 0;
#define Q_PF(r) do { const int l_ = lane < 6 * QPN ? lane : 0, i_ = l_ / 12, u_ = 2 * (i_ + QPU / 2) + (~xcd & 1), t_ = tid < 16 * QPN ? tid : 0;            \
            const unsigned o_ = w < 3 ? (unsigned)(rp * 262144 + (QPU + (t_ >> 4)) * 8192 + ((idx >> 3) * 16 + (t_ & 15)) * 128)                         \
                                      : (unsigned)((32 * u_ + 4 * (idx & 7) + (l_ % 12) / 3) * 6144 + (l_ % 3) * 2048 + cp * 128);                        \
            if (QPD) __builtin_amdgcn_s_sleep(QPD);                                                                                                       \
            asm volatile("global_load_dword %0, %1, %2 " QPOL : "+v"(wjunk) : "v"(o_), "s"(w < 3 ? xn : (const char*)W) : "memory"); } while (0)
        {
            f32x4 p[8];
#pragma unroll
            for (int j = 0; j < 8; ++j) GLD4(p[j], (unsigned)(tid * 16), mod + j * 1024);
            if (tid < 16) cnt[tid] = tid < 4 ? -64 : tid >> 3;
            asm volatile("" : "+v"(acc[0][0]), "+v"(acc[0][1]), "+v"(acc[0][2]), "+v"(acc[1][0]), "+v"(acc[1][1]), "+v"(acc[1][2]), "+v"(a_), "+v"(b_));
            if (QPF & 4) Q_PF(0);
            LDS_BAR();
            if (QPF & 1) Q_PF(0);
            if (QPF & 8) Q_PF(1);
            asm volatile("s_waitcnt vmcnt(%8)" : "+v"(p[0]), "+v"(p[1]), "+v"(p[2]), "+v"(p[3]), "+v"(p[4]), "+v"(p[5]), "+v"(p[6]), "+v"(p[7]) : "n"(!!(QPF & 5) + !!(QPF & 8)) : "memory");
            DIAG_T(2);
            f32x4 sa = (p[0] + p[2]) + (p[4] + p[6]), g = (p[1] + p[3]) + (p[5] + p[7]);
#pragma unroll
            for (int e = 0; e < 4; ++e) { sa[e] = rbf(sa[e]); g[e] = rbf(1.f + rbf(g[e])); }
            *(LAS f32x4*)((lds_ptr)lds + QG_OFF + tid * 32) = f32x4{g[0], g[1], sa[0], sa[1]};
            *(LAS f32x4*)((lds_ptr)lds + QG_OFF + tid * 32 + 16) = f32x4{g[2], g[3], sa[2], sa[3]};
            asm volatile("" ::: "memory");
            cnt[w] = 0;
        }
        DIAG_T(3);
        if (QPF & 2) Q_PF(1);
        const lds_cnt rd = cnt + 4 + (lane & 7);
        unsigned f = *rd;
        Q_SPIN(f, rd, 1, QGS);
        DIAG_T(0);
        int as = 1, bs = 1, da = QA_SLOT, db = QB_SLOT;
        unsigned un = 1;
        a0[0] = ldsfrag(a_); a0[1] = ldsfrag(a_ + 2176);
#pragma unroll
        for (int j = 0; j < 3; ++j) QC_RB(b0, j, 0);
        f = *rd;
        if (CPF & 2) asm volatile("global_load_dword %0, %1, %2" : "+v"(wjunk) : "v"((unsigned)(tid * 128 < SZ_AO ? tid * 128 : 0)), "s"(CODE_ADDR(k_ao)) : "memory");
        const char* qb = (const char*)Qf + (size_t)((cp * 2 + hd) * 64 + rp * 4 + rh * 2) * 2048;
        const char* kb = (const char*)KVf + (size_t)((cp * 2 + hd) * 64 + rp * 4 + rh * 2) * 4096;
        asm volatile("" : "+s"(qb), "+s"(kb));
        SBAR(); __builtin_amdgcn_s_waitcnt(0xC07F); SBAR();
#pragma unroll 2
        for (int u = 0; u < (QTL ? 28 : 32); ++u) {
            QC_STEP(a0, b0, a1, b1, 1, a_ += da; asm volatile("" : "+v"(a_)), asm volatile("" : "+v"(un)), cnt[w] = un; b_ += db; asm volatile("" : "+v"(b_)));
            SBAR();
            asm volatile(".Lqc%=:\n\tv_cmp_gt_i32 vcc, %2, %0\n\ts_cbranch_vccz .Lqd%=\n\tds_read_b32 %0, %1\n\ts_waitcnt lgkmcnt(0)\n\ts_branch .Lqc%=\n.Lqd%=:"
                         : "+v"(f) : "v"(rd), "s"(u + 2) : "vcc", "memory");
            SBAR();
            QC_STEP(a1, b1, a0, b0, 0, da = QA_SLOT; if (++as == 5) { as = 0; da = -4 * QA_SLOT; } asm volatile("" : "+s"(da), "+s"(as)),
                    db = QB_SLOT; if (++bs == 9) { bs = 0; db = -8 * QB_SLOT; } asm volatile("" : "+s"(db), "+s"(bs)), f = *rd; ++un);
        }
        {
            bf16x8 ta[42], tb[21];
            u32x4 ov[12];
#define QT_M(n) ((n) % 7 + 1)
#define QT_J(p) ((p) < 2 ? 1 : (p) < 4 ? 0 : 2)
#define QT_A(t) ta[t] = ldsfrag(a_ + ((QT_M(t) / 2 + 3) % 5) * QA_SLOT + (QT_M(t) & 1) * 32 + ((t) / 7 & 1) * 2176)
#define QT_BO(n) ((QT_M(n) / 2) * QB_SLOT + (QT_M(n) & 1) * 6144 + (2 * QT_J((n) / 7 * 2) % 3) * 1024)
#define QT_B(n) tb[n] = cat4(trread(b_ + QT_BO(n)), trread(b_ + QT_BO(n) + 512))
#define QT_P(p, e) do { float c0_ = acc[(p) & 1][QT_J(p)][2 * (e)], c1_ = acc[(p) & 1][QT_J(p)][2 * (e) + 1];                                          \
                        if (QT_J(p) == 0) { float t_; asm("v_mul_f32 %0, %4, %2\n\tv_mul_f32 %1, %4, %3\n\tv_cvt_pk_bf16_f32 %0, %0, %1" : "=&v"(ov[2 * (p) + ((e) >> 2)][(e) & 3]), "=&v"(t_) : "v"(c0_), "v"(c1_), "s"(QSCALE)); }   \
                        else ov[2 * (p) + ((e) >> 2)][(e) & 3] = pk2(c0_, c1_); } while (0)
#define QT_S2(p, h, pol) do { if (QT_J(p) == 0) asm volatile("global_store_dwordx4 %0, %1, %2 offset:%3 " pol :: "v"(so), "v"(ov[2 * (p) + (h)]), "s"(qb), "n"(((p) & 1) * 2048 + (h) * 1024) : "memory");      \
                        else asm volatile("global_store_dwordx4 %0, %1, %2 offset:%3 " pol :: "v"(QT_J(p) == 1 ? sk : so), "v"(ov[2 * (p) + (h)]), "s"(kb + ((p) & 1) * 4096), "n"((h) * 1024 + (QT_J(p) - 1) * 2048) : "memory"); } while (0)
#define QT_S(p, h) do { if (2 * (p) + (h) >= QLP) QT_S2(p, h, QLS); else QT_S2(p, h, "sc1"); } while (0)
            acc[0][0] = MFMA32(b0[0], a0[0], acc[0][0]); SBAR();
            a_ -= 3 * QA_SLOT; QT_B(0); asm volatile("" : "+v"(a_)); QT_A(0); SBAR();
            acc[1][0] = MFMA32(b0[0], a0[1], acc[1][0]); SBAR();
            asm volatile(".Lqe%=:\n\tv_cmp_gt_i32 vcc, 33, %0\n\ts_cbranch_vccz .Lqf%=\n\tds_read_b32 %0, %1\n\ts_waitcnt lgkmcnt(0)\n\ts_branch .Lqe%=\n.Lqf%=:" : "+v"(f) : "v"(rd) : "vcc", "memory");
            unsigned so = lane_id() * 16; asm volatile("s_waitcnt vmcnt(0)" : "+v"(wjunk), "+v"(so) :: "memory"); SBAR();
            unsigned sk;
            acc[0][1] = MFMA32(b0[1], a0[0], acc[0][1]); SBAR();
            QT_B(1); QT_A(1); SBAR();
            acc[1][1] = MFMA32(b0[1], a0[1], acc[1][1]); SBAR();
            QT_B(2); QT_A(2); SBAR();
            acc[0][2] = MFMA32(a0[0], b0[2], acc[0][2]); SBAR();
            QT_B(3); QT_A(3); sk = ATS ? (so & ~192u) | ((so & 64) << 1) | ((so & 128) >> 1) : so; asm volatile("" : "+v"(sk)); SBAR();
            acc[1][2] = MFMA32(a0[1], b0[2], acc[1][2]); SBAR();
            QT_B(4); QT_A(4);
#pragma unroll
            for (int t = 0; t < 42; ++t) {
                const int p = t / 7, m = t % 7, n = (p >> 1) * 7 + m;
                SBAR();
                if (p < 4) acc[p & 1][QT_J(p)] = MFMA32(tb[n], ta[t], acc[p & 1][QT_J(p)]); else acc[p & 1][2] = MFMA32(ta[t], tb[n], acc[p & 1][2]);
                SBAR();
                if (t < 2) QT_B(t + 5); else if (t >= 8 && t < 22) QT_B(t - 1);
                if (t + 5 < 42) QT_A(t + 5);
                if (p > 0 && m >= 2 && m < 6) { QT_P(p - 1, 2 * m - 4); QT_P(p - 1, 2 * m - 3); }
                if (p > 0 && m == 4) QT_S(p - 1, 0);
                if (p > 1 && m == QTS) QT_S(p - 2, 1);
                if (t == 41) QT_S(4, 1);
            }
            SBAR();
            DIAG_T(1);
#pragma unroll
            for (int h = 0; h < 2; ++h) { QT_P(5, 4 * h); QT_P(5, 4 * h + 1); QT_P(5, 4 * h + 2); QT_P(5, 4 * h + 3); QT_S(5, h); }
            asm volatile("" :: "v"(ov[9]), "v"(ov[10]), "v"(ov[11]));
        }
    }
    DIAG_END(2);
}

#define FFIRST 1
#define FSKIP 1
#define FROT 0
#define BE 8
#define BP 1
#define PKW 1
#define BALL 0
#define H2S 0
#define H2P 2
#define OSL4 0
#define WPUB 1
#define BRT 10
#define NPOLL 1
#define PST 6
#define HRS 2
#define FDRY 1
#define BRW 6
#define BRP 2
#define BRS 2
#define APL 4
#define LP1 2
#define OMAP 1
#define PRE 8
#define OSD 8
#define OSW 0
#define OCO 1
#define FSP ""
#define GF 0
#define NHB 0
#define XLD 6
constexpr int AT_SLOT = 32768, XA = 0, KV0 = 3 * 8192, AT_STG = XA, AT_RED = AT_STG + 4 * 4224, AT_LX = AT_RED + 1024, AT_CNT = KV0 + 4 * AT_SLOT;
constexpr int OV = AT_CNT + 128, OST = OV + 2688, OPT = OST + 3072, OFS = OPT + 1024, OGS = OFS + 512, AO_LDS = OGS + 128;
constexpr int PK_D = 65536, PK_S = 98304, OBN = PK_S + 4096;
typedef __attribute__((ext_vector_type(8))) unsigned u32x8;
typedef __attribute__((ext_vector_type(8))) __bf16 b8_t;
typedef __attribute__((ext_vector_type(16))) __bf16 b16_t;
#define SMF(a, b, c, ix) __builtin_amdgcn_smfmac_f32_16x16x64_bf16(__builtin_bit_cast(b8_t, a), __builtin_bit_cast(b16_t, b), (c), (ix), 0, 0)
typedef __attribute__((ext_vector_type(8))) float f32x8;
#define SMF32(a, b, c) __builtin_amdgcn_smfmac_f32_32x32x32_bf16(__builtin_bit_cast(b8_t, a), __builtin_bit_cast(b16_t, b), (c), six, 0, 0)
DI f32x8 lof(f32x16 p) { return __builtin_shufflevector(p, p, 0, 1, 2, 3, 4, 5, 6, 7); }
DI f32x8 hif(f32x16 p) { return __builtin_shufflevector(p, p, 8, 9, 10, 11, 12, 13, 14, 15); }
DI bf16x8 lo8(u32x8 p) { const u32x4 r = {p[0], p[1], p[2], p[3]}; return __builtin_bit_cast(bf16x8, r); }
DI bf16x8 hi8(u32x8 p) { const u32x4 r = {p[4], p[5], p[6], p[7]}; return __builtin_bit_cast(bf16x8, r); }
#define PIN(x) asm volatile("" : "+v"(x))
#define MFMA32_M4(d, a, b) asm volatile("v_mfma_f32_32x32x16_bf16 %0, %1, %2, -4.0" : "=&v"(d) : "v"(a), "v"(b))
#define HP 6
#define ABAL 6
#define HW0 4
#define HPRIO 2
#define ASL 2
#define LPRIO 3
#define GW0 8
#define FSL 48
#define PW0 8
#define PSL 0
#define BPACE 0
#define PFP 20
#define PW1 4
#define PFPOL ""
#define HW1 8
#define HP2 0
#define BLATE 1
#define AW 6
#define FPOL ""
#define PFON 0
#define OSP "sc1"
#define DPOL ""
#define MPOL "nt"
#define GLD4D(dst, voff, sbase) asm volatile("global_load_dwordx4 %0, %1, %2 " DPOL : "=v"(dst) : "v"(voff), "s"(sbase) : "memory")
#define GLD4M(dst, voff, sbase) asm volatile("global_load_dwordx4 %0, %1, %2 " MPOL : "=v"(dst) : "v"(voff), "s"(sbase) : "memory")
#define GLD4P(dst, voff, sbase) asm volatile("global_load_dwordx4 %0, %1, %2 " FPOL : "=v"(dst) : "v"(voff), "s"(sbase) : "memory")
#define HTH 2
#define OP0 12
#define OB0 24
#define OSPIN 1
#define OROT 1
#define OSL3 2
#define OSPEC 20
DI int ffn_unit(int b) { const int i = b < 32 ? b >> 3 : (b >> 3) - 4; return (b & 7) * 28 + i; }
DI unsigned cnt_wait(lds_cnt p, unsigned target) {
    unsigned v;
    do { v = *p; } while (__any(v < target));
    asm volatile("" ::: "memory");
    return v;
}
template <int SL> DI void cnt_sleep(lds_cnt p, unsigned target) {
    while (__any(*p < target)) __builtin_amdgcn_s_sleep(SL);
    asm volatile("" ::: "memory");
}
constexpr int XOR7 = 0x141;
DI float oct_sum(float v) { v += dpp<XOR1>(v); v += dpp<XOR2>(v); v += dpp<XOR7>(v); return v; }
DI float dot2(unsigned a, unsigned b, float c) { return __builtin_amdgcn_fdot2_f32_bf16(__builtin_bit_cast(bf16x2_t, a), __builtin_bit_cast(bf16x2_t, b), c, false); }
DI u32x4 tr4(u32x4 v) {
    const auto a = __builtin_amdgcn_permlane32_swap(v[0], v[2], false, false), c = __builtin_amdgcn_permlane32_swap(v[1], v[3], false, false);
    const auto p = __builtin_amdgcn_permlane16_swap(a[0], c[0], false, false), q = __builtin_amdgcn_permlane16_swap(a[1], c[1], false, false);
    return u32x4{p[0], p[1], q[0], q[1]};
}
constexpr int bslot(int s) { return ((s & 7) ^ ((s & 4) >> 1)) * 16384; }
#define BAR() asm volatile("s_barrier" ::: "memory")
extern "C" __global__ __launch_bounds__(1024) void k_ao(char* ws, const u16* wg, const u16* wu, const u16* wd, const u16* wmod, const u16* wr, const u16* Wo, const u16* x, u16* out DIAG_ARG) {
    asm volatile("" :: "s"(ws), "s"(wg), "s"(wu), "s"(wd), "s"(wmod), "s"(wr), "s"(Wo), "s"(x), "s"(out));
    DIAG_BEGIN
    const char* __restrict__ Qf = ws + WS_QF; const char* __restrict__ KVf = ws + WS_KVF; char* Of = ws + WS_OF;
    const u16* __restrict__ scb = (const u16*)(ws + WS_SC); const float* __restrict__ mean1 = (const float*)(ws + WS_MEAN);
    char* gran = ws + WS_GRAN; char* gm = ws + WS_GM;
    __shared__ __attribute__((aligned(16))) char lds[AO_LDS];
    const int b = blockIdx.x, tid = threadIdx.x, lane = tid & 63;
    const int w = __builtin_amdgcn_readfirstlane(tid >> 6);
    const unsigned lds0 = (unsigned)(uintptr_t)lds;
    const lds_ptr L = (lds_ptr)lds;
    const lds_cnt cnt = (lds_cnt)(L + AT_CNT);
    const int head = (b & 7) * 4 + ((b >> 3) & 3);
    const char* kvbase = KVf + (size_t)head * 262144;
    const int kh = (w >> 2) & 1, qt = (b >> 5) * 8 + (w & 3) * 2 + kh;
    const int xcd = b & 7, idx = b >> 3;
    const int rp = xcd * 4 + (idx & 3), cp = idx >> 2;
    const int si0 = (cp * 2 + (idx & 3) * 4 + (OMAP - 1) * (idx & 1)) & 15;
    const int c0 = xcd * 128 + idx * 2;
    u32x4 qa, qb, qc, qd;
    if (w < 8) {
        const char* qp = Qf + (size_t)(head * 64 + qt) * 2048;
        const char* qx = Qf + (size_t)(head * 64 + (qt ^ 1)) * 2048;
        const unsigned voff = (unsigned)(w * 4096 + lane * 16), d_ = lds0 + KV0 + w * 4096;
        GLD4(qa, (unsigned)(lane * 16), qx); GLD4(qb, (unsigned)(lane * 16), qx + 1024);
        glds16(kvbase, voff, d_); glds16(kvbase + 1024, voff, d_ + 1024); glds16(kvbase + 2048, voff, d_ + 2048); glds16(kvbase + 3072, voff, d_ + 3072);
        GLD4(qc, (unsigned)(lane * 16), qp); GLD4(qd, (unsigned)(lane * 16), qp + 1024);
    } else if (tid < 544) cnt[lane] = 0;
    else if (tid < 548) *(LAS unsigned*)(L + OGS + 64 + (tid & 3) * 4) = 0;
    LDS_BAR();
    if (w >= 12) {
        const int lw = w - 12, lane = lane_id();
        const unsigned l8 = lane * 8, l16 = lane * 16;
        __builtin_amdgcn_s_setprio(LP1);
        if (lw == 0) {
            __builtin_amdgcn_s_setprio(LPRIO);
#pragma unroll 1
            for (int c = 1; c < 8; ++c) {
                if (c > 3) cnt_sleep<2>(cnt + (lane & 7), c - 1);
#pragma unroll
                for (int i = 0; i < 8; ++i)
                    asm volatile("s_mov_b32 m0, %2\n\ts_nop 0\n\tglobal_load_lds_dwordx4 %0, %1\n\tglobal_load_lds_dwordx4 %0, %1 offset:1024\n\t"
                                 "global_load_lds_dwordx4 %0, %1 offset:2048\n\tglobal_load_lds_dwordx4 %0, %1 offset:3072"
                                 :: "v"(l16), "s"(kvbase + c * AT_SLOT + i * 4096), "s"(lds0 + KV0 + (c & 3) * AT_SLOT + i * 4096) : "memory");
                WAIT_VM(32);
                cnt[12] = c;
            }
            WAIT_VM(0);
            cnt[12] = 8;
        } else if (lw == 2) {
            unsigned wrb; u32x2 gv;
            asm volatile("global_load_ushort %0, %1, %2\n\ts_waitcnt vmcnt(0)" : "=v"(wrb) : "v"(((b * 16 + (lane & 15)) & 1023) * 2), "s"(wr) : "memory");
            cnt_sleep<8>(cnt + 8 + (lane & 3), 1);
            const LAS float* p = (const LAS float*)(L + AT_RED) + lane;
            const u32x2 v = {__float_as_uint((p[0] + p[64]) + (p[128] + p[192])), TAG};
            GST2C(l8, v, gm + b * 512);
            const unsigned gmo = (unsigned)((lane >> 4) * 512 + ((b & 3) * 16 + (lane & 15)) * 8);
#pragma unroll 1
            for (int spin = 0; spin < SPIN_MAX; ++spin) {
                __builtin_amdgcn_s_sleep(FSL);
                asm volatile("global_load_dwordx2 %0, %1, %2 sc1\n\ts_waitcnt vmcnt(0)" : "=v"(gv) : "v"(gmo), "s"(gm + (b >> 2) * 2048) : "memory");
                if (__all(gv[1] == TAG)) break;
            }
            float s = __uint_as_float(gv[0]);
            s = sw16sum(s, s); s = rbf(sw32sum(s, s));
            if ((b >> 6) == 2) s = rbf(1.f + s);
            const float ps = row_sum16(s * bflo(wrb));
            const u32x2 o = {__float_as_uint(lane < 16 ? s : ps), TAG};
            if (lane < 17) GST2C(lane < 16 ? l8 : 32768u, o, gm + G_MF + b * 128);
        }
#define B_LD(s, q) do { const char* b_ = pb + (size_t)((si0 + (s)) & 15) * 131072; const unsigned d_ = lds0 + KV0 + bslot(s) + (q) * 4096;              \
                     glds16(b_, boff, d_); glds16(b_ + 128, boff, d_ + 1024); glds16(b_ + 16384, boff, d_ + 2048); glds16(b_ + 16384 + 128, boff, d_ + 3072); } while (0)
        {
            u32x4 br[16];
            const char* pb = (const char*)Wo + ((size_t)lw * 16 * 1024 + cp * 128) * 2;
            const unsigned boff = (unsigned)((lane >> 3) * 2048 + ((lane & 7) ^ ((lane >> 2) & 4)) * 16);
            cnt_sleep<16>(cnt, BRW);
#pragma unroll
            for (int n = 0; n < 16; ++n) { GLD4(br[n], boff, pb + (size_t)((si0 + 4 + (n >> 2)) & 15) * 131072 + (n & 1) * 128 + (n & 2) * 8192); if (BRP) __builtin_amdgcn_s_sleep(BRP); }
            asm volatile("s_waitcnt vmcnt(0)" : "+v"(br[0]), "+v"(br[1]), "+v"(br[2]), "+v"(br[3]), "+v"(br[4]), "+v"(br[5]), "+v"(br[6]), "+v"(br[7]),
                         "+v"(br[8]), "+v"(br[9]), "+v"(br[10]), "+v"(br[11]), "+v"(br[12]), "+v"(br[13]), "+v"(br[14]), "+v"(br[15]) :: "memory");
            cnt_sleep<8>(cnt + (lane & 7), 7); B_LD(0, lw); B_LD(1, lw);
            cnt_sleep<8>(cnt + (lane & 7), 8); B_LD(2, lw); B_LD(3, lw);
            if (lw == 1) {
                unsigned wr2; u32x4 v[5]; u32x2 u[2];
                asm volatile("global_load_dword %0, %1, %2\n\ts_waitcnt vmcnt(0)" : "=v"(wr2) : "v"(lane * 4), "s"(wr + cp * 128) : "memory");
                const char* g = gm + G_MF + cp * 1024;
                const unsigned o4 = (unsigned)(192 * 128 + (GF ? c0 + (lane & 3) * 2 + (lane & 4) * 16 : c0 + (lane & 1) * 64) * 8), os = (unsigned)(lane * 128);
#pragma unroll 1
                for (int spin = 0; spin < SPIN_MAX; ++spin) {
                    asm volatile("global_load_dwordx4 %0, %7, %10 sc1\n\tglobal_load_dwordx4 %1, %7, %11 sc1\n\tglobal_load_dwordx4 %2, %7, %12 sc1\n\tglobal_load_dwordx4 %3, %7, %13 sc1\n\t"
                                 "global_load_dwordx4 %4, %8, %14 sc1\n\tglobal_load_dwordx2 %5, %9, %15 sc1\n\tglobal_load_dwordx2 %6, %9, %16 sc1\n\ts_waitcnt vmcnt(0)"
                                 : "=&v"(v[0]), "=&v"(v[1]), "=&v"(v[2]), "=&v"(v[3]), "=&v"(v[4]), "=&v"(u[0]), "=&v"(u[1])
                                 : "v"(l16), "v"(o4), "v"(os), "s"(g), "s"(g + 8192), "s"(g + 16384), "s"(g + 24576), "s"(gm + G_MF), "s"(gm + G_MS + 8192), "s"(gm + G_MS + 16384) : "memory");
                    if (__all((v[0][1] & v[0][3] & v[1][1] & v[1][3] & v[2][1] & v[2][3] & v[3][1] & v[3][3] & v[4][1] & v[4][3] & u[0][1] & u[1][1]) == TAG)) break;
                    __builtin_amdgcn_s_sleep(8);
                }
#pragma unroll
                for (int q = 0; q < 4; ++q) *(LAS u32x2*)(L + OV + q * 512 + l8) = u32x2{v[q][0], v[q][2]};
                *(LAS f32x2*)(L + OV + 2048 + l8) = f32x2{__uint_as_float(v[2][0]) * bflo(wr2), __uint_as_float(v[2][2]) * bfhi(wr2)};
                if (lane < (GF ? 8 : 2)) *(LAS u32x2*)(L + OV + 2560 + l8) = u32x2{v[4][0], v[4][2]};
                const float ct = wsum(__uint_as_float(u[0][0])), gt = wsum(__uint_as_float(u[1][0]));
                if (lane == 0) *(LAS f32x2*)(L + OGS) = f32x2{gt, ct};
                DIAG_R(0);
            }
            WAIT_VM(0);
            cnt[20 + lw] = 1;
            LAS u32x4* p = (LAS u32x4*)(L + KV0 + 65536 + lw * 4096) + lane;
            cnt_sleep<16>(cnt + 8 + (lane & 3), 2);
            cnt_sleep<16>(cnt, 9);
            __builtin_amdgcn_s_setprio(3);
            cnt_sleep<BRS>(cnt + (lane & 7), BRT);
#pragma unroll
            for (int n = 0; n < 8; ++n) p[(bslot(4 + (n >> 2)) - 65536) / 16 + (n & 3) * 64] = br[n];
            cnt_sleep<BRS>(cnt + (lane & 7), 11);
#pragma unroll
            for (int n = 8; n < 16; ++n) p[(bslot(4 + (n >> 2)) - 65536) / 16 + (n & 3) * 64] = br[n];
            asm volatile("" ::: "memory");
            cnt[20 + lw] = 2;
        }
        if (lw == 3) {
            cnt_sleep<1>(cnt + 13, PW0);
            DIAG_R(0);
            unsigned f = 0; unsigned long long mb = 0xffffffffull;
            const unsigned fo = (unsigned)((lane & 31) * 128);
#pragma unroll 1
            for (int spin = 0; spin < SPIN_MAX; ++spin) {
                asm volatile("s_mov_b64 exec, %2\n\tglobal_load_dword %0, %1, %3 sc1\n\ts_mov_b64 exec, -1\n\ts_waitcnt vmcnt(0)"
                             : "+v"(f) : "v"(fo), "s"(mb), "s"(gm + G_FL + (rp >> 2) * 4096) : "memory");
                mb = ~__ballot(f == TAG) & 0xffffffffull;
                if (!mb) break;
            }
            cnt[14] = 1;
            DIAG_R(1);
        } else cnt_sleep<1>(cnt + 14, 1);
        {
            const char* pa = Of + (size_t)(rp * 2 + (lw >> 1)) * 65536 + (lw & 1) * 2048;
            asm volatile("s_mov_b32 m0, %0" :: "s"(lds0 + XA + lw * 2048) : "memory");
            u32x4 ar[12][2];
#define A_LD(s) asm volatile("global_load_dwordx4 %0, %2, %3 sc1\n\tglobal_load_dwordx4 %1, %2, %3 offset:1024 sc1" : "=&v"(ar[(s) % 12][0]), "=&v"(ar[(s) % 12][1]) : "v"(l16), "s"(pa + ((si0 + (s)) & 15) * 4096) : "memory")
#define A_ISS(s) ((s) == 0 ? AW : (AW + 2 * (s) < (s) + 12 ? (AW + 2 * (s) < 16 ? AW + 2 * (s) : 16) : ((s) + 12 < 16 ? (s) + 12 : 16)))
#pragma unroll
            for (int s = 0; s < AW; ++s) A_LD(s);
#define A_W4(v_, o_) asm volatile("ds_write_addtid_b32 %0 offset:%4\n\tds_write_addtid_b32 %1 offset:%5\n\tds_write_addtid_b32 %2 offset:%6\n\tds_write_addtid_b32 %3 offset:%7"      \
                             :: "v"(v_[0]), "v"(v_[1]), "v"(v_[2]), "v"(v_[3]), "n"(o_), "n"((o_) + 256), "n"((o_) + 512), "n"((o_) + 768) : "memory")
#define A_WR(s, iss) do { asm volatile("s_waitcnt vmcnt(%2)" : "+v"(ar[(s) % 12][0]), "+v"(ar[(s) % 12][1]) : "n"(2 * ((iss) - (s) - 1)) : "memory");      \
                          A_W4(ar[(s) % 12][0], ((s) % 3) * 8192); A_W4(ar[(s) % 12][1], ((s) % 3) * 8192 + 1024); } while (0)
#pragma unroll
            for (int s = 0; s < 16; ++s) {
                if (s <= APL) A_WR(s, A_ISS(s));
                if (s >= APL && s < 15) { A_WR(s + 1, A_ISS(s)); asm volatile("s_waitcnt lgkmcnt(8)\n\ts_barrier" ::: "memory"); }
                else LDS_BAR();
#pragma unroll
                for (int n = A_ISS(s); n < A_ISS(s + 1); ++n) A_LD(n);
            }
            BAR();
        }
        const char* g2 = gran + G_H2;
        if (lw == 0) {
            u32x4 v[8]; u32x2 u[8];
            const char* gp = gran + (size_t)rp * 12288;
            unsigned need = 0xffu & ~(1u << cp);
#define O_P1(p, s, o0, o1) "s_bitcmp0_b32 %20, " #p "\n\ts_cbranch_scc1 .Lop" #p "_%=\n\tglobal_load_dwordx4 %" #p ", %16, %" #s " offset:" #o0 " sc1\n\tglobal_load_dwordx2 %[u" #p "], %17, %" #s " offset:" #o1 " sc1\n.Lop" #p "_%=:\n\t"
            const float gtot = *(const LAS float*)(L + OGS);
            BAR();
            DIAG_R(4);
            float own[3];
#pragma unroll
            for (int q = 0; q < 3; ++q) { const LAS float* p = (const LAS float*)(L + OST + q * 1024) + lane; own[q] = (p[0] + p[64]) + (p[128] + p[192]); }
#pragma unroll
            for (int p = 0; p < 8; ++p) { v[p] = u32x4{__float_as_uint(own[0]), 0, __float_as_uint(own[1]), 0}; u[p] = u32x2{__float_as_uint(own[2]), 0}; }
            if (PKW == 1) { asm volatile("" :: "v"(own[0]), "v"(own[1]), "v"(own[2]) : "memory"); *(volatile LAS u32x2*)(cnt + 26) = u32x2{1, 1}; }
            if (OP0) __builtin_amdgcn_s_sleep(OP0);
#pragma unroll 1
            for (int spin = 0; spin < SPIN_MAX; ++spin) {
                asm volatile(O_P1(0, 18, -3072, -2048) O_P1(1, 18, -1536, -512) O_P1(2, 18, 0, 1024) O_P1(3, 18, 1536, 2560)
                             O_P1(4, 19, -3072, -2048) O_P1(5, 19, -1536, -512) O_P1(6, 19, 0, 1024) O_P1(7, 19, 1536, 2560) "s_waitcnt vmcnt(0)"
                             : "+v"(v[0]), "+v"(v[1]), "+v"(v[2]), "+v"(v[3]), "+v"(v[4]), "+v"(v[5]), "+v"(v[6]), "+v"(v[7]),
                               [u0] "+v"(u[0]), [u1] "+v"(u[1]), [u2] "+v"(u[2]), [u3] "+v"(u[3]), [u4] "+v"(u[4]), [u5] "+v"(u[5]), [u6] "+v"(u[6]), [u7] "+v"(u[7])
                             : "v"(l16), "v"(l8), "s"(gp + 3072), "s"(gp + 9216), "s"(need) : "scc", "memory");
#pragma unroll
                for (int p = 0; p < 8; ++p) if (__all((v[p][1] & v[p][3] & u[p][1]) == TAG)) need &= ~(1u << p);
                if (!need) break;
            }
            DIAG_R(5);
            float s[3];
#pragma unroll
            for (int q = 0; q < 3; ++q) {
                float e[8];
#pragma unroll
                for (int p = 0; p < 8; ++p) e[p] = __uint_as_float(q == 2 ? u[p][0] : v[p][2 * q]);
                s[q] = ((e[0] + e[4]) + (e[1] + e[5])) + ((e[2] + e[6]) + (e[3] + e[7]));
            }
            const float md = s[0] * (1.f / D_), s2 = s[1] * (1.f / D_);
            const float rstd = __builtin_amdgcn_rsqf(fmaxf(s2 - md * md, 0.f) + LN_EPS);
            float r = (s[2] - md * gtot) * rstd;
            *(LAS f32x2*)(L + OFS + l8) = f32x2{rstd, -md * rstd};
            r = r == r ? r : -INFINITY;
            const float best = wmax(r);
            const unsigned brow = (unsigned)__builtin_ctzll(__ballot(r == best) | (1ull << 63));
            asm volatile("" ::: "memory");
            *(volatile LAS unsigned*)(L + OGS + 72) = 64 + brow;
            if ((BALL || cp == 0) && lane < NREP) { const u32x2 g = {TAG + 2047 - rp * 64 - brow, __float_as_uint(best) ^ ((unsigned)((int)__float_as_uint(best) >> 31) | 0x80000000u)}; GST2C((unsigned)(lane * 4096 + rp * 128), g, g2 + 32 * 4096); }
            DIAG_R(6);
        } else {
            BAR();
            if (lw > 3 - NPOLL) {
                u32x2 v = u32x2{};
                if (H2S) while (!__any(*(volatile LAS unsigned*)(L + OGS + 72))) __builtin_amdgcn_s_sleep(2);
                __builtin_amdgcn_s_sleep(OB0);
                if (lw < 3) __builtin_amdgcn_s_sleep(PST);
                if (lw < 2) __builtin_amdgcn_s_sleep(PST);
                const unsigned bo = (unsigned)((lane & 31) * 128);
                unsigned long long mb = 0xffffffffull, mp = 0;
#pragma unroll 1
                for (int spin = 0; spin < SPIN_MAX; ++spin) {
                    asm volatile("s_mov_b64 exec, %2\n\tglobal_load_dwordx2 %0, %1, %3 sc1\n\ts_mov_b64 exec, -1\n\ts_waitcnt vmcnt(0)"
                                 : "+v"(v) : "v"(bo), "s"(mb), "s"(g2 + 32 * 4096 + (b & (NREP - 1)) * 4096) : "memory");
                    const unsigned long long bk = __ballot((v[0] & 0xffff0000u) == TAG);
                    mb = ~bk & 0xffffffffull;
                    if (bk != mp && __builtin_popcountll(bk) >= OSPEC) {
                        asm volatile("s_mov_b64 exec, %2\n\tds_max_u64 %0, %1\n\ts_mov_b64 exec, -1" :: "v"(lds0 + OGS + 64), "v"(v), "s"(bk & ~mp) : "memory");
                        mp = bk;
                    }
                    if (!mb || (NPOLL > 1 && __any(*(volatile LAS unsigned*)(L + OGS + 76)))) break;
                    if (H2P) __builtin_amdgcn_s_sleep(H2P);
                }
                *(volatile LAS unsigned*)(L + OGS + 76) = 1;
                DIAG_R(6);
            }
        }
        return;
    }
    if (w >= 8) {
        const int hw = w - 8, lane = lane_id();
        __builtin_amdgcn_s_setprio(HPRIO);
        const lds_ptr sp = L + AT_STG + hw * 4224;
        const unsigned moff = (unsigned)(((lane >> 3) * 6144 + (lane & 7) * 8) * 2);
        const char* pm = (const char*)wmod + ((size_t)((b & 3) * 256 + hw * 64) * 6144 + 2048 + (b >> 2) * 64) * 2;
        u32x4 af[2], ra[8], fr[24];
#define PACE() __builtin_amdgcn_s_sleep(HP)
        const int fc = ffn_unit(b);
        const size_t go = ((size_t)hw * 256 * F_ + fc * 16) * 2;
        const char* pd = (const char*)wd + (size_t)fc * 32768 + hw * 512;
        const unsigned goff = FSKIP && b < 32 ? 0u : (unsigned)(((lane >> 1) * F_ + (lane & 1) * 8) * 2), doff = FSKIP && b < 32 ? 0u : (unsigned)((lane >> 4) * 2048 + (lane & 15) * 16);
        if (HW0) cnt_sleep<2>(cnt + 12, HW0);
#pragma unroll
        for (int q = 0; q < 2; ++q) GLD4(af[q], (unsigned)((lane >> 4) * 16), scb + (b & 3) * 256 + hw * 64 + q * 32);
#pragma unroll
        for (int q = 0; q < 8; ++q) { GLD4M(ra[q], moff, pm + (size_t)q * 8 * 6144 * 2); PACE(); }
        asm volatile("s_waitcnt vmcnt(0)" : "+v"(ra[0]), "+v"(ra[1]), "+v"(ra[2]), "+v"(ra[3]), "+v"(ra[4]), "+v"(ra[5]), "+v"(ra[6]), "+v"(ra[7]), "+v"(af[0]), "+v"(af[1]) :: "memory");
        DIAG_R(0);
        {
            f32x4 m[4] = {f32x4{}, f32x4{}, f32x4{}, f32x4{}};
            const lds_ptr wp = sp + ((lane >> 1) & 3) * 1056 + (lane >> 3) * 32 + (lane & 1) * 16;
#pragma unroll
            for (int h = 0; h < 2; ++h) {
#pragma unroll
                for (int q = 0; q < 4; ++q) *(LAS u32x4*)(wp + q * 256) = ra[4 * h + q];
#pragma unroll
                for (int q = 0; q < 4; ++q)
                    m[q] = MFMA16(__builtin_bit_cast(bf16x8, af[h]), cat4(trread(sp + q * 1056 + lane * 8), trread(sp + q * 1056 + 512 + lane * 8)), m[q]);
                asm volatile("" ::: "memory");
            }
            const float lo = (lane & 16) ? m[1][0] : m[0][0], hi = (lane & 16) ? m[3][0] : m[2][0];
            *(LAS float*)(L + AT_RED + hw * 256 + lane * 4) = (lane & 32) ? hi : lo;
            cnt[w] = 1;
        }
        {
            const lds_cptr rg = sp + lane * 8, rd = sp + ((lane & 15) >> 2) * 256 + (lane >> 4) * 32 + (lane & 3) * 8;
#define H_N(t) ((t) % 3 == 2 ? 16 + (t) / 3 : (t) / 3 * 2 + (t) % 3)
#pragma unroll
            for (int t = 0; t < 24 + HTH; ++t) {
                const int n = H_N(t), m = H_N(t - HTH);
                if (t < 24)
                    asm volatile("s_mov_b32 m0, %2\n\ts_waitcnt lgkmcnt(0)\n\tglobal_load_lds_dwordx4 %0, %1 " FPOL :: "v"(n < 16 ? goff : doff),
                                 "s"(n < 16 ? ((n & 4) ? (const char*)wu : (const char*)wg) + go + (size_t)((n >> 3) * 128 + (n & 3) * 32) * F_ * 2 : pd + ((n >> 2) & 1) * 256 + (n & 3) * 8192),
                                 "s"(lds0 + AT_STG + hw * 4224 + (t & 3) * 1024) : "memory");
                asm volatile("s_waitcnt vmcnt(%0)" :: "n"(t < 24 ? HTH : 23 + HTH - t) : "memory");
                if (t >= HTH) {
                    const lds_cptr r = (m < 16 ? rg : rd) + ((t - HTH) & 3) * 1024;
                    fr[m] = __builtin_bit_cast(u32x4, cat4(trread(r), trread(r + (m < 16 ? 512 : 128))));
                }
            }
            DIAG_R(1);
            asm volatile("" ::: "memory");
            cnt[w] = 2;
        }
        DIAG_R(2);
        {
            const char* pb = (const char*)Wo + ((size_t)hw * 16 * 1024 + cp * 128) * 2;
            const unsigned boff = (unsigned)((lane >> 3) * 2048 + ((lane & 7) ^ ((lane >> 2) & 4)) * 16);
            const lds_cnt ca = cnt + (lane & 7);
            DIAG_R(3);
            cnt_sleep<16>(cnt, 9);
            cnt_sleep<2>(ca, 12);
            if (BLATE) cnt_sleep<2>(cnt + 13, 8);
#define B_ISS(s) (8 + BE + BP * (s) < 16 ? 8 + BE + BP * (s) : 16)
#pragma unroll
            for (int n = 8; n < B_ISS(0); ++n) { if (n == 12) cnt_sleep<2>(ca, 13); B_LD(n, hw); }
            DIAG_R(4);
#pragma unroll
            for (int s = 0; s < 16; ++s) {
                if (s < 8) BAR(); else asm volatile("s_waitcnt vmcnt(%0)\n\ts_barrier" :: "n"(4 * (B_ISS(s) - s - 1)) : "memory");
#pragma unroll
                for (int n = B_ISS(s); n < B_ISS(s + 1); ++n) B_LD(n, hw);
            }
            BAR();
        }
        BAR();
        {
            const int ln = lane_id();
            const float ctot = __uint_as_float(__builtin_amdgcn_readfirstlane(*(const LAS unsigned*)(L + OGS + 4)));
            LAS u32x2* hp = (LAS u32x2*)(L + OBN + hw * 512 + (ln >> 3) * 64 + (ln & 3) * 16 + ((ln >> 2) & 1) * 8);
            const lds_cptr hr = (lds_cptr)lds + OBN + hw * 512 + (ln >> 4) * 16;
            unsigned last = FDRY; int dl = 0;
            if (FDRY) cnt_sleep<1>(cnt + 24 + (ln & 3), 1);
#pragma unroll 1
            for (int spin = 0; spin < SPIN_MAX; ++spin) {
                const unsigned fin = *(volatile LAS unsigned*)(L + OGS + 76);
                const u32x2 c = *(volatile LAS u32x2*)(L + OGS + 64);
                const unsigned id = __builtin_amdgcn_readfirstlane(c[0]), lg = __builtin_amdgcn_readfirstlane(c[1]);
                if (id == last) {
                    if (--dl == 0) cnt[16 + hw] = id;
                    if (dl <= 0 && __any(fin)) break;
                    __builtin_amdgcn_s_sleep(OSPIN); continue;
                }
                u32x4 hv;
                asm volatile("global_load_dwordx4 %0, %1, %2 sc1\n\ts_waitcnt vmcnt(0)" : "=v"(hv) : "v"(ln * 16), "s"(gran + G_H2 + hw * 1024 + ((~id >> 6) & 31) * 4096) : "memory");
                if (id && !__all((hv[1] & hv[3]) == TAG)) { if (HRS) __builtin_amdgcn_s_sleep(HRS); continue; }
                DIAG_R(6);
                *hp = u32x2{hv[0], hv[2]};
                asm volatile("" ::: "memory");
                f32x4 ag = f32x4{}, au = f32x4{};
                bf16x8 ah[8];
                ah[0] = ldsfrag(hr); ah[1] = ldsfrag(hr + 64); ah[2] = ldsfrag(hr + 128);
#pragma unroll
                for (int q = 0; q < 8; ++q) {
                    SBAR();
                    ag = MFMA16(ah[q], __builtin_bit_cast(bf16x8, fr[(q >> 2) * 8 + (q & 3)]), ag);
                    au = MFMA16(ah[q], __builtin_bit_cast(bf16x8, fr[(q >> 2) * 8 + 4 + (q & 3)]), au);
                    SBAR();
                    if (q < 5) ah[q + 3] = ldsfrag(hr + (q + 3) * 64);
                }
                float sig = __builtin_amdgcn_rcpf(1.f + __expf(-(__uint_as_float(lg ^ ((unsigned)((int)~lg >> 31) | 0x80000000u)) + ctot)));
                asm volatile("" : "+v"(sig));
                if (ln < 32) *(LAS float*)(L + OPT + ln * 16 + hw * 4) = ln < 16 ? ag[0] : au[0];
                asm volatile("" ::: "memory");
                cnt[28 + hw] = id;
                f32x4 a0, u0; unsigned f, n;
                asm volatile(".Lfa%=:\n\tds_read_b32 %2, %5\n\tds_read_b128 %0, %6\n\tds_read_b128 %1, %6 offset:256\n\tds_read_b32 %3, %7\n\ts_waitcnt lgkmcnt(0)\n\t"
                             "v_cmp_ne_u32 vcc, %8, %2\n\ts_cbranch_vccz .Lfb%=\n\tv_cmp_ne_u32 vcc, %8, %3\n\ts_cbranch_vccnz .Lfb%=\n\t"
                             "s_add_u32 %4, %4, 1\n\ts_cmp_lt_i32 %4, %9\n\ts_cbranch_scc1 .Lfa%=\n.Lfb%=:"
                             : "=&v"(a0), "=&v"(u0), "=&v"(f), "=&v"(n), "+s"(spin)
                             : "v"(lds0 + AT_CNT + 112 + (ln & 3) * 4), "v"(lds0 + OPT + (ln & 15) * 16), "v"(lds0 + OGS + 64), "s"(id), "n"(SPIN_MAX) : "vcc", "scc", "memory");
                if (__any(f != id || n != id)) continue;
                const float sg = (a0[0] + a0[1]) + (a0[2] + a0[3]), su = (u0[0] + u0[1]) + (u0[2] + u0[3]);
                const float act = sg * su * __builtin_amdgcn_rcpf(1.f + __expf(-sg));
                const unsigned pr = pk2(act, dpp<XOR1>(act));
                float y[4] = {0.f, 0.f, 0.f, 0.f};
                unsigned s0[8];
#pragma unroll
                for (int i = 0; i < 8; ++i) s0[i] = __builtin_amdgcn_readlane(pr, 2 * i);
                SBAR();
#pragma unroll
                for (int i = 0; i < 8; ++i)
#pragma unroll
                    for (int j = 0; j < 4; ++j) y[j] = dot2(s0[i], fr[16 + (j >> 1) * 4 + (i >> 1)][(j & 1) * 2 + (i & 1)], y[j]);
#pragma unroll
                for (int j = 0; j < 2; ++j) {
                    const u32x4 v = {__float_as_uint(y[2 * j] * sig), id, __float_as_uint(y[2 * j + 1] * sig), id};
                    if (b >= 32 && id) GST4C(ln * 16, v, gran + G_Y + (b - 32) * 8192 + hw * 2048 + j * 1024);
                }
                last = id; dl = OSL3;
                DIAG_R(7);
            }
        }
        DIAG_R(5);
        return;
    }
    {
        bf16x8 ones;
#pragma unroll
        for (int j = 0; j < 8; ++j) ones[j] = ((j >> 2) == ((lane >> 2) & 1) && !(lane & (ATS ? 3 : 2))) ? (short)0x3f80 : (short)0;
        int six = ATS ? 0xDDDD : (lane & 1) ? 0xEEEE : 0x4444;
        PIN(six);
        lds_cptr p = (lds_cptr)lds + KV0 + lane * 16 + kh * 16384;
        f32x16 oX = f32x16{}, oY = f32x16{}, sX, sY; f32x4 lX = f32x4{}, lY = f32x4{};
        bf16x8 k0, k1, v0, v1 = bf16x8{};
        sY = f32x16{};
        PIN(oX); PIN(oY); PIN(lX); PIN(lY); PIN(v1); PIN(sY);
        asm volatile("s_waitcnt vmcnt(0)" : "+v"(qa), "+v"(qb), "+v"(qc), "+v"(qd) :: "memory");
        const bf16x8 x0 = __builtin_bit_cast(bf16x8, qa), x1 = __builtin_bit_cast(bf16x8, qb);
        const bf16x8 y0 = __builtin_bit_cast(bf16x8, qc), y1 = __builtin_bit_cast(bf16x8, qd);
        cnt[w] = 1;
        cnt_wait(cnt + kh * 4 + (lane & 3), 1);
        DIAG_R(0);
        k0 = ldsfrag(p); k1 = ldsfrag(p + 1024); v0 = ldsfrag(p + 2048);
        MFMA32_M4(sX, k0, x0); sX = MFMA32(k1, x1, sX);
#define AORD 1
#define AE(c, i) c[i] = __builtin_amdgcn_exp2f(c[i])
#define AC(c, i) pp[i] = pk2(c[2 * (i)], c[2 * (i) + 1])
#define AEA(c, i) do { float t_ = c[i]; asm volatile("v_exp_f32 %0, %0" : "+v"(t_)); c[i] = t_; } while (0)
#define AEN(c, a, b) do { _Pragma("unroll") for (int i_ = (a); i_ < (b); ++i_) AEA(c, i_); SBAR(); } while (0)
#define APK(c, i) do { f32x2 a_ = {c[2 * (i)], c[2 * (i) + 1]}, b_ = {c[2 * (i) + 8], c[2 * (i) + 9]};      \
        asm volatile("v_pk_add_f32 %0, %0, %1" : "+v"(a_) : "v"(b_)); c[2 * (i)] = a_[0]; c[2 * (i) + 1] = a_[1]; } while (0)
        asm volatile("s_nop 7\n\ts_nop 3" : "+v"(sX));
#define AS0 3
#define AS1 5
#define AS2 8
#define AS3 11
#define AS4 16
#define SMF32A(pre, d, a, b) asm volatile(pre "v_smfmac_f32_32x32x32_bf16 %0, %1, %2, %3" : "+v"(d) : "v"(a), "v"(b), "v"(six))
#define SMF16A(d, b) asm volatile("v_smfmac_f32_16x16x64_bf16 %0, %1, %2, %3" : "+v"(d) : "v"(ones), "v"(b), "v"(six))
#define MFMA32A(d, a, b) asm volatile("v_mfma_f32_32x32x16_bf16 %0, %1, %2, %0" : "+v"(d) : "v"(a), "v"(b))
        static_assert(AS0 >= 1 && AS2 <= 10 && AS3 >= 10 && AS3 <= AS4 && AS4 <= 16, "wait states");
#define ASN "s_nop 0\n\t"
        static_assert(AS2 <= 8, "wait states");
#define AT_STEP(cur, nxt, op_, lp_, oc_, lc_, qa_, qb_, L1, L2, L3, L4) do {               \
        SBAR(); SMF32A(ASN, op_, v1, hif(nxt)); SBAR(); \
        L1; AEN(cur, 0, AS0);                                                              \
        SMF16A(lp_, hif(nxt)); SBAR();                                                     \
        AEN(cur, AS0, AS1);                                                                \
        MFMA32_M4(nxt, k0, qa_); SBAR();                                                   \
        L2; AEN(cur, AS1, AS2);                                                            \
        MFMA32A(nxt, k1, qb_); SBAR();                                                     \
        L3; AEN(cur, AS2, AS3);                                                            \
        SMF32A("", oc_, v0, lof(cur)); SBAR();                                             \
        L4; AEN(cur, AS3, AS4);                                                            \
        SMF16A(lc_, lof(cur)); SBAR();                                                     \
        AEN(cur, AS4, 16);                                                                 \
    } while (0)
        const unsigned cme = lds0 + AT_CNT + w * 4;
#pragma unroll 1
        for (int c = 0; c < 8; ++c) {
            lds_cptr pn;
            unsigned cv, t0_, t1_;
#pragma unroll
            for (int t = 0; t < 4; ++t) {
                if (t == 3) {
                    SBAR();
                    asm volatile("s_waitcnt lgkmcnt(1)\n.Lac%=:\n\tv_cmp_gt_u32 vcc, %2, %0\n\ts_cbranch_vccz .Lad%=\n\tv_mov_b32 %0, %1\n\tds_read_b32 %0, %0\n\ts_waitcnt lgkmcnt(0)\n\ts_branch .Lac%=\n.Lad%=:"
                                 : "+v"(cv) : "s"(lds0 + AT_CNT + 48), "s"(c < 7 ? c + 2 : 0) : "vcc", "memory");
                    asm volatile("v_add_u32 %0, %1, %2" : "=v"(pn) : "s"((c & 3) == 3 ? -3 * AT_SLOT : AT_SLOT), "v"(p));
                }
                const lds_cptr pc = p + t * 4096, px = t < 3 ? p + (t + 1) * 4096 : pn;
#define AT_PRIO(n) asm volatile("s_cmp_eq_u32 %0, 0\n\ts_cbranch_scc1 .Lap%=\n\ts_setprio " #n "\n.Lap%=:" :: "s"(kh) : "scc")
                if (ABAL && t == 0) AT_PRIO(1);
                if (ABAL && ABAL == 2 * t) AT_PRIO(0);
                AT_STEP(sX, sY, oY, lY, oX, lX, y0, y1, v1 = ldsfrag(pc + 3072), k0 = ldsfrag(px), k1 = ldsfrag(px + 1024),
                        if (t == 0) asm volatile("v_mov_b32 %0, %2\n\tv_mov_b32 %1, %3\n\tds_write_b32 %0, %1" : "=&v"(t0_), "=&v"(t1_) : "s"(cme), "s"(c + 2) : "memory"));
                if (ABAL == 2 * t + 1) AT_PRIO(0);
                AT_STEP(sY, sX, oX, lX, oY, lY, x0, x1, , , if (t == 2) asm volatile("v_mov_b32 %0, %1\n\tds_read_b32 %0, %0" : "=&v"(cv) : "s"(lds0 + AT_CNT + 48) : "memory"), v0 = ldsfrag(px + 2048));
            }
            p = pn;
        }
        SBAR();
        SMF32A("s_nop 1\n\t", oY, v1, hif(sY)); SMF16A(lY, ATS == 2 ? lof(sY) : hif(sY));
        asm volatile("s_nop 7\n\ts_nop 7" : "+v"(oX), "+v"(oY), "+v"(lX), "+v"(lY));
        DIAG_R(1);
        const int ln = lane_id();
        cnt_wait(cnt + (ln & 7), 9);
        LAS f32x4* mine = (LAS f32x4*)(L + KV0 + 2 * AT_SLOT + w * 4096) + ln;
#pragma unroll
        for (int g = 0; g < 4; ++g) mine[g * 64] = f32x4{oX[4 * g], oX[4 * g + 1], oX[4 * g + 2], oX[4 * g + 3]};
        ((LAS float*)(L + AT_LX))[w * 64 + ln] = lX[0] + lX[1];
        asm volatile("" ::: "memory");
        cnt[w] = 10;
        const LAS f32x4* other = (const LAS f32x4*)(L + KV0 + 2 * AT_SLOT + (w ^ 4) * 4096) + ln;
        const char* op = Of + (size_t)(qt * 32 + head) * 2048;
#define O_ST(v0_, v1_) asm volatile("global_store_dwordx4 %0, %1, %3 sc1\n\tglobal_store_dwordx4 %0, %2, %3 offset:1024 sc1\n\ts_nop 1" :: "v"((ln & 15) * 64 + (ln & 48)), "v"(v0_), "v"(v1_), "s"(op) : "memory")
        bool bad;
#pragma unroll 1
        for (int pass = 0;; ++pass) {
            if (pass) while (__any(cnt[w ^ 4] < 10u)) __builtin_amdgcn_s_sleep(ASL);
            const bool ready = !__any(cnt[w ^ 4] < 10u);
            asm volatile("" ::: "memory");
            const float l = (lY[0] + lY[1]) + ((const LAS float*)(L + AT_LX))[(w ^ 4) * 64 + ln];
            f32x2 o[8];
#pragma unroll
            for (int g = 0; g < 4; ++g) {
                const f32x4 v = other[g * 64];
                o[2 * g] = f32x2{oY[4 * g], oY[4 * g + 1]} + f32x2{v[0], v[1]}; o[2 * g + 1] = f32x2{oY[4 * g + 2], oY[4 * g + 3]} + f32x2{v[2], v[3]};
            }
            const float rl = __builtin_amdgcn_rcpf(l);
            u32x4 v[2];
#pragma unroll
            for (int j = 0; j < 8; ++j) { const f32x2 t = o[j] * rl; v[j >> 2][j & 3] = pk2(t[0], t[1]); }
            const f32x2 chk = ((o[0] + o[1]) + (o[2] + o[3])) + ((o[4] + o[5]) + (o[6] + o[7]));
            bad = __any(!((l >= 8.6736174e-19f) & (l <= 1.2676506e30f) & (fabsf(chk[0] + chk[1]) <= 3.0e38f)));
            v[0] = tr4(v[0]); v[1] = tr4(v[1]);
            asm volatile("" : "+v"(v[0]), "+v"(v[1]));
            if (ready) { O_ST(v[0], v[1]); break; }
        }
        cnt[w] = 11;
        if (__builtin_expect(bad, 0)) {
            const bf16x8* kp = (const bf16x8*)kvbase + ln;
            float mx = -INFINITY;
#pragma unroll 1
            for (int t = 0; t < 64; ++t) {
                f32x16 s = MFMA32(kp[t * 256], y0, f32x16{}); s = MFMA32(kp[t * 256 + 64], y1, s);
#pragma unroll
                for (int i = 0; i < 16; ++i) mx = fmaxf(mx, s[i]);
            }
            { auto s_ = __builtin_amdgcn_permlane32_swap(__float_as_uint(mx), __float_as_uint(mx), false, false); mx = fmaxf(__uint_as_float(s_[0]), __uint_as_float(s_[1])); }
            oY = f32x16{}; lY = f32x4{};
#pragma unroll 1
            for (int t = 0; t < 64; ++t) {
                f32x16 s = MFMA32(kp[t * 256], y0, f32x16{}); s = MFMA32(kp[t * 256 + 64], y1, s);
#pragma unroll
                for (int i = 0; i < 16; ++i) s[i] = __builtin_amdgcn_exp2f(s[i] - mx);
                oY = SMF32(kp[t * 256 + 128], lof(s), oY); oY = SMF32(kp[t * 256 + 192], hif(s), oY);
                lY = SMF(ones, lof(s), lY, six); lY = SMF(ones, hif(s), lY, six);
            }
            const float rl = 1.f / (lY[0] + lY[1]);
            u32x4 v[2];
#pragma unroll
            for (int j = 0; j < 8; ++j) v[j >> 2][j & 3] = pk2(oY[2 * j] * rl, oY[2 * j + 1] * rl);
            O_ST(tr4(v[0]), tr4(v[1]));
        }
        DIAG_R(2);
    }
    const int ln = lane_id();
    const int mt = w & 1, nt = w >> 1, blk = ln >> 4, hh = ln >> 5;
    lds_cptr bp = (lds_cptr)lds + KV0 + mt * 8192 + (nt >> 1) * 1024 + hh * 512 + ((ln & 15) >> 2) * 128 + ((nt ^ (ln >> 3)) & 1) * 64 + (blk & 1) * 32 + (ln & 3) * 8;
    lds_cptr bq = bp + 65536;
    asm volatile("" : "+v"(bq), "+v"(bp));
    bf16x8 fb[16][2];
#define B_RD(s, q) do { const lds_cptr bs_ = (((s) & 4) ? bq : bp) + (bslot(s) & 65535) + (q) * 4096; fb[s][q] = cat4(trread(bs_), trread(bs_ + 2048)); } while (0)
    {
        unsigned na;
        asm volatile("s_waitcnt vmcnt(0)\n\ts_mov_b64 exec, 1\n\tds_add_rtn_u32 %0, %1, %2\n\ts_waitcnt lgkmcnt(0)\n\tv_cmp_eq_u32 vcc, 7, %0\n\ts_mov_b64 exec, vcc\n\tglobal_store_dword %3, %4, %5 sc1\n\ts_mov_b64 exec, -1"
                     : "=&v"(na) : "v"(lds0 + AT_CNT + 52), "v"(1u), "v"(0u), "v"(TAG), "s"(gm + G_FL + ((b >> 5) * 32 + head) * 128) : "vcc", "memory");
        DIAG_R(3);
    }
    cnt_sleep<2>(cnt + 20 + (ln & 3), 1);
#pragma unroll
    for (int s = 0; s < 4; ++s) { B_RD(s, 0); B_RD(s, 1); }
    asm volatile("" ::: "memory");
    cnt[w] = 12;
    __builtin_amdgcn_s_setprio(0);
    if (PRE > 4) {
        cnt_sleep<2>(cnt + 20 + (ln & 3), 2);
#pragma unroll
        for (int s = 4; s < PRE; ++s) { B_RD(s, 0); B_RD(s, 1); }
        asm volatile("" ::: "memory");
        cnt[w] = 13;
    }
    u32x4 xi[2]; float ref; unsigned xoff;
    lds_cptr ap = (lds_cptr)lds + XA + mt * 6144 + ln * 16, ax = (lds_cptr)lds + XA + 4096 - mt * 2048 + ln * 16;
    asm volatile("" : "+v"(ax), "+v"(ap));
    f32x16 acc = f32x16{}, acx = f32x16{};
    float d[16];
    bf16x8 fa[2][4];
    f32x4 va[4], vb[4];
    lds_cptr vp;
    DIAG_R(0);
#pragma unroll
    for (int s = 0; s < 17; ++s) {
        if (s < 16) { if (APL > 15 && s > 0) BAR(); else if (s > PRE) asm volatile("s_waitcnt lgkmcnt(4)\n\ts_barrier" ::: "memory"); else LDS_BAR(); }
        if (s == 0) DIAG_R(1);
#pragma unroll
        for (int ks = 0; ks < 4; ++ks) {
            const int q = s < 16 ? ks : (ks >> 1) + (ks & 1) * 2;
            SBAR();
            if (s > 0) { if (q & 1) acc = MFMA32(fb[s - 1][q >> 1], fa[(s - 1) & 1][q], acc); else acx = MFMA32(fb[s - 1][q >> 1], fa[(s - 1) & 1][q], acx); }
            SBAR();
            if (s < 16) {
                if (ks < 2) { fa[s & 1][2 * ks] = ldsfrag(ax + (s % 3) * 8192 + ks * 1024); fa[s & 1][2 * ks + 1] = ldsfrag(ap + (s % 3) * 8192 + ks * 1024); }
                else if (s >= PRE) B_RD(s, ks - 2);
            } else if (ks < 2) {
#pragma unroll
                for (int g = 2 * ks; g < 2 * ks + 2; ++g) { va[g] = *(const LAS f32x4*)(vp + g * 32); vb[g] = *(const LAS f32x4*)(vp + 2048 + g * 32); }
            }
            if (s == XLD && ks == 3) {
                const int l2 = lane_id(), row = rp * 64 + mt * 32 + (l2 & 31);
                xoff = (unsigned)((row * D_ + cp * 128 + nt * 32 + 8 * (l2 >> 5)) * 2);
                vp = (lds_cptr)lds + OV + (nt * 32 + 4 * (l2 >> 5)) * 4;
                asm volatile("global_load_dwordx4 %0, %3, %5\n\tglobal_load_dwordx4 %1, %3, %5 offset:32\n\tglobal_load_dword %2, %4, %6"
                             : "=&v"(xi[0]), "=&v"(xi[1]), "=&v"(ref) : "v"(xoff), "v"(row * 4), "s"(x), "s"(mean1) : "memory");
            }
            if (s == 14) {
                if (ks == 0) asm volatile("s_waitcnt vmcnt(0)" : "+v"(xi[0]), "+v"(xi[1]), "+v"(ref) :: "memory");
                const int j = ks >> 1, e = ks & 1;
                const auto r = __builtin_amdgcn_permlane32_swap(xi[j][e], xi[j][2 + e], false, false);
                d[8 * j + 2 * e] = bflo(r[0]) - ref; d[8 * j + 2 * e + 1] = bfhi(r[0]) - ref;
                d[8 * j + 4 + 2 * e] = bflo(r[1]) - ref; d[8 * j + 5 + 2 * e] = bfhi(r[1]) - ref;
                asm volatile("" : "+v"(d[8 * j + 2 * e]), "+v"(d[8 * j + 2 * e + 1]), "+v"(d[8 * j + 4 + 2 * e]), "+v"(d[8 * j + 5 + 2 * e]));
            }
        }
        SBAR();
    }
    {
        LAS f32x4* p = (LAS f32x4*)(L + KV0 + w * 4096) + ln;
#pragma unroll
        for (int g = 0; g < 4; ++g) p[g * 64] = f32x4{acx[4 * g], acx[4 * g + 1], acx[4 * g + 2], acx[4 * g + 3]};
        LDS_BAR();
        const LAS f32x4* o = (const LAS f32x4*)(L + KV0 + (w ^ 1) * 4096) + ln;
#pragma unroll
        for (int g = 0; g < 4; ++g) { const f32x4 v = o[g * 64]; acc[4 * g] += v[0]; acc[4 * g + 1] += v[1]; acc[4 * g + 2] += v[2]; acc[4 * g + 3] += v[3]; }
    }
    DIAG_R(2);
    {
        float s1a = 0.f, s1b = 0.f, s2a = 0.f, s2b = 0.f, sda = 0.f, sdb = 0.f;
#pragma unroll
        for (int g = 0; g < 4; ++g)
#pragma unroll
            for (int e = 0; e < 4; e += 2) {
                const float v0 = __builtin_fmaf(va[g][e], acc[4 * g + e], d[4 * g + e]), v1 = __builtin_fmaf(va[g][e + 1], acc[4 * g + e + 1], d[4 * g + e + 1]);
                d[4 * g + e] = v0; d[4 * g + e + 1] = v1;
                s1a += v0; s1b += v1; s2a = __builtin_fmaf(v0, v0, s2a); s2b = __builtin_fmaf(v1, v1, s2b);
                sda = __builtin_fmaf(v0, vb[g][e], sda); sdb = __builtin_fmaf(v1, vb[g][e + 1], sdb);
            }
        const float q12 = sw32sum(s1a + s1b, s2a + s2b), sd = sda + sdb, q3 = sw32sum(sd, sd);
        const int o = OST + nt * 256 + (mt * 32 + (ln & 31)) * 4;
        *(LAS float*)(L + o + hh * 1024) = q12;
        *(LAS float*)(L + o + 2048) = q3;
    }
    LDS_BAR();
    if (w < 2) {
        const LAS float* p = (const LAS float*)(L + OST + w * 2048) + (w ? (ln & 31) * 2 : ln);
        const int o = w ? 1 : 256;
        const float s0 = (p[0] + p[64]) + (p[128] + p[192]), s1 = (p[o] + p[o + 64]) + (p[o + 128] + p[o + 192]);
        const u32x4 v = {__float_as_uint(s0), TAG, __float_as_uint(s1), TAG};
        if (w * 64 + ln < 96) GST4C((unsigned)(cp * 1536 + w * 1024 + ln * 16), v, gran + (size_t)rp * 12288);
        if (PKW == 1) cnt[24 + w] = 1;
    }
    DIAG_R(3);
    if (PKW == 1) cnt_sleep<1>(cnt + 24 + (ln & 3), 1);
    f32x4 vc[4];
#pragma unroll
    for (int g = 0; g < 4; ++g) { va[g] = *(const LAS f32x4*)(vp + 512 + g * 32); vb[g] = *(const LAS f32x4*)(vp + 1024 + g * 32); vc[g] = *(const LAS f32x4*)(vp + 1536 + g * 32); }
    int brow; f32x2 fs;
    char* g2 = gran + G_H2;
    {
        unsigned f;
        asm volatile(".Loe%=:\n\tds_read_b32 %0, %2\n\tds_read_b64 %1, %3\n\ts_waitcnt lgkmcnt(0)\n\tv_cmp_eq_u32 vcc, 0, %0\n\ts_cbranch_vccz .Lof%=\n\ts_sleep %4\n\ts_branch .Loe%=\n.Lof%=:"
                     : "=&v"(f), "=&v"(fs) : "v"(lds0 + OGS + 72), "v"(lds0 + OFS + (mt * 32 + (ln & 31)) * 8), "n"(OSPIN) : "vcc", "memory");
        brow = __builtin_amdgcn_readfirstlane(f) - 64;
    }
    DIAG_R(5);
    float h[16];
#pragma unroll
    for (int i = 0; i < 16; ++i) h[i] = __builtin_fmaf(__builtin_fmaf(d[i], fs[0], fs[1]), vb[i >> 2][i & 3], va[i >> 2][i & 3]);
    const bool mine = mt * 32 + (ln & 31) == brow;
    if ((brow >> 5) == mt) {
        LAS u32x2* sp = (LAS u32x2*)(L + PK_S + w * 64);
        if (mine) {
#pragma unroll
            for (int g = 0; g < 4; ++g) sp[2 * g + hh] = u32x2{pk2(h[4 * g], h[4 * g + 1]), pk2(h[4 * g + 2], h[4 * g + 3])};
        }
        asm volatile("" ::: "memory");
        const u32x2 t = *(volatile LAS u32x2*)(sp + (ln & 7));
        const u32x4 v = {t[0], TAG, t[1], TAG};
        if (ln < 8) GST4C((unsigned)(rp * 4096 + (cp * 128 + nt * 32) * 4 + ln * 16), v, g2);
    }
    DIAG_R(6);
#pragma unroll
    for (int i = 0; i < 16; ++i) h[i] = __builtin_fmaf(vc[i >> 2][i & 3], h[i], d[i]) + ref;
    float tb;
    {
        LAS f32x4* sp = (LAS f32x4*)(L + PK_S + 1024 + w * 128);
        if (mine) {
#pragma unroll
            for (int g = 0; g < 4; ++g) sp[2 * g + hh] = f32x4{h[4 * g], h[4 * g + 1], h[4 * g + 2], h[4 * g + 3]};
        }
        asm volatile("" ::: "memory");
        tb = *((volatile LAS float*)sp + (ln & 31));
    }
    u32x4 o[2];
#define O_OUT(j) asm volatile("global_store_dwordx4 %0, %1, %2 offset:%3 " OSP "\n\ts_nop 1" :: "v"(xoff), "v"(o[j]), "s"(out), "n"((j) * 32) : "memory")
#pragma unroll
    for (int j = 0; j < 2; ++j) {
#pragma unroll
        for (int e = 0; e < 2; ++e) {
            const auto r = __builtin_amdgcn_permlane32_swap(pk2(h[8 * j + 2 * e], h[8 * j + 2 * e + 1]), pk2(h[8 * j + 4 + 2 * e], h[8 * j + 5 + 2 * e]), false, false);
            o[j][e] = r[0]; o[j][2 + e] = r[1];
        }
        if (!OSD && !mine) O_OUT(j);
    }
    char* gy = gran + G_Y;
    if ((brow >> 5) == mt) {
        const u32x2 v = {__float_as_uint(tb), TAG + 2047 - rp * 64 - brow};
        if (ln < 32) GST2C(ln * 16, v, gy + (224 + rp) * 8192 + cp * 1024 + (nt & 1) * 512 + (nt >> 1) * 8);
    }
    bool m0, m1;
    {
        const lds_ptr sb = L + KV0 + 32768 + w * 2048;
        const int r = ln >> 2, sw = ((ln & 3) ^ ((r >> 1) & 3)) * 16;
#pragma unroll
        for (int j = 0; j < 2; ++j) *(LAS u32x4*)(sb + (ln & 31) * 64 + (((j * 2 + hh) ^ (ln >> 1)) & 3) * 16) = o[j];
        asm volatile("" ::: "memory");
#pragma unroll
        for (int j = 0; j < 2; ++j) o[j] = *(volatile LAS u32x4*)(sb + j * 1024 + r * 64 + sw);
        xoff = (unsigned)(((rp * 64 + mt * 32 + r) * D_ + cp * 128 + nt * 32) * 2 + (ln & 3) * 16);
        m0 = mt * 32 + r == brow; m1 = mt * 32 + 16 + r == brow;
    }
#define O_OU2(j) asm volatile("global_store_dwordx4 %0, %1, %2 " OSP "\n\ts_nop 1" :: "v"(xoff), "v"(o[j]), "s"(out + (j) * 16 * D_) : "memory")
    if (OSD > 1) __builtin_amdgcn_s_sleep(OSD);
    if (!m0) O_OU2(0);
    if (!m1) O_OU2(1);
    const LAS float* gbp = (const LAS float*)(L + OV + 2560) + (ln & 1);
    const f32x2 gb2 = {gbp[0], gbp[2]};
    unsigned id, wi;
    asm volatile(".Log%=:\n\tds_read_b32 %1, %2 offset:12\n\tds_read_b32 %0, %2\n\ts_waitcnt lgkmcnt(0)\n\tv_cmp_eq_u32 vcc, 0, %1\n\ts_cbranch_vccz .Loh%=\n\ts_sleep %3\n\ts_branch .Log%=\n.Loh%=:"
                 : "=&v"(id), "=&v"(wi) : "v"(lds0 + OGS + 64), "n"(OSPIN) : "vcc", "memory");
    wi = ~id & (S_ - 1);
    DIAG_R(7);
    if ((int)(wi >> 6) != rp) { if (m0) O_OU2(0); if (m1) O_OU2(1); }
    DIAG_R(4);
    {
        const int r = w * 32 + (ln >> 1);
        u32x4 v;
#pragma unroll 1
        for (int spin = 0; spin < WPUB * SPIN_MAX && __any(cnt[16 + (ln & 3)] != id); ++spin) __builtin_amdgcn_s_sleep(OSPIN);
#pragma unroll 1
        for (int spin = 0; spin < SPIN_MAX; ++spin) {
            asm volatile("global_load_dwordx4 %0, %1, %2 sc1\n\ts_waitcnt vmcnt(0)" : "=v"(v) : "v"((unsigned)((r < 224 ? r : 224 + (wi >> 6)) * 8192 + (ln & 1) * 16)), "s"(gy + xcd * 1024 + idx * 32) : "memory");
            if (__all(v[1] == id && v[3] == id)) break;
            if (OSL4) __builtin_amdgcn_s_sleep(OSL4);
        }
        float a = r > 224 ? 0.f : __uint_as_float(v[0]), c = r > 224 ? 0.f : __uint_as_float(v[2]);
        if (w < 7) { a *= gb2[0]; c *= gb2[1]; }
        a = sw32sum(a, c);
        a = sw16sum(a, a); a += dpp<XOR2>(a); a += dpp<ROR4>(a); a += dpp<ROR8>(a);
        if ((ln & 30) == 0) *(LAS float*)(L + OST + w * 16 + (ln & 1) * 8 + hh * 4) = a;
        LDS_BAR();
        if (w == 0 && ln < 8) {
            f32x4 q = *(const LAS f32x4*)(L + OST + ln * 16);
#pragma unroll
            for (int e = 0; e < 4; ++e) q[e] = oct_sum(q[e]);
            if (ln < 2) asm volatile("global_store_dword %0, %1, %2 " FSP :: "v"((wi * D_ + c0 + ln * 64) * 2), "v"(ln ? pk2(q[1], q[3]) : pk2(q[0], q[2])), "s"(out) : "memory");
        }
    }
}

extern "C" void kernel_launch(void* const* d_in, const int* in_sizes, int n_in, void* d_out, int out_size, void* d_ws, size_t ws_size,
                              hipStream_t stream) {
    const u16* x = (const u16*)d_in[0];
    const u16* wqkv = (const u16*)d_in[1];
    const u16* wo = (const u16*)d_in[2];
    const u16* wr = (const u16*)d_in[4];
    const u16* wg = (const u16*)d_in[5];
    const u16* wu = (const u16*)d_in[6];
    const u16* wd = (const u16*)d_in[7];
    const u16* c = (const u16*)d_in[8];
    const u16* wmod = (const u16*)d_in[9];
    char* ws = (char*)d_ws;
    k_mod<<<256, 512, 0, stream>>>(ws, x, c, wmod, wqkv DIAG_PASS);
    k_qkv<<<256, 768, 0, stream>>>(ws, wqkv DIAG_PASS);
    k_ao<<<256, 1024, 0, stream>>>(ws, wg, wu, wd, wmod, wr, wo, x, (u16*)d_out DIAG_PASS);
}
```
